```python
import torch

def _softmax(x, axis=-1):
    return torch.softmax(x, dim=axis)
def _gelu(x):
    return torch.nn.functional.gelu(x, approximate="tanh")
def _silu(x):
    return torch.nn.functional.silu(x)
def _sigmoid(x):
    return torch.sigmoid(x)
def _layernorm(x, axis=-1, eps=1e-5):
    m = x.mean(dim=axis, keepdim=True)
    v = x.var(dim=axis, keepdim=True, unbiased=False)
    return (x - m) / torch.sqrt(v + eps)
def _scatter_add(tgt, idx, src, axis=0):
    return tgt.clone().index_add_(axis, idx, src)
def _segment_sum(src, seg, n):
    out = torch.zeros((n,) + src.shape[1:], dtype=src.dtype)
    return out.index_add_(0, seg, src)
def _pad(x, pw):
    flat = tuple(v for pair in reversed(pw) for v in pair)
    return torch.nn.functional.pad(x, flat)

kernel_name = 'parallel_sum__linear_kernel_hedgehog__glu_gated_glu__prompt'

def setup_inputs():

    _w = (lambda a: (lambda t: t.to(torch.bfloat16) if t.is_floating_point() else t)(torch.as_tensor(a)))
    def _A_setup():
        B, S, D, H = 1, 2048, 640, 32
        Dh = D // H
        return {
            "x": _w((lambda *s: torch.randn(*s, dtype=torch.bfloat16) * 0.05)(B, S, D)),
            "wqkv": _w((lambda *s: torch.randn(*s, dtype=torch.bfloat16) * 0.4)(D, 3 * D)),
            "w_phi": _w((lambda *s: torch.randn(*s, dtype=torch.bfloat16) * 0.4)(H, Dh, Dh) * (Dh ** -0.5)),
            "wo": _w((lambda *s: torch.randn(*s, dtype=torch.bfloat16) * 0.1)(D, D)),
        }
    _A_in = _A_setup()

    def _B_setup():
        B, S, D, F = 1, 2048, 640, 1536
        return {
            "x": _w((lambda *s: torch.randn(*s, dtype=torch.bfloat16) * 0.05)(B, S, D)),
            "w_gate": _w((lambda *s: torch.randn(*s, dtype=torch.bfloat16) * 0.05)(D, F)),
            "w_up": _w((lambda *s: torch.randn(*s, dtype=torch.bfloat16) * 0.05)(D, F)),
            "w_down": _w((lambda *s: torch.randn(*s, dtype=torch.bfloat16) * 0.05)(F, D)),
        }
    _B_in = _B_setup()

    return {
        "A_x": _A_in["x"],
        "A_wqkv": _A_in["wqkv"],
        "A_w_phi": _A_in["w_phi"],
        "A_wo": _A_in["wo"],
        "B_x": _B_in["x"],
        "B_w_gate": _B_in["w_gate"],
        "B_w_up": _B_in["w_up"],
        "B_w_down": _B_in["w_down"],
    }

def reference(A_x, A_wqkv, A_w_phi, A_wo, B_x, B_w_gate, B_w_up, B_w_down):
    def _A_ref(x, wqkv, w_phi, wo):
        B, S, D = x.shape
        H = 32; Dh = D // H
        qkv = (lambda x, *d: x.permute(*d))((x @ wqkv).reshape(B, S, 3, H, Dh), 2, 0, 3, 1, 4)
        q, k, v = qkv[0], qkv[1], qkv[2]
        pq = torch.einsum("bhsd,hdf->bhsf", q, w_phi)
        pk = torch.einsum("bhsd,hdf->bhsf", k, w_phi)
        phi_q = torch.concatenate([_softmax(pq, axis=-1), _softmax(-pq, axis=-1)], axis=-1)
        phi_k = torch.concatenate([_softmax(pk, axis=-1), _softmax(-pk, axis=-1)], axis=-1)
        kv = torch.einsum("bhsf,bhsd->bhfd", phi_k, v)
        z  = torch.einsum("bhsf->bhf", phi_k)
        num = torch.einsum("bhqf,bhfd->bhqd", phi_q, kv)
        den = torch.einsum("bhqf,bhf->bhq", phi_q, z)[..., None] + 1e-6
        o = num / den
        return (lambda x, *d: x.permute(*d))(o, 0, 2, 1, 3).reshape(B, S, D) @ wo

    def _B_ref(x, w_gate, w_up, w_down):
        g = torch.einsum("bsd,df->bsf", x, w_gate)
        u = torch.einsum("bsd,df->bsf", x, w_up)
        h = _sigmoid(g) * u
        return torch.einsum("bsf,fd->bsd", h, w_down)

    x = A_x
    xn = _layernorm(x, axis=-1)
    return x + _A_ref(xn, A_wqkv, A_w_phi, A_wo) + _B_ref(xn, B_w_gate, B_w_up, B_w_down)
```

```cpp
#include <hip/hip_runtime.h>
#include <hip/hip_bf16.h>
typedef unsigned short u16;
__device__ __forceinline__ float b2f(u16 v){ return __uint_as_float(((unsigned)v)<<16); }
__device__ __forceinline__ u16 f2b(float f){ __hip_bfloat16 b = __float2bfloat16(f); return *(u16*)&b; }
constexpr int S=2048, D=640, H=32, DH=20, F=1536;

__global__ void ln_k(const u16* x, u16* xn){
  int row=blockIdx.x; int t=threadIdx.x; __shared__ float red[256];
  float s=0; for(int i=t;i<D;i+=256) s+=b2f(x[row*D+i]);
  red[t]=s; __syncthreads(); for(int o=128;o>0;o>>=1){ if(t<o) red[t]+=red[t+o]; __syncthreads(); }
  float m=red[0]/D; __syncthreads();
  float v=0; for(int i=t;i<D;i+=256){ float d=b2f(x[row*D+i])-m; v+=d*d; }
  red[t]=v; __syncthreads(); for(int o=128;o>0;o>>=1){ if(t<o) red[t]+=red[t+o]; __syncthreads(); }
  float r=rsqrtf(red[0]/D+1e-5f);
  for(int i=t;i<D;i+=256) xn[row*D+i]=f2b((b2f(x[row*D+i])-m)*r);
}
__global__ void gemm_k(const u16* A,int lda,const u16* B,int ldb,float* C,int ldc,int K,int acc){
  int n=blockIdx.x*64+threadIdx.x%64; int m=blockIdx.y*4+threadIdx.x/64;
  float s=0; for(int k=0;k<K;k++) s+=b2f(A[m*lda+k])*b2f(B[k*ldb+n]);
  if(acc) C[m*ldc+n]+=s; else C[m*ldc+n]=s;
}
__global__ void glu_k(const float* g,const float* u,u16* h){
  int i=blockIdx.x*256+threadIdx.x; float gv=g[i],uv=u[i]; h[i]=f2b(uv/(1.f+__expf(-gv)));
}
__global__ void phi_k(const float* qkv,const u16* wphi,float* phiq,float* phik){
  int s=blockIdx.x; int t=threadIdx.x; if(t>=64) return; int h=t%32; int which=t/32;
  float v[DH]; for(int d=0;d<DH;d++) v[d]=b2f(f2b(qkv[s*1920+which*640+h*DH+d]));
  float p[DH]; float mx=-1e30f,mn=1e30f;
  for(int f=0;f<DH;f++){ float a=0; for(int d=0;d<DH;d++) a+=v[d]*b2f(wphi[h*400+d*20+f]); p[f]=a; mx=fmaxf(mx,a); mn=fminf(mn,a);}
  float sp=0,sn=0; for(int f=0;f<DH;f++){ sp+=__expf(p[f]-mx); sn+=__expf(mn-p[f]); }
  float* o=(which?phik:phiq)+(s*H+h)*40;
  for(int f=0;f<DH;f++){ o[f]=__expf(p[f]-mx)/sp; o[20+f]=__expf(mn-p[f])/sn; }
}
__global__ void kv_k(const float* qkv,const float* phik,float* kv){
  int h=blockIdx.x; int t=threadIdx.x; if(t>=840) return; int f=t/21,d=t%21; float a=0;
  for(int s=0;s<S;s++){ float pv=b2f(f2b(phik[(s*H+h)*40+f])); float vv= d<20? b2f(f2b(qkv[s*1920+1280+h*DH+d])):1.f; a+=pv*vv; }
  kv[(h*40+f)*21+d]=a;
}
__global__ void o_k(const float* phiq,const float* kv,u16* o){
  int s=blockIdx.x; int t=threadIdx.x; if(t>=640) return; int h=t/20,d=t%20; float num=0,den=0;
  for(int f=0;f<40;f++){ float p=b2f(f2b(phiq[(s*H+h)*40+f])); num+=p*b2f(f2b(kv[(h*40+f)*21+d])); den+=p*b2f(f2b(kv[(h*40+f)*21+20])); }
  o[s*D+t]=f2b(num/(den+1e-6f));
}
__global__ void fin_k(const u16* x,const float* acc,u16* out){ int i=blockIdx.x*256+threadIdx.x; out[i]=f2b(b2f(x[i])+acc[i]); }

extern "C" void kernel_launch(void* const* d_in, const int* in_sizes, int n_in, void* d_out, int out_size, void* d_ws, size_t ws_size, hipStream_t stream) {
  const u16* x=(const u16*)d_in[0]; const u16* wqkv=(const u16*)d_in[1]; const u16* wphi=(const u16*)d_in[2]; const u16* wo=(const u16*)d_in[3];
  const u16* wg=(const u16*)d_in[5]; const u16* wu=(const u16*)d_in[6]; const u16* wd=(const u16*)d_in[7];
  char* ws=(char*)d_ws; size_t off=0; auto alloc=[&](size_t b){ void* p=ws+off; off+=(b+255)/256*256; return p; };
  u16* xn=(u16*)alloc(S*D*2); float* qkv=(float*)alloc((size_t)S*1920*4); float* g=(float*)alloc((size_t)S*F*4); float* u=(float*)alloc((size_t)S*F*4);
  u16* h=(u16*)alloc(S*F*2); float* phiq=(float*)alloc((size_t)S*H*40*4); float* phik=(float*)alloc((size_t)S*H*40*4); float* kv=(float*)alloc(H*40*21*4);
  u16* o=(u16*)alloc(S*D*2); float* acc=(float*)alloc(S*D*4);
  ln_k<<<S,256,0,stream>>>(x,xn);
  gemm_k<<<dim3(1920/64,S/4),256,0,stream>>>(xn,D,wqkv,1920,qkv,1920,D,0);
  gemm_k<<<dim3(F/64,S/4),256,0,stream>>>(xn,D,wg,F,g,F,D,0);
  gemm_k<<<dim3(F/64,S/4),256,0,stream>>>(xn,D,wu,F,u,F,D,0);
  glu_k<<<S*F/256,256,0,stream>>>(g,u,h);
  phi_k<<<S,64,0,stream>>>(qkv,wphi,phiq,phik);
  kv_k<<<H,896,0,stream>>>(qkv,phik,kv);
  o_k<<<S,640,0,stream>>>(phiq,kv,o);
  gemm_k<<<dim3(D/64,S/4),256,0,stream>>>(o,D,wo,D,acc,D,D,0);
  gemm_k<<<dim3(D/64,S/4),256,0,stream>>>(h,F,wd,D,acc,D,F,1);
  fin_k<<<S*D/256,256,0,stream>>>(x,acc,(u16*)d_out);
}
```

```cpp
#define NPROBE 1
#define STRIDE 320
#include <hip/hip_runtime.h>
#include <hip/hip_bf16.h>
typedef unsigned short u16;
__device__ __forceinline__ float b2f(u16 v){ return __uint_as_float(((unsigned)v)<<16); }
__device__ __forceinline__ u16 f2b(float f){ __hip_bfloat16 b = __float2bfloat16(f); return *(u16*)&b; }
constexpr int S=2048, D=640, H=32, DH=20, F=1536;

__global__ void ln_k(const u16* x, u16* xn){
  int row=blockIdx.x; int t=threadIdx.x; __shared__ float red[256];
  float s=0; for(int i=t;i<D;i+=256) s+=b2f(x[row*D+i]);
  red[t]=s; __syncthreads(); for(int o=128;o>0;o>>=1){ if(t<o) red[t]+=red[t+o]; __syncthreads(); }
  float m=red[0]/D; __syncthreads();
  float v=0; for(int i=t;i<D;i+=256){ float d=b2f(x[row*D+i])-m; v+=d*d; }
  red[t]=v; __syncthreads(); for(int o=128;o>0;o>>=1){ if(t<o) red[t]+=red[t+o]; __syncthreads(); }
  float r=rsqrtf(red[0]/D+1e-5f);
  for(int i=t;i<D;i+=256) xn[row*D+i]=f2b((b2f(x[row*D+i])-m)*r);
}
__global__ void gemm_k(const u16* A,int lda,const u16* B,int ldb,float* C,int ldc,int K,int acc){
  int n=blockIdx.x*64+threadIdx.x%64; int m=blockIdx.y*4+threadIdx.x/64;
  float s=0; for(int k=0;k<K;k++) s+=b2f(A[m*lda+k])*b2f(B[k*ldb+n]);
  if(acc) C[m*ldc+n]+=s; else C[m*ldc+n]=s;
}
__global__ void glu_k(const float* g,const float* u,u16* h){
  int i=blockIdx.x*256+threadIdx.x; float gv=g[i],uv=u[i]; h[i]=f2b(uv/(1.f+__expf(-gv)));
}
__global__ void phi_k(const float* qkv,const u16* wphi,float* phiq,float* phik){
  int s=blockIdx.x; int t=threadIdx.x; if(t>=64) return; int h=t%32; int which=t/32;
  float v[DH]; for(int d=0;d<DH;d++) v[d]=b2f(f2b(qkv[s*1920+which*640+h*DH+d]));
  float p[DH]; float mx=-1e30f,mn=1e30f;
  for(int f=0;f<DH;f++){ float a=0; for(int d=0;d<DH;d++) a+=v[d]*b2f(wphi[h*400+d*20+f]); p[f]=a; mx=fmaxf(mx,a); mn=fminf(mn,a);}
  float sp=0,sn=0; for(int f=0;f<DH;f++){ sp+=__expf(p[f]-mx); sn+=__expf(mn-p[f]); }
  float* o=(which?phik:phiq)+(s*H+h)*40;
  for(int f=0;f<DH;f++){ o[f]=__expf(p[f]-mx)/sp; o[20+f]=__expf(mn-p[f])/sn; }
}
__global__ void kv_k(const float* qkv,const float* phik,float* kv){
  int h=blockIdx.x; int t=threadIdx.x; if(t>=840) return; int f=t/21,d=t%21; float a=0;
  for(int s=0;s<S;s++){ float pv=b2f(f2b(phik[(s*H+h)*40+f])); float vv= d<20? b2f(f2b(qkv[s*1920+1280+h*DH+d])):1.f; a+=pv*vv; }
  kv[(h*40+f)*21+d]=a;
}
__global__ void o_k(const float* phiq,const float* kv,u16* o){
  int s=blockIdx.x; int t=threadIdx.x; if(t>=640) return; int h=t/20,d=t%20; float num=0,den=0;
  for(int f=0;f<40;f++){ float p=b2f(f2b(phiq[(s*H+h)*40+f])); num+=p*b2f(f2b(kv[(h*40+f)*21+d])); den+=p*b2f(f2b(kv[(h*40+f)*21+20])); }
  o[s*D+t]=f2b(num/(den+1e-6f));
}
__global__ void fin_k(const u16* x,const float* acc,u16* out){ int i=blockIdx.x*256+threadIdx.x; out[i]=f2b(b2f(x[i])+acc[i]); }


__global__ void probe_k(const unsigned* p, unsigned* sink, int n, int stride){
  unsigned idx=0; unsigned acc=0;
  for(int i=0;i<n;++i){ unsigned v=*(const volatile unsigned*)(p+idx); acc+=v; unsigned z; asm volatile("v_and_b32 %0, 0, %1":"=v"(z):"v"(v)); idx = (unsigned)(i+1)*stride + z; }
  sink[0]=acc;
}
extern "C" void kernel_launch(void* const* d_in, const int* in_sizes, int n_in, void* d_out, int out_size, void* d_ws, size_t ws_size, hipStream_t stream) {
  const u16* x=(const u16*)d_in[0]; const u16* wqkv=(const u16*)d_in[1]; const u16* wphi=(const u16*)d_in[2]; const u16* wo=(const u16*)d_in[3];
  const u16* wg=(const u16*)d_in[5]; const u16* wu=(const u16*)d_in[6]; const u16* wd=(const u16*)d_in[7];
  char* ws=(char*)d_ws; size_t off=0; auto alloc=[&](size_t b){ void* p=ws+off; off+=(b+255)/256*256; return p; };
  u16* xn=(u16*)alloc(S*D*2); float* qkv=(float*)alloc((size_t)S*1920*4); float* g=(float*)alloc((size_t)S*F*4); float* u=(float*)alloc((size_t)S*F*4);
  u16* h=(u16*)alloc(S*F*2); float* phiq=(float*)alloc((size_t)S*H*40*4); float* phik=(float*)alloc((size_t)S*H*40*4); float* kv=(float*)alloc(H*40*21*4);
  u16* o=(u16*)alloc(S*D*2); float* acc=(float*)alloc(S*D*4);
  probe_k<<<1,64,0,stream>>>((const unsigned*)d_in[4],(unsigned*)(ws+200000000),NPROBE,STRIDE);
  ln_k<<<S,256,0,stream>>>(x,xn);
  gemm_k<<<dim3(1920/64,S/4),256,0,stream>>>(xn,D,wqkv,1920,qkv,1920,D,0);
  gemm_k<<<dim3(F/64,S/4),256,0,stream>>>(xn,D,wg,F,g,F,D,0);
  gemm_k<<<dim3(F/64,S/4),256,0,stream>>>(xn,D,wu,F,u,F,D,0);
  glu_k<<<S*F/256,256,0,stream>>>(g,u,h);
  phi_k<<<S,64,0,stream>>>(qkv,wphi,phiq,phik);
  kv_k<<<H,896,0,stream>>>(qkv,phik,kv);
  o_k<<<S,640,0,stream>>>(phiq,kv,o);
  gemm_k<<<dim3(D/64,S/4),256,0,stream>>>(o,D,wo,D,acc,D,D,0);
  gemm_k<<<dim3(D/64,S/4),256,0,stream>>>(h,F,wd,D,acc,D,F,1);
  fin_k<<<S*D/256,256,0,stream>>>(x,acc,(u16*)d_out);
}
```

```cpp
#include <hip/hip_runtime.h>
#include <hip/hip_bf16.h>
typedef unsigned short u16;
typedef unsigned long long u64;
typedef short s4 __attribute__((ext_vector_type(4)));
typedef short s8 __attribute__((ext_vector_type(8)));
typedef __bf16 b8 __attribute__((ext_vector_type(8)));
typedef __bf16 b2 __attribute__((ext_vector_type(2)));
typedef float f32x16 __attribute__((ext_vector_type(16)));
typedef float f32x4 __attribute__((ext_vector_type(4)));
typedef unsigned u32x4 __attribute__((ext_vector_type(4)));
typedef unsigned u32x2 __attribute__((ext_vector_type(2)));
#define LAS __attribute__((address_space(3)))
#define DI __device__ __forceinline__
typedef __amdgpu_buffer_rsrc_t rsrc_t;

constexpr int S = 2048, D = 640, F = 1536;
constexpr unsigned WS_EPOCH = 0;
constexpr unsigned WS_FP0 = 1024, WS_FP1 = 3072, WS_FH = 5120, WS_FO = 7168, WS_FKV = 9216;
constexpr unsigned WS_STATS = 16384;
constexpr unsigned WS_KVP = 32768;
constexpr unsigned KVP_STRIDE = 1792;
constexpr unsigned WS_WQP = 1u << 20;
constexpr unsigned WS_W2P = 4u << 20;
constexpr unsigned WS_ACT = 8u << 20;
constexpr unsigned WS_USED = 17u << 20;
constexpr unsigned OOB = 0x80000000u;
constexpr unsigned L_X = 0;
constexpr unsigned L_W = 49152;
constexpr unsigned W_STAGE = 24832, W_UOFF = 12320;
constexpr unsigned L_MISC = L_W + 4 * W_STAGE;
constexpr unsigned L_CS = L_MISC;
constexpr unsigned L_WPHI = L_CS + 5120;
constexpr unsigned L_BRING = 0;
constexpr unsigned B_STAGE = 20480;
constexpr unsigned L_KVR = 122880;
constexpr unsigned L_KVS = 149760;
constexpr unsigned L_RED = 0;
constexpr unsigned L_DUMMY = 161792;
constexpr unsigned LDS_BYTES = 160 * 1024;

DI rsrc_t mk_rsrc(const void* p, unsigned bytes) { return __builtin_amdgcn_make_buffer_rsrc((void*)p, 0, bytes, 0x00020000); }
DI void dma16(unsigned lds_addr, unsigned voff, rsrc_t r, unsigned soff) {
  asm volatile("s_mov_b32 m0, %0\n\ts_nop 0\n\tbuffer_load_dwordx4 %1, %2, %3 offen lds" ::"s"(lds_addr), "v"(voff), "s"(r), "s"(soff) : "memory");
}
DI void dma16_sc1(unsigned lds_addr, unsigned voff, rsrc_t r, unsigned soff) {
  asm volatile("s_mov_b32 m0, %0\n\ts_nop 0\n\tbuffer_load_dwordx4 %1, %2, %3 offen sc1 lds" ::"s"(lds_addr), "v"(voff), "s"(r), "s"(soff) : "memory");
}
DI unsigned ld_u16(rsrc_t r, unsigned voff, unsigned soff) { unsigned v; asm volatile("buffer_load_ushort %0, %1, %2, %3 offen" : "=v"(v) : "v"(voff), "s"(r), "s"(soff) : "memory"); return v; }
DI unsigned ld_b32(rsrc_t r, unsigned voff, unsigned soff) { unsigned v; asm volatile("buffer_load_dword %0, %1, %2, %3 offen" : "=v"(v) : "v"(voff), "s"(r), "s"(soff) : "memory"); return v; }
DI u32x2 ld_b64_sc1(rsrc_t r, unsigned voff, unsigned soff) { u32x2 v; asm volatile("buffer_load_dwordx2 %0, %1, %2, %3 offen sc1" : "=v"(v) : "v"(voff), "s"(r), "s"(soff) : "memory"); return v; }
DI u32x4 ld_b128_sc1(rsrc_t r, unsigned voff, unsigned soff) { u32x4 v; asm volatile("buffer_load_dwordx4 %0, %1, %2, %3 offen sc1" : "=v"(v) : "v"(voff), "s"(r), "s"(soff) : "memory"); return v; }
DI u32x4 ld_b128(rsrc_t r, unsigned voff, unsigned soff) { u32x4 v; asm volatile("buffer_load_dwordx4 %0, %1, %2, %3 offen" : "=v"(v) : "v"(voff), "s"(r), "s"(soff) : "memory"); return v; }
DI void st_b128_sc1(u32x4 v, rsrc_t r, unsigned voff) { asm volatile("buffer_store_dwordx4 %0, %1, %2, 0 offen sc1\n\ts_nop 1" ::"v"(v), "v"(voff), "s"(r) : "memory"); }
DI void st_b64_sc1(u32x2 v, rsrc_t r, unsigned voff) { asm volatile("buffer_store_dwordx2 %0, %1, %2, 0 offen sc1" ::"v"(v), "v"(voff), "s"(r) : "memory"); }
DI void st_b128(u32x4 v, rsrc_t r, unsigned voff) { asm volatile("buffer_store_dwordx4 %0, %1, %2, 0 offen\n\ts_nop 1" ::"v"(v), "v"(voff), "s"(r) : "memory"); }
#define TIE(x) asm volatile("" : "+v"(x))
#define WAITV(n) asm volatile("s_waitcnt vmcnt(" #n ")" ::: "memory")
#define WAITV_BAR(n) asm volatile("s_waitcnt vmcnt(" #n ")\n\ts_barrier" ::: "memory")
#define WAITVL_BAR(n) asm volatile("s_waitcnt vmcnt(" #n ") lgkmcnt(0)\n\ts_barrier" ::: "memory")
#define LBAR() asm volatile("s_waitcnt lgkmcnt(0)\n\ts_barrier" ::: "memory")
#define SB() __builtin_amdgcn_sched_barrier(0)
#define MFMA32(a, b, c) __builtin_amdgcn_mfma_f32_32x32x16_bf16(__builtin_bit_cast(b8, (a)), __builtin_bit_cast(b8, (b)), (c), 0, 0, 0)
#define MFMA16(a, b, c) __builtin_amdgcn_mfma_f32_16x16x32_bf16(__builtin_bit_cast(b8, (a)), __builtin_bit_cast(b8, (b)), (c), 0, 0, 0)
DI s8 lds_b128(unsigned addr) { return *(LAS s8*)(size_t)addr; }
DI s8 tr_frag(unsigned addr, unsigned off2) {
  s4 a = __builtin_amdgcn_ds_read_tr16_b64_v4i16((LAS s4*)(size_t)addr);
  s4 b = __builtin_amdgcn_ds_read_tr16_b64_v4i16((LAS s4*)(size_t)(addr + off2));
  return __builtin_shufflevector(a, b, 0, 1, 2, 3, 4, 5, 6, 7);
}
DI float dot_ones(s8 f, float acc) {
  u32x4 u = __builtin_bit_cast(u32x4, f);
#pragma unroll
  for (int i = 0; i < 4; ++i) { const unsigned t = u[i]; acc = __builtin_amdgcn_fdot2_f32_bf16(__builtin_bit_cast(b2, t), __builtin_bit_cast(b2, 0x3f803f80u), acc, false); }
  asm volatile("" : "+v"(acc));
  return acc;
}
DI unsigned pk2(float lo, float hi) { b2 c; c[0] = (__bf16)lo; c[1] = (__bf16)hi; return __builtin_bit_cast(unsigned, c); }
DI float bf_lo(unsigned u) { return __uint_as_float(u << 16); }
DI float bf_hi(unsigned u) { return __uint_as_float(u & 0xffff0000u); }
DI float swap_add(float v) { unsigned u = __float_as_uint(v); auto r = __builtin_amdgcn_permlane32_swap(u, u, false, false); return __uint_as_float(r[0]) + __uint_as_float(r[1]); }
DI float swap_max(float v) { unsigned u = __float_as_uint(v); auto r = __builtin_amdgcn_permlane32_swap(u, u, false, false); return fmaxf(__uint_as_float(r[0]), __uint_as_float(r[1])); }
DI float swap_min(float v) { unsigned u = __float_as_uint(v); auto r = __builtin_amdgcn_permlane32_swap(u, u, false, false); return fminf(__uint_as_float(r[0]), __uint_as_float(r[1])); }
DI s8 pack_step(const f32x16& x, int s) {
  return __builtin_bit_cast(s8, (u32x4){pk2(x[8 * s], x[8 * s + 1]), pk2(x[8 * s + 2], x[8 * s + 3]), pk2(x[8 * s + 4], x[8 * s + 5]), pk2(x[8 * s + 6], x[8 * s + 7])});
}
template <int I> struct IC { static constexpr int v = I; };
template <int B, int E, class Fn> DI void static_for(Fn&& f) { if constexpr (B < E) { f(IC<B>{}); static_for<B + 1, E>(f); } }
DI bool flags_ok(u32x2 v, u32x2 tag, bool act) { return __all(!act || (v[0] == tag[0] && v[1] == tag[1])); }
DI void wait_flags(rsrc_t rws, unsigned base, int n, u32x2 tag, int l) {
  const bool act = l < n; const unsigned off = act ? base + l * 8 : OOB;
  for (int it = 0; it < (1 << 17); ++it) {
    u32x2 v; asm volatile("buffer_load_dwordx2 %0, %1, %2, 0 offen sc1\n\ts_waitcnt vmcnt(0)" : "=&v"(v) : "v"(off), "s"(rws) : "memory");
    if (flags_ok(v, tag, act)) return;
    __builtin_amdgcn_s_sleep(1);
  }
}
DI void wait_flags256(rsrc_t rws, unsigned base, u32x2 tag, int l) {
  for (int it = 0; it < (1 << 17); ++it) {
    u32x4 a, b;
    asm volatile("buffer_load_dwordx4 %0, %2, %3, 0 offen sc1\n\tbuffer_load_dwordx4 %1, %2, %3, 0 offen offset:16 sc1\n\ts_waitcnt vmcnt(0)" : "=&v"(a), "=&v"(b) : "v"(base + l * 32), "s"(rws) : "memory");
    const bool ok = a[0] == tag[0] && a[1] == tag[1] && a[2] == tag[0] && a[3] == tag[1] && b[0] == tag[0] && b[1] == tag[1] && b[2] == tag[0] && b[3] == tag[1];
    if (__all(ok)) return;
    __builtin_amdgcn_s_sleep(1);
  }
}
DI bool flags256_ok(u32x4 a, u32x4 b, u32x2 tag) {
  return __all(a[0] == tag[0] && a[1] == tag[1] && a[2] == tag[0] && a[3] == tag[1] && b[0] == tag[0] && b[1] == tag[1] && b[2] == tag[0] && b[3] == tag[1]);
}
DI void phi_map(const f32x16& p, unsigned (&o)[10]) {
  float mx = p[0], mn = p[0];
#pragma unroll
  for (int r = 1; r < 10; ++r) { mx = fmaxf(mx, p[r]); mn = fminf(mn, p[r]); }
  mx = swap_max(mx); mn = swap_min(mn);
  const float L2E = 1.4426950408889634f;
  const float bp = -mx * L2E, bn = mn * L2E;
  float ep[10], en[10]; float sp = 0.f, sn = 0.f;
#pragma unroll
  for (int r = 0; r < 10; ++r) { ep[r] = __builtin_amdgcn_exp2f(__builtin_fmaf(p[r], L2E, bp)); en[r] = __builtin_amdgcn_exp2f(__builtin_fmaf(p[r], -L2E, bn)); sp += ep[r]; sn += en[r]; }
  sp = swap_add(sp); sn = swap_add(sn);
  const float rp = __builtin_amdgcn_rcpf(sp), rn = __builtin_amdgcn_rcpf(sn);
#pragma unroll
  for (int r = 0; r < 5; ++r) { o[r] = pk2(ep[2 * r] * rp, ep[2 * r + 1] * rp); o[5 + r] = pk2(en[2 * r] * rn, en[2 * r + 1] * rn); }
}

#define STAMP(i)

__global__ __launch_bounds__(512, 2) void fused_k(const u16* __restrict__ x, const u16* __restrict__ wqkv, const u16* __restrict__ wphi, const u16* __restrict__ wo,
                                                   const u16* __restrict__ wg, const u16* __restrict__ wu, const u16* __restrict__ wd, u16* __restrict__ out, char* __restrict__ ws) {
  extern __shared__ __attribute__((aligned(256))) char smem[];
  const int tid = threadIdx.x; const int l = tid & 63; const int w = __builtin_amdgcn_readfirstlane(tid >> 6);
  const int tg = w & 3, ch = w >> 2; const int l31 = l & 31, h = l >> 5;
  const int b = blockIdx.x, xcd = b & 7, idx = b >> 3;
  const int rb = (xcd & 1) * 8 + (idx & 7), cg = (xcd >> 1) * 4 + (idx >> 3);
  const int r2 = (xcd & 3) * 8 + (idx & 7), nb = (xcd >> 2) * 4 + (idx >> 3);
  STAMP(0);
  const rsrc_t rx = mk_rsrc(x, S * D * 2);
  const rsrc_t rw = mk_rsrc(w < 4 ? wg : wu, D * F * 2);
  const rsrc_t rq = mk_rsrc(wqkv, D * 1920 * 2);
  const rsrc_t rws = mk_rsrc(ws, WS_USED);
  unsigned vx[2], vw[3];
#pragma unroll
  for (int i = 0; i < 2; ++i) { const int r = 8 * (2 * w + i) + (l >> 3); const int c = (l & 7) ^ ((r >> 1) & 7); vx[i] = (128 * rb + r) * 1280 + c * 16; }
#pragma unroll
  for (int i = 0; i < 3; ++i) { const int p = 64 * (3 * (w & 3) + i) + l; const int k = p / 12, c = p % 12; vw[i] = k * 3072 + cg * 192 + c * 16; }
  const unsigned lx0 = L_X + 2 * w * 1024;
  const unsigned lw0 = L_W + (w < 4 ? 0 : W_UOFF) + 3 * (w & 3) * 1024;
  auto issue1 = [&](int s) {
#pragma unroll
    for (int i = 0; i < 2; ++i) dma16(lx0 + (s % 3) * 16384 + i * 1024, vx[i], rx, s * 128);
#pragma unroll
    for (int i = 0; i < 3; ++i) dma16(lw0 + (s & 3) * W_STAGE + i * 1024, vw[i], rw, s * 64 * 3072);
  };
  issue1(0);
  u32x2 ep = ld_b64_sc1(rws, WS_EPOCH, 0);
  unsigned sx[5];
  const int srow = 128 * rb + 8 * cg + w;
#pragma unroll
  for (int i = 0; i < 5; ++i) sx[i] = ld_b32(rx, srow * 1280 + (l + 64 * i) * 4, 0);
  unsigned pq[2][8]; unsigned pqdst[2];
#pragma unroll
  for (int z = 0; z < 2; ++z) {
    const int ks = z == 0 ? rb + 16 * (w >> 2) : rb + 32; const bool valid = z == 0 || (rb < 8 && w < 4);
    const int hc = (w >> 1) & 1, t = w & 1; const int head = 2 * cg + hc;
    int col; bool cv = true;
    if (t == 0) col = l31 < 20 ? head * 20 + l31 : 640 + head * 20 + (l31 - 20);
    else { col = l31 < 8 ? 640 + head * 20 + 12 + l31 : 1280 + head * 20 + (l31 - 8); cv = l31 < 28; }
    const unsigned vo = (valid && cv) ? (unsigned)((16 * ks + 8 * h) * 3840 + col * 2) : OOB;
#pragma unroll
    for (int j = 0; j < 8; ++j) pq[z][j] = ld_u16(rq, vo, j * 3840);
    pqdst[z] = valid ? WS_WQP + ((head * 40 + ks) * 2 + t) * 1024 + l * 16 : OOB;
  }
  unsigned pw[8];
  {
    const rsrc_t rp = mk_rsrc(wphi, 32 * 400 * 2);
    const int reg = (l31 & 3) + 4 * (l31 >> 3), hh = (l31 >> 2) & 1; const int f = 10 * hh + reg; const bool fv = reg < 10;
    const int head = 2 * cg + ch;
#pragma unroll
    for (int j = 0; j < 8; ++j) {
      const int rr = 8 * (j >> 2) + 4 * h + (j & 3);
      int d; bool dv;
      if (tg == 0) { d = rr; dv = true; } else if (tg == 1) { d = 16 + rr; dv = d < 20; } else if (tg == 2) { d = 16 + rr - 20; dv = d >= 0; } else { d = 12 + rr; dv = rr < 8; }
      pw[j] = ld_u16(rp, (fv && dv) ? (unsigned)((head * 400 + d * 20 + f) * 2) : OOB, 0);
    }
  }
  issue1(1); issue1(2);
  WAITV_BAR(10);
  STAMP(1);
  TIE(ep);
  u32x2 tag;
  { const unsigned lo = __builtin_amdgcn_readfirstlane(ep[0]), hi = __builtin_amdgcn_readfirstlane(ep[1]); tag[0] = lo + 1u; tag[1] = hi + (lo == 0xffffffffu ? 1u : 0u); }
  {
    float v[10]; float s = 0.f;
#pragma unroll
    for (int i = 0; i < 5; ++i) { TIE(sx[i]); v[2 * i] = bf_lo(sx[i]); v[2 * i + 1] = bf_hi(sx[i]); s += v[2 * i] + v[2 * i + 1]; }
#pragma unroll
    for (int o = 32; o > 0; o >>= 1) s += __shfl_xor(s, o);
    const float m = s * (1.f / D); float q = 0.f;
#pragma unroll
    for (int i = 0; i < 10; ++i) { const float d = v[i] - m; q += d * d; }
#pragma unroll
    for (int o = 32; o > 0; o >>= 1) q += __shfl_xor(q, o);
    const float rs = 1.f / sqrtf(q * (1.f / D) + 1e-5f);
    st_b64_sc1((u32x2){__float_as_uint(m), __float_as_uint(rs)}, rws, l == 0 ? WS_STATS + srow * 8 : OOB);
  }
#pragma unroll
  for (int z = 0; z < 2; ++z) {
#pragma unroll
    for (int j = 0; j < 8; ++j) TIE(pq[z][j]);
    st_b128_sc1((u32x4){pq[z][0] | (pq[z][1] << 16), pq[z][2] | (pq[z][3] << 16), pq[z][4] | (pq[z][5] << 16), pq[z][6] | (pq[z][7] << 16)}, rws, pqdst[z]);
  }
  {
#pragma unroll
    for (int j = 0; j < 8; ++j) TIE(pw[j]);
    *(LAS u32x4*)(size_t)(L_WPHI + w * 1024 + l * 16) = (u32x4){pw[0] | (pw[1] << 16), pw[2] | (pw[3] << 16), pw[4] | (pw[5] << 16), pw[6] | (pw[7] << 16)};
  }
  const int gi = l >> 4, q4 = (l >> 2) & 3, p4 = l & 3;
  const unsigned qoff = (p4 == 1) ? 8 : (p4 == 2) ? 4 : (p4 == 3) ? 12 : 0;
  const unsigned wl = L_W + ((gi & 1) ? W_UOFF : 0) + (8 * h + q4) * 192 + 2 * qoff + 96 * ch;
  const int xrow = 32 * tg + l31;
  const unsigned xl = L_X + xrow * 128 + ((h ^ ((xrow >> 1) & 7)) << 4);
  s8 XF[40];
  f32x16 acc[3]; float cs[3];
#pragma unroll
  for (int i = 0; i < 3; ++i) { cs[i] = 0.f;
#pragma unroll
    for (int r = 0; r < 16; ++r) acc[i][r] = 0.f; }
  s8 wf[2][3];
  auto rdx = [&](int s, int j) { const unsigned ks = (tg + j) & 3; XF[4 * s + j] = lds_b128((xl ^ (ks * 32)) + (s % 3) * 16384); };
  auto rdw = [&](int n, int i) { const int s = n >> 2, j = n & 3; const unsigned ks = (tg + j) & 3; wf[n & 1][i] = tr_frag(wl + ks * 3072 + (s & 3) * W_STAGE + i * 32, 768); };
  const unsigned vq2 = WS_WQP + ((2 * cg + ch) * 40 + tg) * 2048 + l * 16;
  auto slot2 = [&](int q) -> unsigned { return q == 0 || q == 7 ? 16384u : q == 1 || q == 8 ? L_W + 2 * W_STAGE : q == 2 || q == 9 ? 32768u : q == 3 ? L_W + 3 * W_STAGE : q == 4 ? 0u : q == 5 ? L_W : L_W + W_STAGE; };
  auto issue2 = [&](int q) { dma16_sc1(slot2(q) + w * 2048, vq2, rws, q * 8192); dma16_sc1(slot2(q) + w * 2048 + 1024, vq2, rws, q * 8192 + 1024); };
  unsigned p2[2][8]; unsigned p2dst[2];
  u32x4 pl0, pl1; u32x2 st;
  rdx(0, 0); rdw(0, 0); rdw(0, 1); rdw(0, 2); rdx(0, 1); rdx(0, 2); rdx(0, 3);
  static_for<0, 40>([&](auto N_) {
    constexpr int n = decltype(N_)::v;
    constexpr int s = n >> 2, j = n & 3;
    if constexpr (n == 3) WAITV_BAR(8);
    if constexpr (n == 7) WAITV_BAR(8);
    if constexpr (n == 11) { WAITV_BAR(5); st_b64_sc1(tag, rws, tid == 0 ? WS_FP0 + b * 8 : OOB); }
    if constexpr (n == 15) {
      WAITV_BAR(6);
      const rsrc_t rd = mk_rsrc(wd, F * D * 2), ro = mk_rsrc(wo, D * D * 2);
#pragma unroll
      for (int z = 0; z < 2; ++z) {
        const int fid = b * 8 + w + 2048 * z; const bool valid = fid < 2720; const int kst = fid / 40, ct = fid % 40;
        const bool isd = kst < 48; const int krow = (isd ? 32 * kst : 32 * (kst - 48)) + 8 * (l >> 4);
        const unsigned vo = valid ? (unsigned)(krow * 1280 + (16 * ct + (l & 15)) * 2) : OOB;
        const rsrc_t rr = isd ? rd : ro;
#pragma unroll
        for (int jj = 0; jj < 8; ++jj) p2[z][jj] = ld_u16(rr, vo, jj * 1280);
        p2dst[z] = valid ? WS_W2P + (((ct / 5) * 68 + kst) * 5 + ct % 5) * 1024 + l * 16 : OOB;
      }
    }
    if constexpr (n == 19) WAITV_BAR(21);
    if constexpr (n == 23) {
      WAITV_BAR(5);
#pragma unroll
      for (int z = 0; z < 2; ++z) {
#pragma unroll
        for (int jj = 0; jj < 8; ++jj) TIE(p2[z][jj]);
        st_b128_sc1((u32x4){p2[z][0] | (p2[z][1] << 16), p2[z][2] | (p2[z][3] << 16), p2[z][4] | (p2[z][5] << 16), p2[z][6] | (p2[z][7] << 16)}, rws, p2dst[z]);
      }
    }
    if constexpr (n == 27) { WAITV_BAR(7); pl0 = ld_b128_sc1(rws, WS_FP0 + l * 32, 0); pl1 = ld_b128_sc1(rws, WS_FP0 + l * 32 + 16, 0); }
    if constexpr (n == 31) {
      WAITV(5); TIE(pl0); TIE(pl1);
      if (!flags256_ok(pl0, pl1, tag)) wait_flags256(rws, WS_FP0, tag, l);
      asm volatile("s_barrier" ::: "memory");
      st_b64_sc1(tag, rws, tid == 0 ? WS_FP1 + b * 8 : OOB);
      st = ld_b64_sc1(rws, WS_STATS + (128 * rb + xrow) * 8, 0);
      issue2(0); issue2(1);
    }
    if constexpr (n == 35) { WAITV_BAR(6); issue2(2); issue2(3); }
    SB();
#pragma unroll
    for (int i = 0; i < 3; ++i) {
      acc[i] = MFMA32(wf[n & 1][i], XF[n], acc[i]);
      SB();
      if (i == 0 && j == 3 && s < 9) rdx(s + 1, 0);
      if (n + 1 < 40) rdw(n + 1, i);
      if (j == 0 && s > 0) rdx(s, i + 1);
      if (j == 0) cs[i] = dot_ones(wf[n & 1][i], cs[i]);
      if (j == 3 && s + 3 < 10) {
        if (i == 0) { dma16(lx0 + ((s + 3) % 3) * 16384, vx[0], rx, (s + 3) * 128); dma16(lx0 + ((s + 3) % 3) * 16384 + 1024, vx[1], rx, (s + 3) * 128); }
        if (i == 1) { dma16(lw0 + ((s + 3) & 3) * W_STAGE, vw[0], rw, (s + 3) * 64 * 3072); dma16(lw0 + ((s + 3) & 3) * W_STAGE + 1024, vw[1], rw, (s + 3) * 64 * 3072); }
        if (i == 2) { dma16(lw0 + ((s + 3) & 3) * W_STAGE + 2048, vw[2], rw, (s + 3) * 64 * 3072); }
      }
      SB();
    }
  });
  STAMP(2);
#pragma unroll
  for (int i = 0; i < 3; ++i) { const float t = swap_add(cs[i]); if (h == 0) *(LAS float*)(size_t)(L_CS + ((ch * 5 + i) * 32 + l31) * 16 + tg * 4) = t; }
  WAITVL_BAR(6);
  issue2(4); issue2(5); issue2(6);
  TIE(st);
  const float mean = __uint_as_float(st[0]), rstd = __uint_as_float(st[1]);
  s8 bm;
  { const float nm = -mean; const unsigned hi = pk2(nm, 0.f) & 0xffffu; const float lo = nm - __uint_as_float(hi << 16); unsigned mpk = hi | (pk2(lo, 0.f) << 16); if (h) mpk = 0; bm = __builtin_bit_cast(s8, (u32x4){mpk, mpk, 0u, 0u}); }
  auto cs_frag = [&](int tile) -> s8 {
    const f32x4 pp = *(LAS f32x4*)(size_t)(L_CS + ((ch * 5 + tile) * 32 + l31) * 16);
    const float c = (pp[0] + pp[1]) + (pp[2] + pp[3]);
    unsigned hi = pk2(c, c); const float lo = c - __uint_as_float(hi << 16); unsigned lo2 = pk2(lo, lo);
    if (h) { hi = 0; lo2 = 0; }
    return __builtin_bit_cast(s8, (u32x4){hi, lo2, 0u, 0u});
  };
  const unsigned tok = 128 * rb + xrow;
  const unsigned actbase = WS_ACT + (tok >> 4) * 68 * 1024 + (l & 15) * 16;
  {
    const float gs = -1.4426950408889634f * rstd;
#pragma unroll
    for (int i = 0; i < 3; ++i) {
      acc[i] = MFMA32(cs_frag(i), bm, acc[i]);
      float hv[8];
#pragma unroll
      for (int r = 0; r < 8; ++r) { const float e = __builtin_amdgcn_exp2f(acc[i][r] * gs); hv[r] = acc[i][8 + r] * rstd * __builtin_amdgcn_rcpf(1.f + e); }
      const unsigned G = 12 * cg + 6 * ch + 2 * i + h;
      st_b128_sc1((u32x4){pk2(hv[0], hv[1]), pk2(hv[2], hv[3]), pk2(hv[4], hv[5]), pk2(hv[6], hv[7])}, rws, actbase + (G >> 2) * 1024 + (G & 3) * 256);
      SB();
    }
  }
  STAMP(3);
  f32x16 T[2]; float cs2[2] = {0.f, 0.f};
#pragma unroll
  for (int i = 0; i < 2; ++i)
#pragma unroll
    for (int r = 0; r < 16; ++r) T[i][r] = 0.f;
  s8 qf[2][2];
  const unsigned ql = ch * 8192 + l * 16;
  auto rdq = [&](int m, int t) { const int q = m >> 2, j = m & 3; const unsigned ks = (tg + j) & 3; qf[m & 1][t] = lds_b128(slot2(q) + ql + ks * 2048 + t * 1024); };
  rdq(0, 0); rdq(0, 1);
  static_for<0, 40>([&](auto M_) {
    constexpr int m = decltype(M_)::v;
    constexpr int j = m & 3;
    if constexpr (m == 3) WAITV_BAR(13);
    if constexpr (m == 7) { WAITV_BAR(11); issue2(7); }
    if constexpr (m == 11) { WAITV_BAR(2); st_b64_sc1(tag, rws, tid == 0 ? WS_FH + (rb * 16 + cg) * 8 : OOB); issue2(8); }
    if constexpr (m == 15) { WAITV_BAR(5); issue2(9); }
    if constexpr (m == 19) WAITV_BAR(7);
    if constexpr (m == 23) WAITV_BAR(7);
    if constexpr (m == 27) { WAITV_BAR(5); pl0 = ld_b128_sc1(rws, WS_FP1 + l * 32, 0); pl1 = ld_b128_sc1(rws, WS_FP1 + l * 32 + 16, 0); }
    if constexpr (m == 31) WAITV_BAR(4);
    if constexpr (m == 35) WAITV_BAR(0);
    SB();
#pragma unroll
    for (int t = 0; t < 2; ++t) {
      T[t] = MFMA32(qf[m & 1][t], XF[m], T[t]);
      SB();
      if (m + 1 < 40) rdq(m + 1, t);
      if (j == 0) cs2[t] = dot_ones(qf[m & 1][t], cs2[t]);
      SB();
    }
  });
  STAMP(4);
#pragma unroll
  for (int t = 0; t < 2; ++t) { const float v = swap_add(cs2[t]); if (h == 0) *(LAS float*)(size_t)(L_CS + ((ch * 5 + 3 + t) * 32 + l31) * 16 + tg * 4) = v; }
  TIE(pl0); TIE(pl1);
  if (!flags256_ok(pl0, pl1, tag)) wait_flags256(rws, WS_FP1, tag, l);
  LBAR();
  const unsigned vb = WS_W2P + nb * 68 * 5 * 1024 + l * 16;
  auto issueB = [&](int t) {
    const unsigned base = L_BRING + (t % 6) * B_STAGE;
    if (w < 4) { dma16_sc1(base + (3 * w) * 1024, vb, rws, t * B_STAGE + (3 * w) * 1024); dma16_sc1(base + (3 * w + 1) * 1024, vb, rws, t * B_STAGE + (3 * w + 1) * 1024); dma16_sc1(base + (3 * w + 2) * 1024, vb, rws, t * B_STAGE + (3 * w + 2) * 1024); }
    else { dma16_sc1(base + (12 + 2 * (w - 4)) * 1024, vb, rws, t * B_STAGE + (12 + 2 * (w - 4)) * 1024); dma16_sc1(base + (13 + 2 * (w - 4)) * 1024, vb, rws, t * B_STAGE + (13 + 2 * (w - 4)) * 1024); dma16_sc1(L_DUMMY, OOB, rws, 0); }
  };
#pragma unroll
  for (int t = 0; t < 6; ++t) issueB(t);
  unsigned phq[10];
  {
    T[0] = MFMA32(cs_frag(3), bm, T[0]);
    T[1] = MFMA32(cs_frag(4), bm, T[1]);
#pragma unroll
    for (int r = 0; r < 16; ++r) { T[0][r] *= rstd; T[1][r] *= rstd; }
    const s8 t00 = pack_step(T[0], 0), t01 = pack_step(T[0], 1), t10 = pack_step(T[1], 0);
    s8 t11 = pack_step(T[1], 1);
    { u32x4 u = __builtin_bit_cast(u32x4, t11); if (h) u[2] = (u[2] & 0xffff0000u) | 0x3f80u; t11 = __builtin_bit_cast(s8, u); }
    const unsigned wpl = L_WPHI + ch * 4096 + l * 16;
    f32x16 z16;
#pragma unroll
    for (int r = 0; r < 16; ++r) z16[r] = 0.f;
    f32x16 P = MFMA32(lds_b128(wpl), t00, z16); P = MFMA32(lds_b128(wpl + 1024), t01, P);
    phi_map(P, phq);
    f32x16 Pk = MFMA32(lds_b128(wpl + 2048), t01, z16); Pk = MFMA32(lds_b128(wpl + 3072), t10, Pk);
    unsigned phk[10]; phi_map(Pk, phk);
    const unsigned one_lo = 0x3f80u, one_hi = 0x3f800000u;
    auto sel = [&](int kk) -> s8 {
      u32x4 u;
#pragma unroll
      for (int d2 = 0; d2 < 4; ++d2) u[d2] = (kk == 2 * d2) ? one_lo : (kk == 2 * d2 + 1) ? one_hi : 0u;
      return __builtin_bit_cast(s8, u);
    };
    const s8 E0 = sel(l31 - 8 * h), E1 = sel(l31 - 16 - 8 * h);
    const s8 E2 = sel((l31 >> 2) == h && l31 < 8 ? (l31 & 3) : -1);
    const s8 F0 = __builtin_bit_cast(s8, (u32x4){phk[0], phk[1], phk[2], phk[3]}), F1 = __builtin_bit_cast(s8, (u32x4){phk[4], phk[5], phk[6], phk[7]}), F2 = __builtin_bit_cast(s8, (u32x4){phk[8], phk[9], 0u, 0u});
    f32x16 Z0 = MFMA32(F0, E0, z16); Z0 = MFMA32(F1, E1, Z0);
    f32x16 Z1 = MFMA32(F2, E2, z16);
    const int vr = l31 < 20 ? 8 + l31 : (l31 == 20 ? 28 : -100);
    auto selv = [&](int s) -> s8 { const int rr = vr - 16 * s; const bool in = rr >= 0 && rr < 16 && ((rr >> 2) & 1) == h; return sel(in ? 4 * (rr >> 3) + (rr & 3) : -1); };
    f32x16 V = MFMA32(t10, selv(0), z16); V = MFMA32(t11, selv(1), V);
    const s8 v0 = pack_step(V, 0), v1 = pack_step(V, 1);
    f32x16 KV0 = MFMA32(pack_step(Z0, 0), v0, z16); KV0 = MFMA32(pack_step(Z0, 1), v1, KV0);
    f32x16 KV1 = MFMA32(pack_step(Z1, 0), v0, z16); KV1 = MFMA32(pack_step(Z1, 1), v1, KV1);
    if (l31 < 21) {
      const unsigned kb = L_KVR + w * 3360 + l31 * 160;
#pragma unroll
      for (int g = 0; g < 4; ++g) *(LAS f32x4*)(size_t)(kb + (8 * g + 4 * h) * 4) = (f32x4){KV0[4 * g], KV0[4 * g + 1], KV0[4 * g + 2], KV0[4 * g + 3]};
      *(LAS f32x4*)(size_t)(kb + (32 + 4 * h) * 4) = (f32x4){KV1[0], KV1[1], KV1[2], KV1[3]};
    }
  }
  LBAR();
  {
    const int c = tid & 255, hc = tid >> 8;
    u32x4 o = {0u, 0u, 0u, 0u};
    if (c < 105) {
      float a[8];
#pragma unroll
      for (int e = 0; e < 8; ++e) a[e] = 0.f;
#pragma unroll
      for (int g = 0; g < 4; ++g) {
        const unsigned ad = L_KVR + (hc * 4 + g) * 3360 + c * 32;
        const f32x4 u0 = *(LAS f32x4*)(size_t)ad, u1 = *(LAS f32x4*)(size_t)(ad + 16);
#pragma unroll
        for (int e = 0; e < 4; ++e) { a[e] += u0[e]; a[4 + e] += u1[e]; }
      }
      o = (u32x4){pk2(a[0], a[1]), pk2(a[2], a[3]), pk2(a[4], a[5]), pk2(a[6], a[7])};
    }
    st_b128_sc1(o, rws, c < 105 ? WS_KVP + ((cg * 2 + hc) * 16 + rb) * KVP_STRIDE + c * 16 : OOB);
  }
  WAITV_BAR(0);
  st_b64_sc1(tag, rws, tid == 0 ? WS_FKV + (cg * 16 + rb) * 8 : OOB);
  STAMP(5);
  const int kq = w & 3, rh = w >> 2;
  const rsrc_t rout = mk_rsrc(out, S * D * 2);
  wait_flags(rws, WS_FH + (r2 >> 1) * 128, 16, tag, l);
  const unsigned va = WS_ACT + (4 * r2 + 2 * rh) * 68 * 1024 + kq * 1024 + l * 16;
  f32x4 a2[2][5];
#pragma unroll
  for (int a = 0; a < 2; ++a)
#pragma unroll
    for (int c = 0; c < 5; ++c) a2[a][c] = (f32x4){0.f, 0.f, 0.f, 0.f};
  u32x4 af[3][2];
  auto lda = [&](int t) { af[t % 3][0] = ld_b128_sc1(rws, va, t * 4096); af[t % 3][1] = ld_b128_sc1(rws, va + 68 * 1024, t * 4096); };
  lda(0); lda(1);
  static_for<0, 17>([&](auto T_) {
    constexpr int t = decltype(T_)::v;
    if constexpr (t == 3) {
      wait_flags(rws, WS_FKV + cg * 128, 16, tag, l);
      const int c = 27 * tg + l; const bool act = l < 27 && c < 105;
      u32x4 kp[16];
#pragma unroll
      for (int r = 0; r < 16; ++r) kp[r] = ld_b128_sc1(rws, act ? WS_KVP + ((cg * 2 + ch) * 16) * KVP_STRIDE + c * 16 : OOB, r * KVP_STRIDE);
      WAITV(0);
      float a[8];
#pragma unroll
      for (int e = 0; e < 8; ++e) a[e] = 0.f;
#pragma unroll
      for (int r = 0; r < 16; ++r) { TIE(kp[r]);
#pragma unroll
        for (int e = 0; e < 4; ++e) { a[2 * e] += bf_lo(kp[r][e]); a[2 * e + 1] += bf_hi(kp[r][e]); } }
      if (act) { const int d = c / 5, cc = c % 5; *(LAS u32x4*)(size_t)(L_KVS + ch * 2016 + d * 96 + cc * 16) = (u32x4){pk2(a[0], a[1]), pk2(a[2], a[3]), pk2(a[4], a[5]), pk2(a[6], a[7])}; }
      LBAR();
      const int reg = (l31 & 3) + 4 * (l31 >> 3), hh = (l31 >> 2) & 1;
      int d;
      if (reg == 12) d = 20; else if (reg > 12) d = 20; else if (ch == 0) d = reg < 8 ? 8 * hh + reg : (hh == 0 ? 8 + reg : 20); else d = reg < 8 ? (hh == 0 ? 4 + reg : 12 + reg) : (hh == 0 ? reg - 8 : 20);
      const unsigned ka = L_KVS + ch * 2016 + d * 96;
      const s8 A0 = lds_b128(ka + h * 16), A1 = lds_b128(ka + 32 + h * 16);
      const u32x2 a2l = *(LAS u32x2*)(size_t)(ka + 64 + h * 8);
      const s8 A2 = __builtin_bit_cast(s8, (u32x4){a2l[0], a2l[1], 0u, 0u});
      f32x16 O;
#pragma unroll
      for (int r = 0; r < 16; ++r) O[r] = 0.f;
      O = MFMA32(A0, __builtin_bit_cast(s8, (u32x4){phq[0], phq[1], phq[2], phq[3]}), O);
      O = MFMA32(A1, __builtin_bit_cast(s8, (u32x4){phq[4], phq[5], phq[6], phq[7]}), O);
      O = MFMA32(A2, __builtin_bit_cast(s8, (u32x4){phq[8], phq[9], 0u, 0u}), O);
      const float rc = 1.f / (O[12] + 1e-6f);
      const unsigned G0 = 192 + 5 * cg + (ch == 0 ? h : 3 + h);
      st_b128_sc1((u32x4){pk2(O[0] * rc, O[1] * rc), pk2(O[2] * rc, O[3] * rc), pk2(O[4] * rc, O[5] * rc), pk2(O[6] * rc, O[7] * rc)}, rws, actbase + (G0 >> 2) * 1024 + (G0 & 3) * 256);
      const unsigned G2 = 192 + 5 * cg + 2;
      st_b64_sc1((u32x2){pk2(O[8] * rc, O[9] * rc), pk2(O[10] * rc, O[11] * rc)}, rws, h == 0 ? actbase + (G2 >> 2) * 1024 + (G2 & 3) * 256 + ch * 8 : OOB);
      WAITV_BAR(0);
      st_b64_sc1(tag, rws, tid == 0 ? WS_FO + (rb * 16 + cg) * 8 : OOB);
    }
    if constexpr (t == 10) wait_flags(rws, WS_FO + (r2 >> 1) * 128, 16, tag, l);
    {
      constexpr int nprev = (t == 0 || t == 1) ? 2 : ((t + 1 < 17 ? 2 : 0) + ((t + 4 < 17) ? 3 : 0));
      asm volatile("s_waitcnt vmcnt(%0)\n\ts_barrier" ::"i"(nprev) : "memory");
    }
    if (t + 2 < 17) lda(t + 2);
    if (t >= 1 && t + 5 < 17) issueB(t + 5);
    TIE(af[t % 3][0]); TIE(af[t % 3][1]);
    const unsigned bb = L_BRING + (t % 6) * B_STAGE + kq * 5120 + l * 16;
#pragma unroll
    for (int c = 0; c < 5; ++c) {
      const s8 bf = lds_b128(bb + c * 1024);
      a2[0][c] = MFMA16(bf, af[t % 3][0], a2[0][c]);
      a2[1][c] = MFMA16(bf, af[t % 3][1], a2[1][c]);
    }
  });
  STAMP(6);
  LBAR();
#pragma unroll
  for (int a = 0; a < 2; ++a)
#pragma unroll
    for (int c = 0; c < 5; ++c) *(LAS f32x4*)(size_t)(L_RED + (w * 10 + a * 5 + c) * 1024 + l * 16) = a2[a][c];
  LBAR();
#pragma unroll
  for (int it = 0; it < 2; ++it) {
    const int item = tid + 512 * it;
    const bool act = item < 640;
    const int tk = item / 10, cx = item % 10;
    const unsigned go = act ? (unsigned)((64 * r2 + tk) * 1280 + (80 * nb + 8 * cx) * 2) : OOB;
    u32x4 xr = ld_b128(rx, go, 0);
    float v[8];
#pragma unroll
    for (int e = 0; e < 8; ++e) v[e] = 0.f;
    if (act) {
      const int rh2 = tk >> 5, a = (tk >> 4) & 1, t16 = tk & 15, c = cx >> 1, g0 = (cx & 1) * 2;
#pragma unroll
      for (int k = 0; k < 4; ++k) {
        const unsigned ad = L_RED + ((rh2 * 4 + k) * 10 + a * 5 + c) * 1024 + (g0 * 16 + t16) * 16;
        const f32x4 u0 = *(LAS f32x4*)(size_t)ad, u1 = *(LAS f32x4*)(size_t)(ad + 256);
#pragma unroll
        for (int e = 0; e < 4; ++e) { v[e] += u0[e]; v[4 + e] += u1[e]; }
      }
    }
    WAITV(0); TIE(xr);
    st_b128((u32x4){pk2(v[0] + bf_lo(xr[0]), v[1] + bf_hi(xr[0])), pk2(v[2] + bf_lo(xr[1]), v[3] + bf_hi(xr[1])), pk2(v[4] + bf_lo(xr[2]), v[5] + bf_hi(xr[2])), pk2(v[6] + bf_lo(xr[3]), v[7] + bf_hi(xr[3]))}, rout, go);
  }
  if (b == 0 && tid == 0) st_b64_sc1(tag, rws, WS_EPOCH);
  STAMP(7);
}

extern "C" void kernel_launch(void* const* d_in, const int* in_sizes, int n_in, void* d_out, int out_size, void* d_ws, size_t ws_size, hipStream_t stream) {
  (void)in_sizes; (void)n_in; (void)out_size; (void)ws_size;
  hipFuncSetAttribute((const void*)fused_k, hipFuncAttributeMaxDynamicSharedMemorySize, LDS_BYTES);
  fused_k<<<256, 512, LDS_BYTES, stream>>>((const u16*)d_in[0], (const u16*)d_in[1], (const u16*)d_in[2], (const u16*)d_in[3], (const u16*)d_in[5], (const u16*)d_in[6], (const u16*)d_in[7], (u16*)d_out, (char*)d_ws);
}
```

```cpp
#include <hip/hip_runtime.h>
#include <hip/hip_bf16.h>
typedef unsigned short u16;
typedef unsigned long long u64;
typedef short s4 __attribute__((ext_vector_type(4)));
typedef short s8 __attribute__((ext_vector_type(8)));
typedef __bf16 b8 __attribute__((ext_vector_type(8)));
typedef __bf16 b2 __attribute__((ext_vector_type(2)));
typedef float f32x16 __attribute__((ext_vector_type(16)));
typedef float f32x4 __attribute__((ext_vector_type(4)));
typedef float f32x2 __attribute__((ext_vector_type(2)));
typedef unsigned u32x4 __attribute__((ext_vector_type(4)));
typedef unsigned u32x2 __attribute__((ext_vector_type(2)));
#define LAS __attribute__((address_space(3)))
#define DI __device__ __forceinline__
typedef __amdgpu_buffer_rsrc_t rsrc_t;

constexpr int S = 2048, D = 640, F = 1536;
constexpr unsigned WS_EPOCH = 0;
constexpr unsigned WS_FP0 = 1024, WS_FP1 = 3072, WS_FH = 5120, WS_FO = 7168, WS_FKV = 9216, WS_FCS = 11264;
constexpr unsigned WS_STATS = 16384;
constexpr unsigned WS_KVP = 32768;
constexpr unsigned KVP_STRIDE = 1792;
constexpr unsigned WS_WQP = 1u << 20;
constexpr unsigned WS_W2P = 4u << 20;
constexpr unsigned WS_CSP = 7u << 20;
constexpr unsigned WS_ACT = 8u << 20;
constexpr unsigned WS_USED = 17u << 20;
constexpr unsigned OOB = 0x80000000u;
constexpr unsigned L_X = 0;
constexpr unsigned L_W = 49152;
constexpr unsigned W_STAGE = 24832, W_UOFF = 12320;
constexpr unsigned L_MISC = L_W + 4 * W_STAGE;
constexpr unsigned L_W2S = L_MISC;
constexpr unsigned L_CS = L_W2S + 4096;
constexpr unsigned L_WPF = L_CS + 3072;
constexpr unsigned L_QRAW = L_W + 3 * W_STAGE;
constexpr unsigned L_WPR = L_QRAW + 12288;
constexpr unsigned L_W2A = L_QRAW + 14336;
constexpr unsigned L_BRING = 0;
constexpr unsigned B_STAGE = 20480;
constexpr unsigned L_KVR = 122880;
constexpr unsigned L_KVS = 149760;
constexpr unsigned L_DUMMY1 = L_QRAW + 23552;
constexpr unsigned L_DUMMY = 153856;
constexpr unsigned L_RED = 0;
constexpr unsigned LDS_BYTES = 160 * 1024;

DI rsrc_t mk_rsrc(const void* p, unsigned bytes) { return __builtin_amdgcn_make_buffer_rsrc((void*)p, 0, bytes, 0x00020000); }
DI void dma16(unsigned lds_addr, unsigned voff, rsrc_t r, unsigned soff) {
  asm volatile("s_mov_b32 m0, %0\n\ts_nop 0\n\tbuffer_load_dwordx4 %1, %2, %3 offen lds" ::"s"(lds_addr), "v"(voff), "s"(r), "s"(soff) : "memory");
}
DI void dma16_sc1(unsigned lds_addr, unsigned voff, rsrc_t r, unsigned soff) {
  asm volatile("s_mov_b32 m0, %0\n\ts_nop 0\n\tbuffer_load_dwordx4 %1, %2, %3 offen sc1 lds" ::"s"(lds_addr), "v"(voff), "s"(r), "s"(soff) : "memory");
}
DI unsigned ld_u16(rsrc_t r, unsigned voff, unsigned soff) { unsigned v; asm volatile("buffer_load_ushort %0, %1, %2, %3 offen" : "=v"(v) : "v"(voff), "s"(r), "s"(soff) : "memory"); return v; }
DI unsigned ld_b32(rsrc_t r, unsigned voff, unsigned soff) { unsigned v; asm volatile("buffer_load_dword %0, %1, %2, %3 offen" : "=v"(v) : "v"(voff), "s"(r), "s"(soff) : "memory"); return v; }
DI u32x2 ld_b64_sc1(rsrc_t r, unsigned voff, unsigned soff) { u32x2 v; asm volatile("buffer_load_dwordx2 %0, %1, %2, %3 offen sc1" : "=v"(v) : "v"(voff), "s"(r), "s"(soff) : "memory"); return v; }
DI u32x4 ld_b128_sc1(rsrc_t r, unsigned voff, unsigned soff) { u32x4 v; asm volatile("buffer_load_dwordx4 %0, %1, %2, %3 offen sc1" : "=v"(v) : "v"(voff), "s"(r), "s"(soff) : "memory"); return v; }
DI u32x2 ld_b64_m(rsrc_t r, unsigned voff, u64 mask) { u32x2 v; asm volatile("s_mov_b64 exec, %3\n\tbuffer_load_dwordx2 %0, %1, %2, 0 offen\n\ts_mov_b64 exec, -1" : "=v"(v) : "v"(voff), "s"(r), "s"(mask) : "memory"); return v; }
DI void st_b64(u32x2 v, rsrc_t r, unsigned voff) { asm volatile("buffer_store_dwordx2 %0, %1, %2, 0 offen" ::"v"(v), "v"(voff), "s"(r) : "memory"); }
DI u32x4 ld_b128(rsrc_t r, unsigned voff, unsigned soff) { u32x4 v; asm volatile("buffer_load_dwordx4 %0, %1, %2, %3 offen" : "=v"(v) : "v"(voff), "s"(r), "s"(soff) : "memory"); return v; }
DI void st_b128_sc1(u32x4 v, rsrc_t r, unsigned voff) {
 asm volatile("buffer_store_dwordx4 %0, %1, %2, 0 offen sc1\n\ts_nop 1" ::"v"(v), "v"(voff), "s"(r) : "memory"); }
DI void st_b64_sc1(u32x2 v, rsrc_t r, unsigned voff) { asm volatile("buffer_store_dwordx2 %0, %1, %2, 0 offen sc1" ::"v"(v), "v"(voff), "s"(r) : "memory"); }
DI void st_b128(u32x4 v, rsrc_t r, unsigned voff) { asm volatile("buffer_store_dwordx4 %0, %1, %2, 0 offen\n\ts_nop 1" ::"v"(v), "v"(voff), "s"(r) : "memory"); }
DI void st_b64_sc1_m(u32x2 v, rsrc_t r, unsigned voff, u64 mask) {
 asm volatile("s_mov_b64 exec, %3\n\tbuffer_store_dwordx2 %0, %1, %2, 0 offen sc1\n\ts_mov_b64 exec, -1" ::"v"(v), "v"(voff), "s"(r), "s"(mask) : "memory"); }
DI u32x2 ld_b64_sc1_m(rsrc_t r, unsigned voff, u64 mask) { u32x2 v; asm volatile("s_mov_b64 exec, %3\n\tbuffer_load_dwordx2 %0, %1, %2, 0 offen sc1\n\ts_mov_b64 exec, -1" : "=v"(v) : "v"(voff), "s"(r), "s"(mask) : "memory"); return v; }
DI u32x4 ld_b128_sc1_m(rsrc_t r, unsigned voff, unsigned soff, u64 mask) { u32x4 v; asm volatile("s_mov_b64 exec, %4\n\tbuffer_load_dwordx4 %0, %1, %2, %3 offen sc1\n\ts_mov_b64 exec, -1" : "=v"(v) : "v"(voff), "s"(r), "s"(soff), "s"(mask) : "memory"); return v; }
DI void st_b128_sc1_m(u32x4 v, rsrc_t r, unsigned voff, u64 mask) {
 asm volatile("s_mov_b64 exec, %3\n\tbuffer_store_dwordx4 %0, %1, %2, 0 offen sc1\n\ts_mov_b64 exec, -1\n\ts_nop 0" ::"v"(v), "v"(voff), "s"(r), "s"(mask) : "memory"); }
DI void dma16_m(unsigned lds_addr, unsigned voff, rsrc_t r, unsigned soff, u64 mask) {
  asm volatile("s_mov_b32 m0, %0\n\ts_mov_b64 exec, %4\n\tbuffer_load_dwordx4 %1, %2, %3 offen lds\n\ts_mov_b64 exec, -1" ::"s"(lds_addr), "v"(voff), "s"(r), "s"(soff), "s"(mask) : "memory");
}
#define TIE(x) asm volatile("" : "+v"(x))
#define WAITV(n) asm volatile("s_waitcnt vmcnt(" #n ")" ::: "memory")
#define WAITV_BAR(n) asm volatile("s_waitcnt vmcnt(" #n ")\n\ts_barrier" ::: "memory")
#define WAITVL_BAR(n) asm volatile("s_waitcnt vmcnt(" #n ") lgkmcnt(0)\n\ts_barrier" ::: "memory")
#define LBAR() asm volatile("s_waitcnt lgkmcnt(0)\n\ts_barrier" ::: "memory")
#define SB() __builtin_amdgcn_sched_barrier(0)
#define MFMA32(a, b, c) __builtin_amdgcn_mfma_f32_32x32x16_bf16(__builtin_bit_cast(b8, (a)), __builtin_bit_cast(b8, (b)), (c), 0, 0, 0)
#define MFMA16(a, b, c) __builtin_amdgcn_mfma_f32_16x16x32_bf16(__builtin_bit_cast(b8, (a)), __builtin_bit_cast(b8, (b)), (c), 0, 0, 0)
DI s8 lds_b128(unsigned addr) { return *(LAS s8*)(size_t)addr; }
DI s8 tr_frag(unsigned addr, unsigned off2) {
  s4 a = __builtin_amdgcn_ds_read_tr16_b64_v4i16((LAS s4*)(size_t)addr);
  s4 b = __builtin_amdgcn_ds_read_tr16_b64_v4i16((LAS s4*)(size_t)(addr + off2));
  return __builtin_shufflevector(a, b, 0, 1, 2, 3, 4, 5, 6, 7);
}
DI float dot_ones(s8 f, float acc) {
  u32x4 u = __builtin_bit_cast(u32x4, f);
#pragma unroll
  for (int i = 0; i < 4; ++i) { const unsigned t = u[i]; acc = __builtin_amdgcn_fdot2_f32_bf16(__builtin_bit_cast(b2, t), __builtin_bit_cast(b2, 0x3f803f80u), acc, false); }
  asm volatile("" : "+v"(acc));
  return acc;
}
DI unsigned pk2(float lo, float hi) { b2 c; c[0] = (__bf16)lo; c[1] = (__bf16)hi; return __builtin_bit_cast(unsigned, c); }
DI float bf_lo(unsigned u) { return __uint_as_float(u << 16); }
DI float bf_hi(unsigned u) { return __uint_as_float(u & 0xffff0000u); }
DI float swap_add(float v) { unsigned u = __float_as_uint(v); auto r = __builtin_amdgcn_permlane32_swap(u, u, false, false); return __uint_as_float(r[0]) + __uint_as_float(r[1]); }
DI float swap_max(float v) { unsigned u = __float_as_uint(v); auto r = __builtin_amdgcn_permlane32_swap(u, u, false, false); return fmaxf(__uint_as_float(r[0]), __uint_as_float(r[1])); }
DI float swap_min(float v) { unsigned u = __float_as_uint(v); auto r = __builtin_amdgcn_permlane32_swap(u, u, false, false); return fminf(__uint_as_float(r[0]), __uint_as_float(r[1])); }
DI float dpp_add(float v, const int ctrl_sel) {
  const int u = __float_as_int(v); int r;
  if (ctrl_sel == 0) r = __builtin_amdgcn_update_dpp(0, u, 0xB1, 0xf, 0xf, true);
  else if (ctrl_sel == 1) r = __builtin_amdgcn_update_dpp(0, u, 0x4E, 0xf, 0xf, true);
  else if (ctrl_sel == 2) r = __builtin_amdgcn_update_dpp(0, u, 0x141, 0xf, 0xf, true);
  else r = __builtin_amdgcn_update_dpp(0, u, 0x140, 0xf, 0xf, true);
  return v + __int_as_float(r);
}
DI float wave_sum(float v) {
  v = dpp_add(v, 0); v = dpp_add(v, 1); v = dpp_add(v, 2); v = dpp_add(v, 3);
  { unsigned u = __float_as_uint(v); auto r = __builtin_amdgcn_permlane16_swap(u, u, false, false); v = __uint_as_float(r[0]) + __uint_as_float(r[1]); }
  return swap_add(v);
}
DI float sum8(s8 f, float acc) {
  const u32x4 u = __builtin_bit_cast(u32x4, f);
#pragma unroll
  for (int i = 0; i < 4; ++i) { const unsigned t = u[i]; acc += __uint_as_float(t << 16); acc += __uint_as_float(t & 0xffff0000u); }
  asm volatile("" : "+v"(acc));
  return acc;
}
DI s8 pack_step(const f32x16& x, int s) {
  return __builtin_bit_cast(s8, (u32x4){pk2(x[8 * s], x[8 * s + 1]), pk2(x[8 * s + 2], x[8 * s + 3]), pk2(x[8 * s + 4], x[8 * s + 5]), pk2(x[8 * s + 6], x[8 * s + 7])});
}
template <int I> struct IC { static constexpr int v = I; };
template <int B, int E, class Fn> DI void static_for(Fn&& f) { if constexpr (B < E) { f(IC<B>{}); static_for<B + 1, E>(f); } }
DI bool flags_ok(u32x2 v, u32x2 tag, bool act) { return __all(!act || (v[0] == tag[0] && v[1] == tag[1])); }
DI void wait_flags(rsrc_t rws, unsigned base, unsigned stride, int n, u32x2 tag, int l) {

  const bool act = l < n; const unsigned off = act ? base + l * stride : OOB;
  for (int it = 0; it < (1 << 17); ++it) {
    u32x2 v; asm volatile("buffer_load_dwordx2 %0, %1, %2, 0 offen sc1\n\ts_waitcnt vmcnt(0)" : "=&v"(v) : "v"(off), "s"(rws) : "memory");
    if (flags_ok(v, tag, act)) return;
    __builtin_amdgcn_s_sleep(1);
  }
}
DI void wait_flags256(rsrc_t rws, unsigned base, u32x2 tag, int l) {
  for (int it = 0; it < (1 << 17); ++it) {
    u32x4 a, b;
    asm volatile("buffer_load_dwordx4 %0, %2, %3, 0 offen sc1\n\tbuffer_load_dwordx4 %1, %2, %3, 0 offen offset:16 sc1\n\ts_waitcnt vmcnt(0)" : "=&v"(a), "=&v"(b) : "v"(base + l * 32), "s"(rws) : "memory");
    const bool ok = a[0] == tag[0] && a[1] == tag[1] && a[2] == tag[0] && a[3] == tag[1] && b[0] == tag[0] && b[1] == tag[1] && b[2] == tag[0] && b[3] == tag[1];
    if (__all(ok)) return;
    __builtin_amdgcn_s_sleep(1);
  }
}
DI bool flags256_ok(u32x4 a, u32x4 b, u32x2 tag) {
  return __all(a[0] == tag[0] && a[1] == tag[1] && a[2] == tag[0] && a[3] == tag[1] && b[0] == tag[0] && b[1] == tag[1] && b[2] == tag[0] && b[3] == tag[1]);
}
DI float max3(float a, float b, float c) { float r; asm("v_max3_f32 %0, %1, %2, %3" : "=v"(r) : "v"(a), "v"(b), "v"(c)); return r; }
DI float min3(float a, float b, float c) { float r; asm("v_min3_f32 %0, %1, %2, %3" : "=v"(r) : "v"(a), "v"(b), "v"(c)); return r; }
DI f32x2 exp2_2(f32x2 a) { f32x2 r; r[0] = __builtin_amdgcn_exp2f(a[0]); r[1] = __builtin_amdgcn_exp2f(a[1]); return r; }
DI unsigned pk2v(f32x2 a) { return pk2(a[0], a[1]); }
DI void phi_map(const f32x16& p, float sc, unsigned (&o)[10]) {
  float mx = max3(p[0], p[1], p[2]), mn = min3(p[0], p[1], p[2]);
#pragma unroll
  for (int r = 3; r < 9; r += 2) { mx = max3(mx, p[r], p[r + 1]); mn = min3(mn, p[r], p[r + 1]); }
  mx = max3(mx, p[9], p[9]); mn = min3(mn, p[9], p[9]);
  { unsigned u = __float_as_uint(mx); auto r = __builtin_amdgcn_permlane32_swap(u, u, false, false); mx = max3(__uint_as_float(r[0]), __uint_as_float(r[1]), __uint_as_float(r[1])); }
  { unsigned u = __float_as_uint(mn); auto r = __builtin_amdgcn_permlane32_swap(u, u, false, false); mn = min3(__uint_as_float(r[0]), __uint_as_float(r[1]), __uint_as_float(r[1])); }
  const f32x2 s2 = {sc, sc}, bp = {-mx * sc, -mx * sc}, bn = {mn * sc, mn * sc};
  f32x2 ep[5], en[5];
#pragma unroll
  for (int r = 0; r < 5; ++r) { const f32x2 v = {p[2 * r], p[2 * r + 1]}; ep[r] = exp2_2(__builtin_elementwise_fma(v, s2, bp)); en[r] = exp2_2(__builtin_elementwise_fma(v, -s2, bn)); }
  const f32x2 sp2 = (ep[0] + ep[1]) + (ep[2] + ep[3]) + ep[4], sn2 = (en[0] + en[1]) + (en[2] + en[3]) + en[4];
  const float rp = __builtin_amdgcn_rcpf(swap_add(sp2[0] + sp2[1])), rn = __builtin_amdgcn_rcpf(swap_add(sn2[0] + sn2[1]));
  const f32x2 rp2 = {rp, rp}, rn2 = {rn, rn};
#pragma unroll
  for (int r = 0; r < 5; ++r) { o[r] = pk2v(ep[r] * rp2); o[5 + r] = pk2v(en[r] * rn2); }
}

#define STAMP(i)
#define STAMPL(k)
#define STAMPC(i)
#define STAMPF(i)

__global__ __launch_bounds__(512, 2) void fused_k(const u16* __restrict__ x, const u16* __restrict__ wqkv, const u16* __restrict__ wphi, const u16* __restrict__ wo,
                                                   const u16* __restrict__ wg, const u16* __restrict__ wu, const u16* __restrict__ wd, u16* __restrict__ out, char* __restrict__ ws) {
  extern __shared__ __attribute__((aligned(256))) char smem[];
  const int tid = threadIdx.x; const int l = tid & 63; const int w = __builtin_amdgcn_readfirstlane(tid >> 6);
  const int tg = w & 3, ch = w >> 2; const int l31 = l & 31, h = l >> 5;
  const int b = blockIdx.x, xcd = b & 7, idx = b >> 3;
  const int rb = (xcd & 1) * 8 + (idx & 7), cg = (xcd >> 1) * 4 + (idx >> 3);
  const int r2 = (xcd & 3) * 8 + (idx & 7), nb = (xcd >> 2) * 4 + (idx >> 3);
  const int klo = (5 * rb) >> 1, kcnt = ((5 * (rb + 1)) >> 1) - klo, rot = klo >> 2, kloc = klo - 4 * rot;
  STAMP(0);
  STAMPL(0);
  const rsrc_t rx = mk_rsrc(x, S * D * 2);
  const rsrc_t rw = mk_rsrc(w < 4 ? wg : wu, D * F * 2);
  const rsrc_t rws = mk_rsrc(ws, WS_USED);
  const u64 M_ALL = ~0ull, M_ONE = 1ull;
  auto sig = [&](int s) -> unsigned { const int t = s + rot; return t >= 10 ? t - 10 : t; };
  unsigned vx[2], vw[3];
#pragma unroll
  for (int i = 0; i < 2; ++i) { const int r = 8 * (2 * w + i) + (l >> 3); const int c = (l & 7) ^ ((r >> 1) & 7); vx[i] = (128 * rb + r) * 1280 + c * 16; }
#pragma unroll
  for (int i = 0; i < 3; ++i) { const int p = 64 * (3 * (w & 3) + i) + l; const int k = p / 12, c = p % 12; vw[i] = k * 3072 + cg * 192 + c * 16; }
  const unsigned lx0 = L_X + 2 * w * 1024;
  const unsigned lw0 = L_W + (w < 4 ? 0 : W_UOFF) + 3 * (w & 3) * 1024;
  auto dmax = [&](int s, int i) { dma16(lx0 + (s % 3) * 16384 + i * 1024, vx[i], rx, sig(s) * 128); };
  auto dmaw = [&](int s, int i) { dma16(lw0 + (s & 3) * W_STAGE + i * 1024, vw[i], rw, sig(s) * (64 * 3072)); };
  auto issue1 = [&](int s) { dmax(s, 0); dmax(s, 1); dmaw(s, 0); dmaw(s, 1); dmaw(s, 2); };
  issue1(0);
  u32x2 ep = ld_b64_sc1_m(rws, WS_EPOCH, M_ONE);
  const int srow = 128 * rb + 8 * cg + w;
  const bool v3 = kcnt == 3 && w < 4;
  u32x4 sx0, sx1;
  {
    const rsrc_t rq = mk_rsrc(wqkv, D * 1920 * 2);
    const int p = 64 * (w & 3) + l; const int k = p / 15, c = p % 15;
    const unsigned vo = p < 240 ? (unsigned)(k * 3840 + (c / 5) * 1280 + cg * 80 + (c % 5) * 16) : OOB;
    dma16(L_QRAW + w * 1024, vo, rq, (16 * (klo + (w >> 2))) * 3840);
    dma16_m(v3 ? L_QRAW + 8192 + w * 1024 : L_DUMMY1, v3 ? vo : OOB, rq, v3 ? (16 * (klo + 2)) * 3840 : 0, v3 ? M_ALL : M_ONE);
    const rsrc_t rp = mk_rsrc(wphi, 32 * 400 * 2);
    dma16_m(w < 2 ? L_WPR + w * 1024 : L_DUMMY1, (w < 2 && w * 64 + l < 100) ? (unsigned)(cg * 1600 + (w * 64 + l) * 16) : OOB, rp, 0, w == 0 ? M_ALL : (w == 1 ? 0xfffffffffull : M_ONE));
    sx0 = ld_b128(rx, srow * 1280 + l * 16, 0);
    sx1 = ld_b128(rx, l < 16 ? (unsigned)(srow * 1280 + 1024 + l * 16) : OOB, 0);
    const int kst = b / 5, cb = b % 5; const bool isd = kst < 48;
    const rsrc_t rr = mk_rsrc(isd ? wd : wo, isd ? F * D * 2 : D * D * 2);
    dma16(L_W2A + w * 1024, (unsigned)((l >> 4) * 1280 + (l & 15) * 16), rr, ((isd ? 32 * kst : 32 * (kst - 48)) + 4 * w) * 1280 + cb * 256);
  }
  const int gi = l >> 4, q4 = (l >> 2) & 3, p4 = l & 3;
  WAITV_BAR(0);
  STAMP(1);
  STAMPL(1);
  issue1(1); issue1(2);
  TIE(ep);
  u32x2 tag;
  { const unsigned lo = __builtin_amdgcn_readfirstlane(ep[0]), hi = __builtin_amdgcn_readfirstlane(ep[1]); tag[0] = lo + 1u; tag[1] = hi + (lo == 0xffffffffu ? 1u : 0u); }
  const int shc = (w >> 1) & 1, sht = w & 1;
  s8 sf0, sf1, sf2; unsigned se[8]; unsigned smA, smB;
  {
    const int Q = 4 * (gi & 1) + p4;
    int seg, qd;
    if (sht == 0) { seg = Q < 5 ? 0 : 1; qd = Q < 5 ? Q : Q - 5; } else { seg = Q < 2 ? 1 : 2; qd = Q < 2 ? 3 + Q : (Q < 7 ? Q - 2 : 4); }
    const unsigned lo = L_QRAW + (8 * h + q4) * 240 + seg * 80 + shc * 40 + qd * 8;
    sf0 = tr_frag(lo + (w >> 2) * 4096, 960); sf1 = tr_frag(lo + 8192, 960);
    sf2 = tr_frag(L_W2A + (8 * gi + q4) * 256 + w * 32 + p4 * 8, 1024);
    const int reg = (l31 & 3) + 4 * (l31 >> 3), hh = (l31 >> 2) & 1;
    const int d0 = (tg == 0 ? 0 : tg == 1 ? 16 : tg == 2 ? -4 : 12) + 4 * h;
    const unsigned wa = L_WPR + ch * 800 + (10 * hh + reg) * 2 + d0 * 40;
#pragma unroll
    for (int j = 0; j < 8; ++j) se[j] = *(LAS u16*)(size_t)(wa + (8 * (j >> 2) + (j & 3)) * 40);
    smA = (reg < 10 && (unsigned)d0 < 20u) ? ~0u : 0u; smB = (reg < 10 && (unsigned)(d0 + 8) < 20u) ? ~0u : 0u;
  }
  auto side = [&](int part) {
    if (part == 0) {
      TIE(sx0); TIE(sx1);
      float sm = 0.f, sq = 0.f;
#pragma unroll
      for (int i = 0; i < 4; ++i) { const unsigned u0 = sx0[i], u1 = sx1[i];
        sm = __builtin_amdgcn_fdot2_f32_bf16(__builtin_bit_cast(b2, u0), __builtin_bit_cast(b2, 0x3f803f80u), sm, false); sq = __builtin_amdgcn_fdot2_f32_bf16(__builtin_bit_cast(b2, u0), __builtin_bit_cast(b2, u0), sq, false);
        sm = __builtin_amdgcn_fdot2_f32_bf16(__builtin_bit_cast(b2, u1), __builtin_bit_cast(b2, 0x3f803f80u), sm, false); sq = __builtin_amdgcn_fdot2_f32_bf16(__builtin_bit_cast(b2, u1), __builtin_bit_cast(b2, u1), sq, false); }
      const float m = wave_sum(sm) * (1.f / D);
      const float var = fmaxf(wave_sum(sq) * (1.f / D) - m * m, 0.f);
      st_b64_sc1_m((u32x2){__float_as_uint(m), __float_as_uint(__builtin_amdgcn_rsqf(var + 1e-5f))}, rws, WS_STATS + srow * 8, M_ONE);
    } else if (part == 1) {
      st_b128_sc1(__builtin_bit_cast(u32x4, sf0), rws, WS_WQP + (((2 * cg + shc) * 40 + klo + (w >> 2)) * 2 + sht) * 1024 + l * 16);
      st_b128_sc1_m(__builtin_bit_cast(u32x4, sf1), rws, v3 ? WS_WQP + (((2 * cg + shc) * 40 + klo + 2) * 2 + sht) * 1024 + l * 16 : OOB, v3 ? M_ALL : M_ONE);
      const int kst = b / 5, cb = b % 5, ct = 8 * cb + w;
      st_b128_sc1(__builtin_bit_cast(u32x4, sf2), rws, WS_W2P + (((ct / 5) * 68 + kst) * 5 + ct % 5) * 1024 + l * 16);
      const float c0 = swap_add(dot_ones(sf0, 0.f)), c1 = swap_add(dot_ones(sf1, 0.f));
      if (h == 0) { const unsigned qa = L_W2S + ((shc * 2 + sht) * 32 + l31) * 16 + (w >> 2) * 4; *(LAS float*)(size_t)qa = c0; *(LAS float*)(size_t)(qa + 8) = v3 ? c1 : 0.f; }
    } else {
      *(LAS u32x4*)(size_t)(L_WPF + w * 1024 + l * 16) = (u32x4){(se[0] | (se[1] << 16)) & smA, (se[2] | (se[3] << 16)) & smA, (se[4] | (se[5] << 16)) & smB, (se[6] | (se[7] << 16)) & smB};
    }
  };
  STAMPF(8);
  const unsigned qoff = (p4 == 1) ? 8 : (p4 == 2) ? 4 : (p4 == 3) ? 12 : 0;
  const unsigned wl = L_W + ((gi & 1) ? W_UOFF : 0) + (8 * h + q4) * 192 + 2 * qoff + 96 * ch;
  const unsigned wl2 = wl + 2 * W_STAGE;
  const int xrow = 32 * tg + l31;
  unsigned xa[4];
#pragma unroll
  for (int j = 0; j < 4; ++j) { xa[j] = (L_X + xrow * 128 + ((h ^ ((xrow >> 1) & 7)) << 4)) ^ (j * 32); asm volatile("" : "+v"(xa[j])); }
  s8 XF[40];
  f32x16 acc[3]; float cs[3];
#pragma unroll
  for (int i = 0; i < 3; ++i) { cs[i] = 0.f;
#pragma unroll
    for (int r = 0; r < 16; ++r) acc[i][r] = 0.f; }
  s8 wf[2][3];
  auto rdx = [&](int s, int j) { XF[4 * s + j] = lds_b128(xa[j] + (s % 3) * 16384); };
  auto rdw = [&](int n, int i) { const int s = n >> 2, j = n & 3; wf[n & 1][i] = tr_frag(((s & 2) ? wl2 : wl) + (s & 1) * W_STAGE + j * 3072 + i * 32, 768); };
  const unsigned vq2 = WS_WQP + ((2 * cg + ch) * 40 + tg) * 2048 + l * 16;
  auto slot2 = [&](int q) -> unsigned { return q == 0 || q == 7 ? 16384u : q == 1 || q == 8 ? L_W + 2 * W_STAGE : q == 2 || q == 9 ? 32768u : q == 3 ? L_W + 3 * W_STAGE : q == 4 ? 0u : q == 5 ? L_W : L_W + W_STAGE; };
  auto issue2 = [&](int q) { const unsigned so = sig(q) * 8192; dma16_sc1(slot2(q) + w * 2048, vq2, rws, so); dma16_sc1(slot2(q) + w * 2048 + 1024, vq2, rws, so + 1024); };
  const bool hv = b < 168 && w < 4; const int hu = 256 + (b >> 1), hkst = hu / 5, hcb = hu % 5;
  u32x2 pf, pf2, st; u32x4 pl0, pl1, cg4[4];
  rdx(0, 0); rdw(0, 0); rdw(0, 1); rdw(0, 2); rdx(0, 1); rdx(0, 2); rdx(0, 3);
  static_for<0, 40>([&](auto N_) {
    constexpr int n = decltype(N_)::v;
    constexpr int s = n >> 2, j = n & 3;
    if constexpr (n == 3) STAMPF(9);
    if constexpr (n == 11) STAMPF(10);
    if constexpr (n == 19) STAMPF(11);
    if constexpr (n == 27) STAMPF(12);
    if constexpr (n == 35) STAMPF(13);
    if constexpr (j == 3 && s < 9) STAMPL(2 + 2 * s);
    if constexpr (n == 3) WAITV_BAR(9);
    if constexpr (n == 7) WAITV_BAR(9);
    if constexpr (n == 8) {
#pragma unroll
      for (int i = 0; i < 3; ++i) { const float t = swap_add(cs[i]); if (h == 0) *(LAS float*)(size_t)(L_CS + ((ch * 3 + i) * 32 + l31) * 16 + tg * 4) = t; }
    }
    if constexpr (n == 11) WAITVL_BAR(5);
    if constexpr (n == 15) WAITV_BAR(7);
    if constexpr (n == 19) WAITV_BAR(8);
    if constexpr (n == 23) WAITV_BAR(5);
    if constexpr (n == 27) { WAITV_BAR(7); pf = ld_b64_sc1_m(rws, WS_FP0 + cg * 128 + l * 8, 0xffffull); pf2 = ld_b64_sc1_m(rws, WS_FP0 + rb * 8 + (l & 15) * 128, 0xffffull); }
    if constexpr (n == 31) {
      WAITV(5); TIE(pf); TIE(pf2);
      if (!flags_ok(pf, tag, l < 16)) wait_flags(rws, WS_FP0 + cg * 128, 8, 16, tag, l);
      if (!flags_ok(pf2, tag, l < 16)) wait_flags(rws, WS_FP0 + rb * 8, 128, 16, tag, l);
      asm volatile("s_barrier" ::: "memory");
      st_b64_sc1_m(tag, rws, w == 0 ? WS_FP1 + b * 8 : OOB, M_ONE);
      st = ld_b64_sc1(rws, WS_STATS + (128 * rb + xrow) * 8, 0);
      pf = ld_b64_sc1_m(rws, WS_FCS + cg * 128 + l * 8, 0xffffull);
      issue2(0); issue2(1);
    }
    if constexpr (n == 35) {
      WAITV(4); TIE(pf);
      if (!flags_ok(pf, tag, l < 16)) wait_flags(rws, WS_FCS + cg * 128, 8, 16, tag, l);
      asm volatile("s_barrier" ::: "memory");
      { const int c48 = tid % 48, g = tid / 48; const bool a = tid < 192;
        const unsigned vo = a ? WS_CSP + (cg * 16 + 4 * g) * 1280 + ((c48 / 24) * 160 + ((c48 % 24) >> 3) * 32 + (c48 & 7) * 4) * 4 : OOB;
#pragma unroll
        for (int r = 0; r < 4; ++r) cg4[r] = ld_b128_sc1_m(rws, vo, r * 1280, w < 3 ? M_ALL : M_ONE); }
      issue2(2); issue2(3);
    }
    if constexpr (j == 3 && s < 9) STAMPL(3 + 2 * s);
    SB();
#pragma unroll
    for (int i = 0; i < 3; ++i) {
      acc[i] = MFMA32(wf[n & 1][i], XF[n], acc[i]);
      SB();
      if (i == 0 && j == 3 && s < 9) rdx(s + 1, 0);
      if (n + 1 < 40) rdw(n + 1, i);
      if (j == 0 && s > 0) rdx(s, i + 1);
      if (n < 3 && i == 2) side(n);
      if (n < 8 && i == 2) { if ((unsigned)(n - kloc) < (unsigned)kcnt && tg == (n & 3)) { cs[0] = dot_ones(wf[n & 1][0], cs[0]); cs[1] = dot_ones(wf[n & 1][1], cs[1]); cs[2] = dot_ones(wf[n & 1][2], cs[2]); } }
      if (j == 3 && s + 3 < 10) {
        if (i == 0) { dmax(s + 3, 0); dmax(s + 3, 1); }
        if (i == 1) { dmaw(s + 3, 0); dmaw(s + 3, 1); }
        if (i == 2) dmaw(s + 3, 2);
      }
      if constexpr (n == 11) { if (i == 2) {
        st_b64_sc1_m(tag, rws, w == 0 ? WS_FP0 + (cg * 16 + rb) * 8 : OOB, M_ONE);
        u32x4 o = {0u, 0u, 0u, 0u};
        if (tid < 80) {
          const int c2 = tid / 40, t5 = (tid % 40) >> 3, n0 = (tid & 7) * 4;
          const unsigned ad = t5 < 3 ? L_CS + ((c2 * 3 + t5) * 32 + n0) * 16 : L_W2S + ((c2 * 2 + t5 - 3) * 32 + n0) * 16;
#pragma unroll
          for (int e = 0; e < 4; ++e) { const f32x4 pp = *(LAS f32x4*)(size_t)(ad + e * 16); o[e] = __float_as_uint((pp[0] + pp[1]) + (pp[2] + pp[3])); }
        }
        st_b128_sc1_m(o, rws, tid < 80 ? WS_CSP + (cg * 16 + rb) * 1280 + tid * 16 : OOB, w == 0 ? M_ALL : (w == 1 ? 0xffffull : M_ONE));
      } }
      if constexpr (n == 15) { if (i == 2) {
        const rsrc_t rr = mk_rsrc(wo, D * D * 2);
        dma16_m(hv ? L_W2S + w * 1024 : L_DUMMY, hv ? (unsigned)((l >> 3) * 1280 + (l & 7) * 16) : OOB, rr, hv ? (32 * (hkst - 48) + 8 * w) * 1280 + hcb * 256 + (b & 1) * 128 : 0, hv ? M_ALL : M_ONE);
      } }
      if constexpr (n == 23) { if (i == 2) {
        st_b64_sc1_m(tag, rws, w == 0 ? WS_FCS + (cg * 16 + rb) * 8 : OOB, M_ONE);
        const int ct = 8 * hcb + 4 * (b & 1) + (w & 3);
        const s8 f = tr_frag(L_W2S + (8 * gi + q4) * 128 + (w & 3) * 32 + p4 * 8, 512);
        st_b128_sc1_m(__builtin_bit_cast(u32x4, f), rws, hv ? WS_W2P + (((ct / 5) * 68 + hkst) * 5 + ct % 5) * 1024 + l * 16 : OOB, hv ? M_ALL : M_ONE);
      } }
      SB();
    }
  });
  STAMP(2);
  STAMPL(20);
  WAITV(4);
#pragma unroll
  for (int r = 0; r < 4; ++r) TIE(cg4[r]);
  if (tid < 192) { const int c48 = tid % 48, g = tid / 48; const unsigned ad = L_CS + (((c48 / 24) * 3 + ((c48 % 24) >> 3)) * 32 + (c48 & 7) * 4) * 16 + g * 4;
#pragma unroll
    for (int e = 0; e < 4; ++e) *(LAS float*)(size_t)(ad + e * 16) = (__uint_as_float(cg4[0][e]) + __uint_as_float(cg4[1][e])) + (__uint_as_float(cg4[2][e]) + __uint_as_float(cg4[3][e])); }
  LBAR();
  TIE(st);
  const float mean = __uint_as_float(st[0]), rstd = __uint_as_float(st[1]);
  s8 bm;
  { const float nm = -mean; const unsigned hi = pk2(nm, 0.f) & 0xffffu; const float lo = nm - __uint_as_float(hi << 16); unsigned mpk = hi | (pk2(lo, 0.f) << 16); if (h) mpk = 0; bm = __builtin_bit_cast(s8, (u32x4){mpk, mpk, 0u, 0u}); }
  auto cs_frag = [&](int tile) -> s8 {
    const f32x4 pp = *(LAS f32x4*)(size_t)(L_CS + ((ch * 3 + tile) * 32 + l31) * 16);
    const float c = (pp[0] + pp[1]) + (pp[2] + pp[3]);
    unsigned hi = pk2(c, c); const float lo = c - __uint_as_float(hi << 16); unsigned lo2 = pk2(lo, lo);
    if (h) { hi = 0; lo2 = 0; }
    return __builtin_bit_cast(s8, (u32x4){hi, lo2, 0u, 0u});
  };
  const unsigned tok = 128 * rb + xrow;
  const unsigned actbase = WS_ACT + (tok >> 4) * 68 * 1024 + (l & 15) * 16;
  {
    const float gs = -1.4426950408889634f * rstd, irs = __builtin_amdgcn_rcpf(rstd);
#pragma unroll
    for (int i = 0; i < 3; ++i) acc[i] = MFMA32(cs_frag(i), bm, acc[i]);
#pragma unroll
    for (int i = 0; i < 3; ++i) {
      issue2(4 + i);
      const f32x2 gs2 = {gs, gs}, irs2 = {irs, irs};
      u32x4 ho;
#pragma unroll
      for (int r = 0; r < 4; ++r) {
        const f32x2 g2 = {acc[i][2 * r], acc[i][2 * r + 1]}, u2 = {acc[i][8 + 2 * r], acc[i][9 + 2 * r]};
        const f32x2 dn = __builtin_elementwise_fma(exp2_2(g2 * gs2), irs2, irs2);
        const f32x2 rc = {__builtin_amdgcn_rcpf(dn[0]), __builtin_amdgcn_rcpf(dn[1])};
        ho[r] = pk2v(u2 * rc);
      }
      const unsigned G = 12 * cg + 6 * ch + 2 * i + h;
      st_b128_sc1(ho, rws, actbase + (G >> 2) * 1024 + (G & 3) * 256);
      SB();
    }
  }
  STAMP(3);
  STAMPL(21);
  f32x16 T[2];
#pragma unroll
  for (int i = 0; i < 2; ++i)
#pragma unroll
    for (int r = 0; r < 16; ++r) T[i][r] = 0.f;
  s8 qf[3][2];
  const unsigned ql = ch * 8192 + l * 16, ql2 = ql + L_W;
  auto rdq = [&](int m, int t) { const int q = m >> 2, j = m & 3; const unsigned sl = slot2(q); qf[m % 3][t] = lds_b128((sl >= L_W ? ql2 + (sl - L_W) : ql + sl) + j * 2048 + t * 1024); };
  rdq(0, 0); rdq(0, 1); rdq(1, 0); rdq(1, 1);
  static_for<0, 40>([&](auto M_) {
    constexpr int m = decltype(M_)::v;
    if constexpr (m == 6) STAMPL(22);
    if constexpr (m == 14) STAMPL(23);
    if constexpr (m == 15) STAMPL(24);
    if constexpr (m == 26) STAMPL(25);
    if constexpr (m == 34) STAMPL(26);
    if constexpr (m == 6) { WAITV_BAR(11); issue2(7); }
    if constexpr (m == 10) { WAITV_BAR(11); issue2(8); }
    if constexpr (m == 14) { WAITV_BAR(4); st_b64_sc1_m(tag, rws, w == 0 ? WS_FH + (rb * 16 + cg) * 8 : OOB, M_ONE); issue2(9); }
    if constexpr (m == 18) {
      const int c32 = tid & 31, g = (tid >> 5) & 3; const bool a = tid < 128;
      const unsigned vo = a ? WS_CSP + (cg * 16 + 4 * g) * 1280 + ((c32 >> 4) * 160 + (3 + ((c32 >> 3) & 1)) * 32 + (c32 & 7) * 4) * 4 : OOB;
#pragma unroll
      for (int r = 0; r < 4; ++r) cg4[r] = ld_b128_sc1_m(rws, vo, r * 1280, w < 2 ? M_ALL : M_ONE);
    }
    if constexpr (m == 26) WAITV_BAR(9);
    if constexpr (m == 30) {
      WAITV_BAR(6);
      pl0 = ld_b128_sc1_m(rws, WS_FP1 + l * 32, 0, w == 0 ? M_ALL : M_ONE); pl1 = ld_b128_sc1_m(rws, WS_FP1 + l * 32 + 16, 0, w == 0 ? M_ALL : M_ONE);
      pf = ld_b64_sc1_m(rws, WS_FH + (r2 >> 1) * 128 + l * 8, 0xffffull);
    }
    if constexpr (m == 34) {
      WAITV_BAR(3);
#pragma unroll
      for (int r = 0; r < 4; ++r) TIE(cg4[r]);
      if (tid < 128) { const int c32 = tid & 31, g = tid >> 5; const unsigned ad = L_CS + (((c32 >> 4) * 3 + ((c32 >> 3) & 1)) * 32 + (c32 & 7) * 4) * 16 + g * 4;
#pragma unroll
        for (int e = 0; e < 4; ++e) *(LAS float*)(size_t)(ad + e * 16) = (__uint_as_float(cg4[0][e]) + __uint_as_float(cg4[1][e])) + (__uint_as_float(cg4[2][e]) + __uint_as_float(cg4[3][e])); }
    }
    SB();
#pragma unroll
    for (int t = 0; t < 2; ++t) {
      T[t] = MFMA32(qf[m % 3][t], XF[m], T[t]);
      SB();
      if (m + 2 < 40) rdq(m + 2, t);
      SB();
    }
  });
  STAMP(4);
  STAMPL(27);
  WAITV(0); TIE(pl0); TIE(pl1); TIE(pf);
  if (w == 0) { if (!flags256_ok(pl0, pl1, tag)) wait_flags256(rws, WS_FP1, tag, l); }
  if (!flags_ok(pf, tag, l < 16)) wait_flags(rws, WS_FH + (r2 >> 1) * 128, 8, 16, tag, l);
  LBAR();
  STAMPL(28);
  const unsigned vb = WS_W2P + nb * 68 * 5 * 1024 + l * 16;
  const unsigned bf0 = w < 4 ? 3 * w : 12 + 2 * (w - 4);
  auto issueB = [&](int t) {
    const unsigned base = L_BRING + (t % 6) * B_STAGE;
    dma16_sc1(base + bf0 * 1024, vb, rws, t * B_STAGE + bf0 * 1024);
    dma16_sc1(base + (bf0 + 1) * 1024, vb, rws, t * B_STAGE + (bf0 + 1) * 1024);
    asm volatile("s_mov_b32 m0, %0\n\ts_mov_b64 exec, %4\n\tbuffer_load_dwordx4 %1, %2, %3 offen sc1 lds\n\ts_mov_b64 exec, -1" ::"s"(w < 4 ? base + (bf0 + 2) * 1024 : L_DUMMY), "v"(w < 4 ? vb : OOB), "s"(rws), "s"(t * B_STAGE + (bf0 + 2) * 1024), "s"(w < 4 ? M_ALL : M_ONE) : "memory");
  };
  f32x16 P;
  const float psc = 1.4426950408889634f * rstd;
  {
    issueB(0);
    T[0] = MFMA32(cs_frag(0), bm, T[0]);
    T[1] = MFMA32(cs_frag(1), bm, T[1]);
    const unsigned wpl = L_WPF + ch * 4096 + l * 16;
    const s8 A0 = lds_b128(wpl), A1 = lds_b128(wpl + 1024), A2 = lds_b128(wpl + 2048), A3 = lds_b128(wpl + 3072);
    s8 E0, E1;
    { const int k0 = l31 - 8 * h; const unsigned val = (k0 & 1) ? 0x3f800000u : 0x3f80u; u32x4 u0, u1;
#pragma unroll
      for (int d2 = 0; d2 < 4; ++d2) { u0[d2] = (k0 >> 1) == d2 ? val : 0u; u1[d2] = ((k0 - 16) >> 1) == d2 ? val : 0u; }
      E0 = __builtin_bit_cast(s8, u0); E1 = __builtin_bit_cast(s8, u1); }
    issueB(1);
    const s8 t01 = pack_step(T[0], 1), t10u = pack_step(T[1], 0), t00 = pack_step(T[0], 0);
    f32x16 z16;
#pragma unroll
    for (int r = 0; r < 16; ++r) z16[r] = 0.f;
    f32x16 Pk = MFMA32(A2, t01, z16); Pk = MFMA32(A3, t10u, Pk);
    P = MFMA32(A0, t00, z16); P = MFMA32(A1, t01, P);
#pragma unroll
    for (int r = 0; r < 8; ++r) { const f32x2 v = (f32x2){T[1][2 * r], T[1][2 * r + 1]} * (f32x2){rstd, rstd}; T[1][2 * r] = v[0]; T[1][2 * r + 1] = v[1]; }
    const s8 t10 = pack_step(T[1], 0);
    s8 t11 = pack_step(T[1], 1);
    { u32x4 u = __builtin_bit_cast(u32x4, t11); if (h) u[2] = (u[2] & 0xffff0000u) | 0x3f80u; t11 = __builtin_bit_cast(s8, u); }
    f32x16 V = MFMA32(t10, E0, z16); V = MFMA32(t11, E1, V);
    issueB(2);
    unsigned phk[10]; phi_map(Pk, psc, phk);
    issueB(3);
    const s8 v0 = pack_step(V, 0), v1 = pack_step(V, 1);
    const s8 F0 = __builtin_bit_cast(s8, (u32x4){phk[0], phk[1], phk[2], phk[3]}), F1 = __builtin_bit_cast(s8, (u32x4){phk[4], phk[5], phk[6], phk[7]}), F2 = __builtin_bit_cast(s8, (u32x4){phk[8], phk[9], 0u, 0u});
    f32x16 Z0 = MFMA32(F0, E0, z16); Z0 = MFMA32(F1, E1, Z0);
    f32x16 Z1 = MFMA32(F2, E0, z16);
    issueB(4);
    f32x16 KV0 = MFMA32(pack_step(Z0, 0), v0, z16); KV0 = MFMA32(pack_step(Z0, 1), v1, KV0);
    f32x16 KV1 = MFMA32(pack_step(Z1, 0), v0, z16);
    const int rho = 16 * (l31 >> 4) + 8 * ((l31 & 7) >> 2) + 4 * ((l31 >> 3) & 1) + (l31 & 3);
    const int d = rho == 28 ? 20 : rho - 8;
    KV1 = MFMA32(pack_step(Z1, 1), v1, KV1);
    if (d >= 0 && d <= 20) {
      const unsigned kb = L_KVR + w * 3360 + d * 160;
#pragma unroll
      for (int g = 0; g < 4; ++g) *(LAS f32x4*)(size_t)(kb + (8 * g + 4 * h) * 4) = (f32x4){KV0[4 * g], KV0[4 * g + 1], KV0[4 * g + 2], KV0[4 * g + 3]};
      if (h == 0) { *(LAS f32x4*)(size_t)(kb + 128) = (f32x4){KV1[0], KV1[1], KV1[2], KV1[3]}; *(LAS f32x4*)(size_t)(kb + 144) = (f32x4){KV1[4], KV1[5], KV1[6], KV1[7]}; }
    }
  }
  STAMPL(29);
  LBAR();
  {
    const int c = tid & 255, hc = tid >> 8;
    u32x4 o = {0u, 0u, 0u, 0u};
    if (c < 105) {
      float a[8];
#pragma unroll
      for (int e = 0; e < 8; ++e) a[e] = 0.f;
#pragma unroll
      for (int g = 0; g < 4; ++g) {
        const unsigned ad = L_KVR + (hc * 4 + g) * 3360 + c * 32;
        const f32x4 u0 = *(LAS f32x4*)(size_t)ad, u1 = *(LAS f32x4*)(size_t)(ad + 16);
#pragma unroll
        for (int e = 0; e < 4; ++e) { a[e] += u0[e]; a[4 + e] += u1[e]; }
      }
      o = (u32x4){pk2(a[0], a[1]), pk2(a[2], a[3]), pk2(a[4], a[5]), pk2(a[6], a[7])};
    }
    st_b128_sc1_m(o, rws, c < 105 ? WS_KVP + ((cg * 2 + hc) * 16 + rb) * KVP_STRIDE + c * 16 : OOB, tg == 0 ? M_ALL : (tg == 1 ? 0x1ffffffffffull : M_ONE));
  }
  issueB(5);
  STAMPL(30);
  const int kq = w & 3, rh = w >> 2;
  const rsrc_t rout = mk_rsrc(out, S * D * 2);
  const unsigned va = WS_ACT + (4 * r2 + 2 * rh) * 68 * 1024 + kq * 1024 + l * 16;
  u32x4 af[10][2];
  auto lda = [&](int t) { af[t % 10][0] = ld_b128_sc1(rws, va, t * 4096); af[t % 10][1] = ld_b128_sc1(rws, va + 68 * 1024, t * 4096); };
  lda(0); lda(1); lda(2); lda(3); lda(4); lda(5);
  unsigned phq[10]; phi_map(P, psc, phq);
  STAMPL(31);
  WAITV_BAR(15);
  st_b64_sc1_m(tag, rws, w == 0 ? WS_FKV + (cg * 16 + rb) * 8 : OOB, M_ONE);
  lda(6); lda(7); lda(8); lda(9);
  STAMP(5);
  STAMPL(32);
  f32x4 a2[2][5];
#pragma unroll
  for (int a = 0; a < 2; ++a)
#pragma unroll
    for (int c = 0; c < 5; ++c) a2[a][c] = (f32x4){0.f, 0.f, 0.f, 0.f};
  u32x4 kp[8]; u32x2 xr[3]; unsigned go[3]; s8 bfr[2][5];
  const int kc = (64 * tg + l) >> 1; const bool kact = kc < 105;
  static_for<0, 17>([&](auto T_) {
    constexpr int t = decltype(T_)::v;
    if constexpr (t == 4) STAMPF(16);
    if constexpr (t == 7) STAMPF(17);
    if constexpr (t == 11) STAMPF(18);
    if constexpr (t == 12) STAMPF(19);
    constexpr int allow_tab[17] = {19, 17, 20, 23, 3, 14, 6, 9, 11, 6, 10, 3, 11, 9, 7, 3, 3};
    STAMPL(33 + t);
    asm volatile("s_waitcnt vmcnt(%0) lgkmcnt(0)\n\ts_barrier" ::"i"(allow_tab[t]) : "memory");
    if constexpr (t == 3) pf = ld_b64_sc1_m(rws, WS_FKV + cg * 128 + l * 8, 0xffffull);
    if constexpr (t == 4) {
      TIE(pf);
      if (!flags_ok(pf, tag, l < 16)) wait_flags(rws, WS_FKV + cg * 128, 8, 16, tag, l);
#pragma unroll
      for (int r = 0; r < 8; ++r) kp[r] = ld_b128_sc1_m(rws, kact ? WS_KVP + ((cg * 2 + ch) * 16 + 8 * (l & 1)) * KVP_STRIDE + kc * 16 : OOB, r * KVP_STRIDE, tg < 3 ? M_ALL : 0x3ffffull);
    }
    if constexpr (t == 6) {
      f32x2 a2v[4];
#pragma unroll
      for (int e = 0; e < 4; ++e) a2v[e] = (f32x2){0.f, 0.f};
#pragma unroll
      for (int r = 0; r < 8; ++r) { TIE(kp[r]);
#pragma unroll
        for (int e = 0; e < 4; ++e) a2v[e] += (f32x2){bf_lo(kp[r][e]), bf_hi(kp[r][e])}; }
      float a[8];
#pragma unroll
      for (int e = 0; e < 8; ++e) a[e] = dpp_add(a2v[e >> 1][e & 1], 0);
      if (kact && (l & 1) == 0) { const int d = kc / 5, cc = kc % 5; *(LAS u32x4*)(size_t)(L_KVS + ch * 2016 + d * 96 + cc * 16) = (u32x4){pk2(a[0], a[1]), pk2(a[2], a[3]), pk2(a[4], a[5]), pk2(a[6], a[7])}; }
    }
    if constexpr (t == 7) {
      const int reg = (l31 & 3) + 4 * (l31 >> 3), hh = (l31 >> 2) & 1;
      int d;
      if (reg >= 12) d = 20; else if (ch == 0) d = reg < 8 ? 8 * hh + reg : (hh == 0 ? 8 + reg : 20); else d = reg < 8 ? (hh == 0 ? 4 + reg : 12 + reg) : (hh == 0 ? reg - 8 : 20);
      const unsigned ka = L_KVS + ch * 2016 + d * 96;
      const s8 A0 = lds_b128(ka + h * 16), A1 = lds_b128(ka + 32 + h * 16);
      const u32x2 a2l = *(LAS u32x2*)(size_t)(ka + 64 + h * 8);
      const s8 A2 = __builtin_bit_cast(s8, (u32x4){a2l[0], a2l[1], 0u, 0u});
      f32x16 O;
#pragma unroll
      for (int r = 0; r < 16; ++r) O[r] = 0.f;
      O = MFMA32(A0, __builtin_bit_cast(s8, (u32x4){phq[0], phq[1], phq[2], phq[3]}), O);
      O = MFMA32(A1, __builtin_bit_cast(s8, (u32x4){phq[4], phq[5], phq[6], phq[7]}), O);
      O = MFMA32(A2, __builtin_bit_cast(s8, (u32x4){phq[8], phq[9], 0u, 0u}), O);
      const float rc = __builtin_amdgcn_rcpf(O[12] + 1e-6f);
      const unsigned G0 = 192 + 5 * cg + (ch == 0 ? h : 3 + h);
      st_b128_sc1((u32x4){pk2(O[0] * rc, O[1] * rc), pk2(O[2] * rc, O[3] * rc), pk2(O[4] * rc, O[5] * rc), pk2(O[6] * rc, O[7] * rc)}, rws, actbase + (G0 >> 2) * 1024 + (G0 & 3) * 256);
      const unsigned G2 = 192 + 5 * cg + 2;
      st_b64_sc1_m((u32x2){pk2(O[8] * rc, O[9] * rc), pk2(O[10] * rc, O[11] * rc)}, rws, actbase + (G2 >> 2) * 1024 + (G2 & 3) * 256 + ch * 8, 0xffffffffull);
    }
    if constexpr (t == 9) st_b64_sc1_m(tag, rws, w == 0 ? WS_FO + (rb * 16 + cg) * 8 : OOB, M_ONE);
    if constexpr (t == 10) pf = ld_b64_sc1_m(rws, WS_FO + (r2 >> 1) * 128 + l * 8, 0xffffull);
    if constexpr (t == 11) { TIE(pf); if (!flags_ok(pf, tag, l < 16)) wait_flags(rws, WS_FO + (r2 >> 1) * 128, 8, 16, tag, l); lda(12); lda(13); lda(14); lda(15); lda(16); }
    if constexpr (t == 1) lda(10);
    if constexpr (t == 2) lda(11);
    if constexpr (t >= 1 && t + 5 < 17) issueB(t + 5);
    if constexpr (t == 14) {
#pragma unroll
      for (int z = 0; z < 3; ++z) { const int ix = kq + 4 * z; const bool v = ix < 10; const int a = ix >= 5 ? 1 : 0, c = ix - 5 * a;
        go[z] = v ? (unsigned)((64 * r2 + 32 * rh + 16 * a + (l & 15)) * 1280 + (80 * nb + 16 * c + 4 * (l >> 4)) * 2) : OOB; xr[z] = ld_b64_m(rx, go[z], v ? M_ALL : M_ONE); }
    }
    TIE(af[t % 10][0]); TIE(af[t % 10][1]);
    if constexpr (t == 0) {
#pragma unroll
      for (int c = 0; c < 5; ++c) bfr[0][c] = lds_b128(L_BRING + kq * 5120 + l * 16 + c * 1024);
    }
    SB();
#pragma unroll
    for (int c = 0; c < 5; ++c) {
      a2[0][c] = MFMA16(bfr[t & 1][c], af[t % 10][0], a2[0][c]);
      a2[1][c] = MFMA16(bfr[t & 1][c], af[t % 10][1], a2[1][c]);
      if constexpr (t + 1 < 17) bfr[(t + 1) & 1][c] = lds_b128(L_BRING + ((t + 1) % 6) * B_STAGE + kq * 5120 + l * 16 + c * 1024);
      SB();
    }
  });
  STAMP(6);
  STAMPL(50);
  LBAR();
  static_for<0, 10>([&](auto I_) { constexpr int ix = decltype(I_)::v;
    if ((ix & 3) != kq) *(LAS f32x4*)(size_t)(L_RED + ((rh * 10 + ix) * 3 + ((kq - ix - 1) & 3)) * 1024 + l * 16) = a2[ix / 5][ix % 5]; });
  asm volatile("s_waitcnt vmcnt(0) lgkmcnt(0)\n\ts_barrier" ::: "memory");
  auto fin = [&](auto I_, int z) { constexpr int ix = decltype(I_)::v;
    const unsigned ad = L_RED + (rh * 10 + ix) * 3 * 1024 + l * 16;
    const f32x4 v = (a2[ix / 5][ix % 5] + *(LAS f32x4*)(size_t)ad) + (*(LAS f32x4*)(size_t)(ad + 1024) + *(LAS f32x4*)(size_t)(ad + 2048));
    st_b64((u32x2){pk2(v[0] + bf_lo(xr[z][0]), v[1] + bf_hi(xr[z][0])), pk2(v[2] + bf_lo(xr[z][1]), v[3] + bf_hi(xr[z][1]))}, rout, go[z]); };
#pragma unroll
  for (int z = 0; z < 3; ++z) TIE(xr[z]);
  if (kq == 0) { fin(IC<0>{}, 0); fin(IC<4>{}, 1); fin(IC<8>{}, 2); }
  else if (kq == 1) { fin(IC<1>{}, 0); fin(IC<5>{}, 1); fin(IC<9>{}, 2); }
  else if (kq == 2) { fin(IC<2>{}, 0); fin(IC<6>{}, 1); }
  else { fin(IC<3>{}, 0); fin(IC<7>{}, 1); }
  if (b == 0 && tid == 0) st_b64_sc1(tag, rws, WS_EPOCH);
  STAMPL(51);
  STAMP(7);
}

extern "C" void kernel_launch(void* const* d_in, const int* in_sizes, int n_in, void* d_out, int out_size, void* d_ws, size_t ws_size, hipStream_t stream) {
  (void)in_sizes; (void)n_in; (void)out_size; (void)ws_size;
  hipFuncSetAttribute((const void*)fused_k, hipFuncAttributeMaxDynamicSharedMemorySize, LDS_BYTES);
  fused_k<<<256, 512, LDS_BYTES, stream>>>((const u16*)d_in[0], (const u16*)d_in[1], (const u16*)d_in[2], (const u16*)d_in[3], (const u16*)d_in[5], (const u16*)d_in[6], (const u16*)d_in[7], (u16*)d_out, (char*)d_ws);
}
```

```cpp
#include <hip/hip_runtime.h>
#include <hip/hip_bf16.h>
typedef unsigned short u16;
typedef unsigned long long u64;
typedef short s4 __attribute__((ext_vector_type(4)));
typedef short s8 __attribute__((ext_vector_type(8)));
typedef __bf16 b8 __attribute__((ext_vector_type(8)));
typedef __bf16 b2 __attribute__((ext_vector_type(2)));
typedef float f32x16 __attribute__((ext_vector_type(16)));
typedef float f32x4 __attribute__((ext_vector_type(4)));
typedef float f32x2 __attribute__((ext_vector_type(2)));
typedef unsigned u32x4 __attribute__((ext_vector_type(4)));
typedef unsigned u32x2 __attribute__((ext_vector_type(2)));
#define LAS __attribute__((address_space(3)))
#define DI __device__ __forceinline__
typedef __amdgpu_buffer_rsrc_t rsrc_t;

constexpr int S = 2048, D = 640, F = 1536;
constexpr unsigned WS_EPOCH = 0;
constexpr unsigned WS_FP0 = 1024, WS_FP1 = 3072, WS_FH = 5120, WS_FO = 7168, WS_FKV = 9216, WS_FCS = 11264;
constexpr unsigned WS_STATS = 16384;
constexpr unsigned WS_KVP = 32768;
constexpr unsigned KVP_STRIDE = 1792;
constexpr unsigned WS_WQP = 1u << 20;
constexpr unsigned WS_W2P = 4u << 20;
constexpr unsigned WS_CSP = 7u << 20;
constexpr unsigned WS_ACT = 8u << 20;
constexpr unsigned WS_USED = 17u << 20;
constexpr unsigned OOB = 0x80000000u;
constexpr unsigned L_X = 0;
constexpr unsigned L_W = 49152;
constexpr unsigned W_STAGE = 24832, W_UOFF = 12320;
constexpr unsigned L_MISC = L_W + 4 * W_STAGE;
constexpr unsigned L_W2S = L_MISC;
constexpr unsigned L_CS = L_W2S;
constexpr unsigned L_WPF = L_W2S + 7168;
constexpr unsigned L_QRAW = L_W + 3 * W_STAGE;
constexpr unsigned L_WPR = L_QRAW + 12288;
constexpr unsigned L_W2A = L_QRAW + 14336;
constexpr unsigned L_BRING = 0;
constexpr unsigned B_STAGE = 20480;
constexpr unsigned L_KVR = 122880;
constexpr unsigned L_KVS = 149760;
constexpr unsigned L_DUMMY1 = L_QRAW + 23552;
constexpr unsigned L_DUMMY = L_W2S + 6144;
constexpr unsigned L_RED = 0;
constexpr unsigned LDS_BYTES = 160 * 1024;

DI rsrc_t mk_rsrc(const void* p, unsigned bytes) { return __builtin_amdgcn_make_buffer_rsrc((void*)p, 0, bytes, 0x00020000); }
DI void dma16(unsigned lds_addr, unsigned voff, rsrc_t r, unsigned soff) {
  asm volatile("s_mov_b32 m0, %0\n\ts_nop 0\n\tbuffer_load_dwordx4 %1, %2, %3 offen lds" ::"s"(lds_addr), "v"(voff), "s"(r), "s"(soff) : "memory");
}
DI void dma16_sc1(unsigned lds_addr, unsigned voff, rsrc_t r, unsigned soff) {
  asm volatile("s_mov_b32 m0, %0\n\ts_nop 0\n\tbuffer_load_dwordx4 %1, %2, %3 offen sc1 lds" ::"s"(lds_addr), "v"(voff), "s"(r), "s"(soff) : "memory");
}
DI unsigned ld_u16(rsrc_t r, unsigned voff, unsigned soff) { unsigned v; asm volatile("buffer_load_ushort %0, %1, %2, %3 offen" : "=v"(v) : "v"(voff), "s"(r), "s"(soff) : "memory"); return v; }
DI unsigned ld_b32(rsrc_t r, unsigned voff, unsigned soff) { unsigned v; asm volatile("buffer_load_dword %0, %1, %2, %3 offen" : "=v"(v) : "v"(voff), "s"(r), "s"(soff) : "memory"); return v; }
DI u32x2 ld_b64_sc1(rsrc_t r, unsigned voff, unsigned soff) { u32x2 v; asm volatile("buffer_load_dwordx2 %0, %1, %2, %3 offen sc1" : "=v"(v) : "v"(voff), "s"(r), "s"(soff) : "memory"); return v; }
DI u32x4 ld_b128_sc1(rsrc_t r, unsigned voff, unsigned soff) { u32x4 v; asm volatile("buffer_load_dwordx4 %0, %1, %2, %3 offen sc1" : "=v"(v) : "v"(voff), "s"(r), "s"(soff) : "memory"); return v; }
DI u32x2 ld_b64_m(rsrc_t r, unsigned voff, u64 mask) { u32x2 v; asm volatile("s_mov_b64 exec, %3\n\tbuffer_load_dwordx2 %0, %1, %2, 0 offen\n\ts_mov_b64 exec, -1" : "=v"(v) : "v"(voff), "s"(r), "s"(mask) : "memory"); return v; }
DI void st_b64(u32x2 v, rsrc_t r, unsigned voff) { asm volatile("buffer_store_dwordx2 %0, %1, %2, 0 offen" ::"v"(v), "v"(voff), "s"(r) : "memory"); }
DI u32x4 ld_b128(rsrc_t r, unsigned voff, unsigned soff) { u32x4 v; asm volatile("buffer_load_dwordx4 %0, %1, %2, %3 offen" : "=v"(v) : "v"(voff), "s"(r), "s"(soff) : "memory"); return v; }
DI void st_b128_sc1(u32x4 v, rsrc_t r, unsigned voff) {
 asm volatile("buffer_store_dwordx4 %0, %1, %2, 0 offen sc1\n\ts_nop 1" ::"v"(v), "v"(voff), "s"(r) : "memory"); }
DI void st_b64_sc1(u32x2 v, rsrc_t r, unsigned voff) { asm volatile("buffer_store_dwordx2 %0, %1, %2, 0 offen sc1" ::"v"(v), "v"(voff), "s"(r) : "memory"); }
DI void st_b128(u32x4 v, rsrc_t r, unsigned voff) { asm volatile("buffer_store_dwordx4 %0, %1, %2, 0 offen\n\ts_nop 1" ::"v"(v), "v"(voff), "s"(r) : "memory"); }
DI void st_b64_sc1_m(u32x2 v, rsrc_t r, unsigned voff, u64 mask) {
 asm volatile("s_mov_b64 exec, %3\n\tbuffer_store_dwordx2 %0, %1, %2, 0 offen sc1\n\ts_mov_b64 exec, -1" ::"v"(v), "v"(voff), "s"(r), "s"(mask) : "memory"); }
DI u32x2 ld_b64_sc1_m(rsrc_t r, unsigned voff, u64 mask) { u32x2 v; asm volatile("s_mov_b64 exec, %3\n\tbuffer_load_dwordx2 %0, %1, %2, 0 offen sc1\n\ts_mov_b64 exec, -1" : "=v"(v) : "v"(voff), "s"(r), "s"(mask) : "memory"); return v; }
DI unsigned ld_b32_sc1_m(rsrc_t r, unsigned voff, unsigned soff, u64 mask) { unsigned v; asm volatile("s_mov_b64 exec, %4\n\tbuffer_load_dword %0, %1, %2, %3 offen sc1\n\ts_mov_b64 exec, -1" : "=v"(v) : "v"(voff), "s"(r), "s"(soff), "s"(mask) : "memory"); return v; }
DI u32x4 ld_b128_sc1_m(rsrc_t r, unsigned voff, unsigned soff, u64 mask) { u32x4 v; asm volatile("s_mov_b64 exec, %4\n\tbuffer_load_dwordx4 %0, %1, %2, %3 offen sc1\n\ts_mov_b64 exec, -1" : "=v"(v) : "v"(voff), "s"(r), "s"(soff), "s"(mask) : "memory"); return v; }
DI void st_b128_sc1_m(u32x4 v, rsrc_t r, unsigned voff, u64 mask) {
 asm volatile("s_mov_b64 exec, %3\n\tbuffer_store_dwordx4 %0, %1, %2, 0 offen sc1\n\ts_mov_b64 exec, -1\n\ts_nop 0" ::"v"(v), "v"(voff), "s"(r), "s"(mask) : "memory"); }
DI void dma16_m(unsigned lds_addr, unsigned voff, rsrc_t r, unsigned soff, u64 mask) {
  asm volatile("s_mov_b32 m0, %0\n\ts_mov_b64 exec, %4\n\tbuffer_load_dwordx4 %1, %2, %3 offen lds\n\ts_mov_b64 exec, -1" ::"s"(lds_addr), "v"(voff), "s"(r), "s"(soff), "s"(mask) : "memory");
}
#define POLL16(off) ld_b64_sc1_m(rws, w == 0 ? (off) : OOB, w == 0 ? 0xffffull : M_ONE)
#define TIE(x) asm volatile("" : "+v"(x))
#define WAITV(n) asm volatile("s_waitcnt vmcnt(" #n ")" ::: "memory")
#define WAITV_BAR(n) asm volatile("s_waitcnt vmcnt(" #n ")\n\ts_barrier" ::: "memory")
#define WAITVL_BAR(n) asm volatile("s_waitcnt vmcnt(" #n ") lgkmcnt(0)\n\ts_barrier" ::: "memory")
#define LBAR() asm volatile("s_waitcnt lgkmcnt(0)\n\ts_barrier" ::: "memory")
#define SB() __builtin_amdgcn_sched_barrier(0)
#define MFMA32(a, b, c) __builtin_amdgcn_mfma_f32_32x32x16_bf16(__builtin_bit_cast(b8, (a)), __builtin_bit_cast(b8, (b)), (c), 0, 0, 0)
#define MFMA16(a, b, c) __builtin_amdgcn_mfma_f32_16x16x32_bf16(__builtin_bit_cast(b8, (a)), __builtin_bit_cast(b8, (b)), (c), 0, 0, 0)
DI s8 lds_b128(unsigned addr) { return *(LAS s8*)(size_t)addr; }
DI s8 tr_frag(unsigned addr, unsigned off2) {
  s4 a = __builtin_amdgcn_ds_read_tr16_b64_v4i16((LAS s4*)(size_t)addr);
  s4 b = __builtin_amdgcn_ds_read_tr16_b64_v4i16((LAS s4*)(size_t)(addr + off2));
  return __builtin_shufflevector(a, b, 0, 1, 2, 3, 4, 5, 6, 7);
}
DI float dot_ones(s8 f, float acc) {
  u32x4 u = __builtin_bit_cast(u32x4, f);
#pragma unroll
  for (int i = 0; i < 4; ++i) { const unsigned t = u[i]; acc = __builtin_amdgcn_fdot2_f32_bf16(__builtin_bit_cast(b2, t), __builtin_bit_cast(b2, 0x3f803f80u), acc, false); }
  asm volatile("" : "+v"(acc));
  return acc;
}
DI unsigned pk2(float lo, float hi) { b2 c; c[0] = (__bf16)lo; c[1] = (__bf16)hi; return __builtin_bit_cast(unsigned, c); }
DI float bf_lo(unsigned u) { return __uint_as_float(u << 16); }
DI float bf_hi(unsigned u) { return __uint_as_float(u & 0xffff0000u); }
DI float swap_add(float v) { unsigned u = __float_as_uint(v); auto r = __builtin_amdgcn_permlane32_swap(u, u, false, false); return __uint_as_float(r[0]) + __uint_as_float(r[1]); }
DI float swap_max(float v) { unsigned u = __float_as_uint(v); auto r = __builtin_amdgcn_permlane32_swap(u, u, false, false); return fmaxf(__uint_as_float(r[0]), __uint_as_float(r[1])); }
DI float swap_min(float v) { unsigned u = __float_as_uint(v); auto r = __builtin_amdgcn_permlane32_swap(u, u, false, false); return fminf(__uint_as_float(r[0]), __uint_as_float(r[1])); }
DI float sadd(float a, float b) { float r; asm("v_add_f32 %0, %1, %2" : "=v"(r) : "v"(a), "v"(b)); return r; }
DI float smul(float a, float b) { float r; asm("v_mul_f32 %0, %1, %2" : "=v"(r) : "v"(a), "v"(b)); return r; }
DI float sfma(float a, float b, float c) { float r; asm("v_fma_f32 %0, %1, %2, %3" : "=v"(r) : "v"(a), "v"(b), "v"(c)); return r; }
DI float dpp_add(float v, const int ctrl_sel) {
  const int u = __float_as_int(v); int r;
  if (ctrl_sel == 0) r = __builtin_amdgcn_update_dpp(0, u, 0xB1, 0xf, 0xf, true);
  else if (ctrl_sel == 1) r = __builtin_amdgcn_update_dpp(0, u, 0x4E, 0xf, 0xf, true);
  else if (ctrl_sel == 2) r = __builtin_amdgcn_update_dpp(0, u, 0x141, 0xf, 0xf, true);
  else r = __builtin_amdgcn_update_dpp(0, u, 0x140, 0xf, 0xf, true);
  return v + __int_as_float(r);
}
DI float wave_sum(float v) {
  v = dpp_add(v, 0); v = dpp_add(v, 1); v = dpp_add(v, 2); v = dpp_add(v, 3);
  { unsigned u = __float_as_uint(v); auto r = __builtin_amdgcn_permlane16_swap(u, u, false, false); v = __uint_as_float(r[0]) + __uint_as_float(r[1]); }
  return swap_add(v);
}
DI float sum8(s8 f, float acc) {
  const u32x4 u = __builtin_bit_cast(u32x4, f);
#pragma unroll
  for (int i = 0; i < 4; ++i) { const unsigned t = u[i]; acc += __uint_as_float(t << 16); acc += __uint_as_float(t & 0xffff0000u); }
  asm volatile("" : "+v"(acc));
  return acc;
}
DI s8 pack_step(const f32x16& x, int s) {
  return __builtin_bit_cast(s8, (u32x4){pk2(x[8 * s], x[8 * s + 1]), pk2(x[8 * s + 2], x[8 * s + 3]), pk2(x[8 * s + 4], x[8 * s + 5]), pk2(x[8 * s + 6], x[8 * s + 7])});
}
template <int I> struct IC { static constexpr int v = I; };
template <int B, int E, class Fn> DI void static_for(Fn&& f) { if constexpr (B < E) { f(IC<B>{}); static_for<B + 1, E>(f); } }
DI bool flags_ok(u32x2 v, u32x2 tag, bool act) { return __all(!act || (v[0] == tag[0] && v[1] == tag[1])); }
DI void wait_flags(rsrc_t rws, unsigned base, unsigned stride, int n, u32x2 tag, int l) {

  const bool act = l < n; const unsigned off = act ? base + l * stride : OOB;
#pragma nounroll
  for (int it = 0; it < (1 << 17); ++it) {
    u32x2 v; asm volatile("buffer_load_dwordx2 %0, %1, %2, 0 offen sc1\n\ts_waitcnt vmcnt(0)" : "=&v"(v) : "v"(off), "s"(rws) : "memory");
    if (flags_ok(v, tag, act)) return;
    __builtin_amdgcn_s_sleep(4);
  }
}
DI void wait_flags256(rsrc_t rws, unsigned base, u32x2 tag, int l) {
#pragma nounroll
  for (int it = 0; it < (1 << 17); ++it) {
    u32x4 a, b;
    asm volatile("buffer_load_dwordx4 %0, %2, %3, 0 offen sc1\n\tbuffer_load_dwordx4 %1, %2, %3, 0 offen offset:16 sc1\n\ts_waitcnt vmcnt(0)" : "=&v"(a), "=&v"(b) : "v"(base + l * 32), "s"(rws) : "memory");
    const bool ok = a[0] == tag[0] && a[1] == tag[1] && a[2] == tag[0] && a[3] == tag[1] && b[0] == tag[0] && b[1] == tag[1] && b[2] == tag[0] && b[3] == tag[1];
    if (__all(ok)) return;
    __builtin_amdgcn_s_sleep(4);
  }
}
DI bool flags256_ok(u32x4 a, u32x4 b, u32x2 tag) {
  return __all(a[0] == tag[0] && a[1] == tag[1] && a[2] == tag[0] && a[3] == tag[1] && b[0] == tag[0] && b[1] == tag[1] && b[2] == tag[0] && b[3] == tag[1]);
}
DI float max3(float a, float b, float c) { float r; asm("v_max3_f32 %0, %1, %2, %3" : "=v"(r) : "v"(a), "v"(b), "v"(c)); return r; }
DI float min3(float a, float b, float c) { float r; asm("v_min3_f32 %0, %1, %2, %3" : "=v"(r) : "v"(a), "v"(b), "v"(c)); return r; }
DI f32x2 exp2_2(f32x2 a) { f32x2 r; r[0] = __builtin_amdgcn_exp2f(a[0]); r[1] = __builtin_amdgcn_exp2f(a[1]); return r; }
DI unsigned pk2v(f32x2 a) { return pk2(a[0], a[1]); }
DI void phi_map1(const f32x16& p, float sc, f32x2 (&ep)[5], f32x2 (&en)[5]) {
  float mx = max3(p[0], p[1], p[2]), mn = min3(p[0], p[1], p[2]);
#pragma unroll
  for (int r = 3; r < 9; r += 2) { mx = max3(mx, p[r], p[r + 1]); mn = min3(mn, p[r], p[r + 1]); }
  mx = max3(mx, p[9], p[9]); mn = min3(mn, p[9], p[9]);
  { unsigned u = __float_as_uint(mx); auto r = __builtin_amdgcn_permlane32_swap(u, u, false, false); mx = max3(__uint_as_float(r[0]), __uint_as_float(r[1]), __uint_as_float(r[1])); }
  { unsigned u = __float_as_uint(mn); auto r = __builtin_amdgcn_permlane32_swap(u, u, false, false); mn = min3(__uint_as_float(r[0]), __uint_as_float(r[1]), __uint_as_float(r[1])); }
  const f32x2 s2 = {sc, sc}, bp = {-mx * sc, -mx * sc}, bn = {mn * sc, mn * sc};
#pragma unroll
  for (int r = 0; r < 5; ++r) { const f32x2 v = {p[2 * r], p[2 * r + 1]}; ep[r] = exp2_2(__builtin_elementwise_fma(v, s2, bp)); en[r] = exp2_2(__builtin_elementwise_fma(v, -s2, bn)); }
}
DI void phi_map2(const f32x2 (&ep)[5], const f32x2 (&en)[5], unsigned (&o)[10]) {
  const f32x2 sp2 = (ep[0] + ep[1]) + (ep[2] + ep[3]) + ep[4], sn2 = (en[0] + en[1]) + (en[2] + en[3]) + en[4];
  const float rp = __builtin_amdgcn_rcpf(swap_add(sp2[0] + sp2[1])), rn = __builtin_amdgcn_rcpf(swap_add(sn2[0] + sn2[1]));
  const f32x2 rp2 = {rp, rp}, rn2 = {rn, rn};
#pragma unroll
  for (int r = 0; r < 5; ++r) { o[r] = pk2v(ep[r] * rp2); o[5 + r] = pk2v(en[r] * rn2); }
}

#define STAMP(i)
#define STAMPL(k)
#define STAMPC(i)
#define STAMPF(i)

__global__ __launch_bounds__(512, 2) void fused_k(const u16* __restrict__ x, const u16* __restrict__ wqkv, const u16* __restrict__ wphi, const u16* __restrict__ wo,
                                                   const u16* __restrict__ wg, const u16* __restrict__ wu, const u16* __restrict__ wd, u16* __restrict__ out, char* __restrict__ ws) {
  extern __shared__ __attribute__((aligned(256))) char smem[];
  const int tid = threadIdx.x; const int l = tid & 63; const int w = __builtin_amdgcn_readfirstlane(tid >> 6);
  const int tg = w & 3, ch = w >> 2; const int l31 = l & 31, h = l >> 5;
  const int b = blockIdx.x, xcd = b & 7, idx = b >> 3;
  const int rb = (xcd & 1) * 8 + (idx & 7), cg = (xcd >> 1) * 4 + (idx >> 3);
  const int r2 = (xcd & 3) * 8 + (idx & 7), nb = (xcd >> 2) * 4 + (idx >> 3);
  const int klo = (5 * rb) >> 1, kcnt = ((5 * (rb + 1)) >> 1) - klo, rot = klo >> 2, kloc = klo - 4 * rot;
  STAMP(0);
  STAMPL(0);
  const rsrc_t rx = mk_rsrc(x, S * D * 2);
  const rsrc_t rw = mk_rsrc(w < 4 ? wg : wu, D * F * 2);
  const rsrc_t rws = mk_rsrc(ws, WS_USED);
  const u64 M_ALL = ~0ull, M_ONE = 1ull;
  auto sig = [&](int s) -> unsigned { const int t = s + rot; return t >= 10 ? t - 10 : t; };
  unsigned vx[2], vw[3];
#pragma unroll
  for (int i = 0; i < 2; ++i) { const int r = 8 * (2 * w + i) + (l >> 3); const int c = (l & 7) ^ ((r >> 1) & 7); vx[i] = (128 * rb + r) * 1280 + c * 16; }
#pragma unroll
  for (int i = 0; i < 3; ++i) { const int p = 64 * (3 * (w & 3) + i) + l; const int k = p / 12, c = p % 12; vw[i] = k * 3072 + cg * 192 + c * 16; }
  const unsigned lx0 = L_X + 2 * w * 1024;
  const unsigned lw0 = L_W + (w < 4 ? 0 : W_UOFF) + 3 * (w & 3) * 1024;
  auto dmax = [&](int s, int i) { dma16(lx0 + (s % 3) * 16384 + i * 1024, vx[i], rx, sig(s) * 128); };
  auto dmaw = [&](int s, int i) { dma16(lw0 + (s & 3) * W_STAGE + i * 1024, vw[i], rw, sig(s) * (64 * 3072)); };
  auto issue1 = [&](int s) { dmax(s, 0); dmax(s, 1); dmaw(s, 0); dmaw(s, 1); dmaw(s, 2); };
  issue1(0);
  u32x2 ep = ld_b64_sc1_m(rws, WS_EPOCH, M_ONE);
  const int srow = 128 * rb + 8 * cg + w;
  const bool v3 = kcnt == 3 && w < 4;
  u32x4 sx0, sx1;
  {
    const rsrc_t rq = mk_rsrc(wqkv, D * 1920 * 2);
    const int p = 64 * (w & 3) + l; const int k = p / 15, c = p % 15;
    const unsigned vo = p < 240 ? (unsigned)(k * 3840 + (c / 5) * 1280 + cg * 80 + (c % 5) * 16) : OOB;
    dma16(L_QRAW + w * 1024, vo, rq, (16 * (klo + (w >> 2))) * 3840);
    dma16_m(v3 ? L_QRAW + 8192 + w * 1024 : L_DUMMY1, v3 ? vo : OOB, rq, v3 ? (16 * (klo + 2)) * 3840 : 0, v3 ? M_ALL : M_ONE);
    const rsrc_t rp = mk_rsrc(wphi, 32 * 400 * 2);
    dma16_m(w < 2 ? L_WPR + w * 1024 : L_DUMMY1, (w < 2 && w * 64 + l < 100) ? (unsigned)(cg * 1600 + (w * 64 + l) * 16) : OOB, rp, 0, w == 0 ? M_ALL : (w == 1 ? 0xfffffffffull : M_ONE));
    sx0 = ld_b128(rx, srow * 1280 + l * 16, 0);
    sx1 = ld_b128(rx, l < 16 ? (unsigned)(srow * 1280 + 1024 + l * 16) : OOB, 0);
    const int kst = b / 5, cb = b % 5; const bool isd = kst < 48;
    const rsrc_t rr = mk_rsrc(isd ? wd : wo, isd ? F * D * 2 : D * D * 2);
    dma16(L_W2A + w * 1024, (unsigned)((l >> 4) * 1280 + (l & 15) * 16), rr, ((isd ? 32 * kst : 32 * (kst - 48)) + 4 * w) * 1280 + cb * 256);
  }
  const int gi = l >> 4, q4 = (l >> 2) & 3, p4 = l & 3;
  issue1(1); issue1(2);
  {
    u64 pc; unsigned len;
    asm volatile("s_getpc_b64 %0\n.Lfk_here:\n\ts_mov_b32 %1, .Lfk_end-.Lfk_here" : "=s"(pc), "=s"(len));
    const rsrc_t rc = mk_rsrc((const void*)pc, len);
    const unsigned off = (idx * 8 + w) * 256;
    asm volatile("s_mov_b32 m0, %0\n\ts_mov_b64 exec, 0xffff\n\tbuffer_load_dwordx4 %1, %2, 0 offen lds\n\ts_mov_b64 exec, -1" ::"s"(L_DUMMY1), "v"(off + l * 16), "s"(rc) : "memory");
  }
  WAITV_BAR(11);
  STAMP(1);
  STAMPL(1);
  TIE(ep);
  u32x2 tag;
  { const unsigned lo = __builtin_amdgcn_readfirstlane(ep[0]), hi = __builtin_amdgcn_readfirstlane(ep[1]); tag[0] = lo + 1u; tag[1] = hi + (lo == 0xffffffffu ? 1u : 0u); }
  const int shc = (w >> 1) & 1, sht = w & 1;
  s8 sf0, sf1, sf2; unsigned se[8]; unsigned smA, smB;
  {
    const int Q = 4 * (gi & 1) + p4;
    int seg, qd;
    if (sht == 0) { seg = Q < 5 ? 0 : 1; qd = Q < 5 ? Q : Q - 5; } else { seg = Q < 2 ? 1 : 2; qd = Q < 2 ? 3 + Q : (Q < 7 ? Q - 2 : 4); }
    const unsigned lo = L_QRAW + (8 * h + q4) * 240 + seg * 80 + shc * 40 + qd * 8;
    sf0 = tr_frag(lo + (w >> 2) * 4096, 960); sf1 = tr_frag(lo + 8192, 960);
    sf2 = tr_frag(L_W2A + (8 * gi + q4) * 256 + w * 32 + p4 * 8, 1024);
    const int reg = (l31 & 3) + 4 * (l31 >> 3), hh = (l31 >> 2) & 1;
    const int d0 = (tg == 0 ? 0 : tg == 1 ? 16 : tg == 2 ? -4 : 12) + 4 * h;
    const unsigned wa = L_WPR + ch * 800 + (10 * hh + reg) * 2 + d0 * 40;
#pragma unroll
    for (int j = 0; j < 8; ++j) se[j] = *(LAS u16*)(size_t)(wa + (8 * (j >> 2) + (j & 3)) * 40);
    smA = (reg < 10 && (unsigned)d0 < 20u) ? ~0u : 0u; smB = (reg < 10 && (unsigned)(d0 + 8) < 20u) ? ~0u : 0u;
  }
  auto side = [&](int part) {
    if (part == 0) {
      TIE(sx0); TIE(sx1);
      float sm = 0.f, sq = 0.f;
#pragma unroll
      for (int i = 0; i < 4; ++i) { const unsigned u0 = sx0[i], u1 = sx1[i];
        sm = __builtin_amdgcn_fdot2_f32_bf16(__builtin_bit_cast(b2, u0), __builtin_bit_cast(b2, 0x3f803f80u), sm, false); sq = __builtin_amdgcn_fdot2_f32_bf16(__builtin_bit_cast(b2, u0), __builtin_bit_cast(b2, u0), sq, false);
        sm = __builtin_amdgcn_fdot2_f32_bf16(__builtin_bit_cast(b2, u1), __builtin_bit_cast(b2, 0x3f803f80u), sm, false); sq = __builtin_amdgcn_fdot2_f32_bf16(__builtin_bit_cast(b2, u1), __builtin_bit_cast(b2, u1), sq, false); }
      const float m = wave_sum(sm) * (1.f / D);
      const float var = fmaxf(wave_sum(sq) * (1.f / D) - m * m, 0.f);
      st_b64_sc1_m((u32x2){__float_as_uint(m), __float_as_uint(__builtin_amdgcn_rsqf(var + 1e-5f))}, rws, WS_STATS + srow * 8, M_ONE);
    } else if (part == 1) {
      st_b128_sc1(__builtin_bit_cast(u32x4, sf0), rws, WS_WQP + (((2 * cg + shc) * 40 + klo + (w >> 2)) * 2 + sht) * 1024 + l * 16);
      st_b128_sc1_m(__builtin_bit_cast(u32x4, sf1), rws, v3 ? WS_WQP + (((2 * cg + shc) * 40 + klo + 2) * 2 + sht) * 1024 + l * 16 : OOB, v3 ? M_ALL : M_ONE);
      const int kst = b / 5, cb = b % 5, ct = 8 * cb + w;
      st_b128_sc1(__builtin_bit_cast(u32x4, sf2), rws, WS_W2P + (((ct / 5) * 68 + kst) * 5 + ct % 5) * 1024 + l * 16);
      const float c0 = swap_add(dot_ones(sf0, 0.f)), c1 = swap_add(dot_ones(sf1, 0.f));
      if (h == 0) { const unsigned qa = L_W2S + ((shc * 2 + sht) * 32 + l31) * 16 + (w >> 2) * 4; *(LAS float*)(size_t)qa = c0; *(LAS float*)(size_t)(qa + 8) = v3 ? c1 : 0.f; }
    } else {
      *(LAS u32x4*)(size_t)(L_WPF + w * 1024 + l * 16) = (u32x4){(se[0] | (se[1] << 16)) & smA, (se[2] | (se[3] << 16)) & smA, (se[4] | (se[5] << 16)) & smB, (se[6] | (se[7] << 16)) & smB};
    }
  };
  STAMPF(8);
  const unsigned qoff = (p4 == 1) ? 8 : (p4 == 2) ? 4 : (p4 == 3) ? 12 : 0;
  const unsigned wl = L_W + ((gi & 1) ? W_UOFF : 0) + (8 * h + q4) * 192 + 2 * qoff + 96 * ch;
  const unsigned wl2 = wl + 2 * W_STAGE;
  const int xrow = 32 * tg + l31;
  unsigned xa[4];
#pragma unroll
  for (int j = 0; j < 4; ++j) { xa[j] = (L_X + xrow * 128 + ((h ^ ((xrow >> 1) & 7)) << 4)) ^ (j * 32); asm volatile("" : "+v"(xa[j])); }
  s8 XF[40];
  f32x16 acc[3]; float cs[3];
#pragma unroll
  for (int i = 0; i < 3; ++i) { cs[i] = 0.f;
#pragma unroll
    for (int r = 0; r < 16; ++r) acc[i][r] = 0.f; }
  s8 wf[2][3];
  auto rdx = [&](int s, int j) { XF[4 * s + j] = lds_b128(xa[j] + (s % 3) * 16384); };
  auto rdw = [&](int n, int i) { const int s = n >> 2, j = n & 3; wf[n & 1][i] = tr_frag(((s & 2) ? wl2 : wl) + (s & 1) * W_STAGE + j * 3072 + i * 32, 768); };
  const unsigned vq2 = WS_WQP + ((2 * cg + ch) * 40 + tg) * 2048 + l * 16;
  auto slot2 = [&](int q) -> unsigned { return q == 0 || q == 7 ? 16384u : q == 1 || q == 8 ? L_W + 2 * W_STAGE : q == 2 || q == 9 ? 32768u : q == 3 ? L_W + 3 * W_STAGE : q == 4 ? 0u : q == 5 ? L_W : L_W + W_STAGE; };
  auto issue2 = [&](int q) { const unsigned so = sig(q) * 8192; dma16_sc1(slot2(q) + w * 2048, vq2, rws, so); dma16_sc1(slot2(q) + w * 2048 + 1024, vq2, rws, so + 1024); };
  const bool hv = b < 168 && w < 4; const int hu = 256 + (b >> 1), hkst = hu / 5, hcb = hu % 5;
  u32x2 pf, pf2, st; u32x4 pl0, pl1, cg4[4]; unsigned cg1[4]; f32x4 qs0, qs1;
  rdx(0, 0); rdw(0, 0); rdw(0, 1); rdw(0, 2); rdx(0, 1); rdx(0, 2); rdx(0, 3);
  static_for<0, 40>([&](auto N_) {
    constexpr int n = decltype(N_)::v;
    constexpr int s = n >> 2, j = n & 3;
    if constexpr (n == 3) STAMPF(9);
    if constexpr (n == 11) STAMPF(10);
    if constexpr (n == 19) STAMPF(11);
    if constexpr (n == 27) STAMPF(12);
    if constexpr (n == 35) STAMPF(13);
    if constexpr (j == 3 && s < 9) STAMPL(2 + 2 * s);
    if constexpr (n == 3) WAITV_BAR(10);
    if constexpr (n == 7) { WAITV_BAR(9); qs0 = *(LAS f32x4*)(size_t)(L_W2S + ((ch * 2) * 32 + l31) * 16); qs1 = *(LAS f32x4*)(size_t)(L_W2S + ((ch * 2 + 1) * 32 + l31) * 16); }
    if constexpr (n == 11) WAITV_BAR(7);
    if constexpr (n == 15) WAITV_BAR(5);
    if constexpr (n == 19) WAITV_BAR(7);
    if constexpr (n == 23) { WAITV_BAR(6); pf = POLL16(WS_FP0 + cg * 128 + l * 8); pf2 = POLL16(WS_FP0 + rb * 8 + (l & 15) * 128); }
    if constexpr (n == 27) {
      WAITV(6); TIE(pf); TIE(pf2);
      if (w == 0) {
        if (__builtin_expect(!flags_ok(pf, tag, l < 16), 0)) wait_flags(rws, WS_FP0 + cg * 128, 8, 16, tag, l);
        if (__builtin_expect(!flags_ok(pf2, tag, l < 16), 0)) wait_flags(rws, WS_FP0 + rb * 8, 128, 16, tag, l);
      }
      asm volatile("s_barrier" ::: "memory");
      st = ld_b64_sc1(rws, WS_STATS + (128 * rb + xrow) * 8, 0);
      {
        const unsigned vo = w < 4 ? WS_CSP + (cg * 16 + 4 * w) * 2048 + l * 32 : OOB;
#pragma unroll
        for (int r = 0; r < 4; ++r) { cg4[r] = ld_b128_sc1_m(rws, vo, r * 2048, w < 4 ? M_ALL : M_ONE); cg1[r] = ld_b32_sc1_m(rws, vo + 16, r * 2048, w < 4 ? M_ALL : M_ONE); } }
    }
    if constexpr (n == 31) {
      WAITV_BAR(14);
      st_b64_sc1_m(tag, rws, w == 0 ? WS_FP1 + b * 8 : OOB, M_ONE);
      issue2(0); issue2(1);
    }
    if constexpr (n == 35) {
      WAITV_BAR(5);
#pragma unroll
      for (int r = 0; r < 4; ++r) { TIE(cg4[r]); TIE(cg1[r]); }
      if (w < 4) { const unsigned ad = L_CS + ((h * 5) * 32 + l31) * 16 + w * 4;
#pragma unroll
        for (int e = 0; e < 4; ++e) *(LAS float*)(size_t)(ad + e * 512) = (__uint_as_float(cg4[0][e]) + __uint_as_float(cg4[1][e])) + (__uint_as_float(cg4[2][e]) + __uint_as_float(cg4[3][e]));
        *(LAS float*)(size_t)(ad + 4 * 512) = (__uint_as_float(cg1[0]) + __uint_as_float(cg1[1])) + (__uint_as_float(cg1[2]) + __uint_as_float(cg1[3])); }
      issue2(2); issue2(3);
    }
    if constexpr (j == 3 && s < 9) STAMPL(3 + 2 * s);
    SB();
#pragma unroll
    for (int i = 0; i < 3; ++i) {
      acc[i] = MFMA32(wf[n & 1][i], XF[n], acc[i]);
      SB();
      if (i == 0 && j == 3 && s < 9) rdx(s + 1, 0);
      if (n + 1 < 40) rdw(n + 1, i);
      if (j == 0 && s > 0) rdx(s, i + 1);
      if (n < 3 && i == 2) side(n);
      if (n < 8 && i == 2) { if ((unsigned)(n - kloc) < (unsigned)kcnt && tg == 0) { cs[0] = dot_ones(wf[n & 1][0], cs[0]); cs[1] = dot_ones(wf[n & 1][1], cs[1]); cs[2] = dot_ones(wf[n & 1][2], cs[2]); } }
      if (n == 8 && i == 0) {
        const unsigned dst = tg == 0 ? WS_CSP + (cg * 16 + rb) * 2048 + (ch * 32 + l31) * 32 : OOB; const u64 mk = tg == 0 ? 0xffffffffull : M_ONE;
        st_b128_sc1_m((u32x4){__float_as_uint(swap_add(cs[0])), __float_as_uint(swap_add(cs[1])), __float_as_uint(swap_add(cs[2])), __float_as_uint((qs0[0] + qs0[1]) + (qs0[2] + qs0[3]))}, rws, dst, mk);
        st_b128_sc1_m((u32x4){__float_as_uint((qs1[0] + qs1[1]) + (qs1[2] + qs1[3])), 0u, 0u, 0u}, rws, dst + 16, mk);
      }
      if (j == 3 && s + 3 < 10) {
        if (i == 0) { dmax(s + 3, 0); dmax(s + 3, 1); }
        if (i == 1) { dmaw(s + 3, 0); dmaw(s + 3, 1); }
        if (i == 2) dmaw(s + 3, 2);
      }
      if constexpr (n == 15) { if (i == 2) {
        const rsrc_t rr = mk_rsrc(wo, D * D * 2);
        dma16_m(hv ? L_W2S + w * 1024 : L_DUMMY, hv ? (unsigned)((l >> 3) * 1280 + (l & 7) * 16) : OOB, rr, hv ? (32 * (hkst - 48) + 8 * w) * 1280 + hcb * 256 + (b & 1) * 128 : 0, hv ? M_ALL : M_ONE);
        st_b64_sc1_m(tag, rws, w == 0 ? WS_FP0 + (cg * 16 + rb) * 8 : OOB, M_ONE);
      } }
      if constexpr (n == 23) { if (i == 2) {
        const int ct = 8 * hcb + 4 * (b & 1) + (w & 3);
        const s8 f = tr_frag(L_W2S + (8 * gi + q4) * 128 + (w & 3) * 32 + p4 * 8, 512);
        st_b128_sc1_m(__builtin_bit_cast(u32x4, f), rws, hv ? WS_W2P + (((ct / 5) * 68 + hkst) * 5 + ct % 5) * 1024 + l * 16 : OOB, hv ? M_ALL : M_ONE);
      } }
      SB();
    }
  });
  STAMP(2);
  STAMPL(20);
  WAITVL_BAR(4);
  TIE(st);
  const float mean = __uint_as_float(st[0]), rstd = __uint_as_float(st[1]);
  s8 bm;
  { const float nm = -mean; const unsigned hi = pk2(nm, 0.f) & 0xffffu; const float lo = nm - __uint_as_float(hi << 16); unsigned mpk = hi | (pk2(lo, 0.f) << 16); if (h) mpk = 0; bm = __builtin_bit_cast(s8, (u32x4){mpk, mpk, 0u, 0u}); }
  auto cs_frag = [&](int tile) -> s8 {
    const f32x4 pp = *(LAS f32x4*)(size_t)(L_CS + ((ch * 5 + tile) * 32 + l31) * 16);
    const float c = (pp[0] + pp[1]) + (pp[2] + pp[3]);
    unsigned hi = pk2(c, c); const float lo = c - __uint_as_float(hi << 16); unsigned lo2 = pk2(lo, lo);
    if (h) { hi = 0; lo2 = 0; }
    return __builtin_bit_cast(s8, (u32x4){hi, lo2, 0u, 0u});
  };
  const unsigned tok = 128 * rb + xrow;
  const unsigned actbase = WS_ACT + (tok >> 4) * 68 * 1024 + (l & 15) * 16;
  {
    const float gs = -1.4426950408889634f * rstd, irs = __builtin_amdgcn_rcpf(rstd);
#pragma unroll
    for (int i = 0; i < 3; ++i) acc[i] = MFMA32(cs_frag(i), bm, acc[i]);
#pragma unroll
    for (int i = 0; i < 3; ++i) {
      issue2(4 + i);
      const f32x2 gs2 = {gs, gs}, irs2 = {irs, irs};
      u32x4 ho;
#pragma unroll
      for (int r = 0; r < 4; ++r) {
        const f32x2 g2 = {acc[i][2 * r], acc[i][2 * r + 1]}, u2 = {acc[i][8 + 2 * r], acc[i][9 + 2 * r]};
        const f32x2 dn = __builtin_elementwise_fma(exp2_2(g2 * gs2), irs2, irs2);
        const f32x2 rc = {__builtin_amdgcn_rcpf(dn[0]), __builtin_amdgcn_rcpf(dn[1])};
        ho[r] = pk2v(u2 * rc);
      }
      const unsigned G = 12 * cg + 6 * ch + 2 * i + h;
      st_b128_sc1(ho, rws, actbase + (G >> 2) * 1024 + (G & 3) * 256);
      SB();
    }
  }
  STAMP(3);
  STAMPL(21);
  f32x16 T[2];
#pragma unroll
  for (int i = 0; i < 2; ++i)
#pragma unroll
    for (int r = 0; r < 16; ++r) T[i][r] = 0.f;
  s8 qf[3][2];
  const unsigned ql = ch * 8192 + l * 16, ql2 = ql + L_W;
  auto rdq = [&](int m, int t) { const int q = m >> 2, j = m & 3; const unsigned sl = slot2(q); qf[m % 3][t] = lds_b128((sl >= L_W ? ql2 + (sl - L_W) : ql + sl) + j * 2048 + t * 1024); };
  rdq(0, 0); rdq(0, 1); rdq(1, 0); rdq(1, 1);
  static_for<0, 40>([&](auto M_) {
    constexpr int m = decltype(M_)::v;
    if constexpr (m == 6) STAMPL(22);
    if constexpr (m == 14) STAMPL(23);
    if constexpr (m == 15) STAMPL(24);
    if constexpr (m == 26) STAMPL(25);
    if constexpr (m == 34) STAMPL(26);
    if constexpr (m == 6) { WAITV_BAR(11); issue2(7); }
    if constexpr (m == 10) { WAITV_BAR(11); issue2(8); }
    if constexpr (m == 14) { WAITV_BAR(4); st_b64_sc1_m(tag, rws, w == 0 ? WS_FH + (rb * 16 + cg) * 8 : OOB, M_ONE); issue2(9); }
    if constexpr (m == 26) {
      WAITV_BAR(5);
      pl0 = ld_b128_sc1_m(rws, WS_FP1 + l * 32, 0, w == 0 ? M_ALL : M_ONE); pl1 = ld_b128_sc1_m(rws, WS_FP1 + l * 32 + 16, 0, w == 0 ? M_ALL : M_ONE);
      pf = POLL16(WS_FH + (r2 >> 1) * 128 + l * 8);
    }
    if constexpr (m == 30) WAITV_BAR(6);
    if constexpr (m == 34) WAITV_BAR(3);
    if constexpr (m == 38) {
      WAITV(0); TIE(pl0); TIE(pl1); TIE(pf);
      if (w == 0) {
        if (__builtin_expect(!flags256_ok(pl0, pl1, tag), 0)) wait_flags256(rws, WS_FP1, tag, l);
        if (__builtin_expect(!flags_ok(pf, tag, l < 16), 0)) wait_flags(rws, WS_FH + (r2 >> 1) * 128, 8, 16, tag, l);
      }
    }
    SB();
#pragma unroll
    for (int t = 0; t < 2; ++t) {
      T[t] = MFMA32(qf[m % 3][t], XF[m], T[t]);
      SB();
      if (m + 2 < 40) rdq(m + 2, t);
      SB();
    }
  });
  STAMP(4);
  STAMPL(27);
  LBAR();
  STAMPL(28);
  const unsigned vb = WS_W2P + nb * 68 * 5 * 1024 + l * 16;
  const unsigned bf0 = w < 4 ? 3 * w : 12 + 2 * (w - 4);
  auto issueB = [&](int t) {
    const unsigned base = L_BRING + (t % 6) * B_STAGE;
    dma16_sc1(base + bf0 * 1024, vb, rws, t * B_STAGE + bf0 * 1024);
    dma16_sc1(base + (bf0 + 1) * 1024, vb, rws, t * B_STAGE + (bf0 + 1) * 1024);
    asm volatile("s_mov_b32 m0, %0\n\ts_mov_b64 exec, %4\n\tbuffer_load_dwordx4 %1, %2, %3 offen sc1 lds\n\ts_mov_b64 exec, -1" ::"s"(w < 4 ? base + (bf0 + 2) * 1024 : L_DUMMY), "v"(w < 4 ? vb : OOB), "s"(rws), "s"(t * B_STAGE + (bf0 + 2) * 1024), "s"(w < 4 ? M_ALL : M_ONE) : "memory");
  };
  const int kq = w & 3, rh = w >> 2;
  const unsigned va = WS_ACT + (4 * r2 + 2 * rh) * 68 * 1024 + kq * 1024 + l * 16;
  u32x4 af[10][2];
  auto lda = [&](int t) { af[t % 10][0] = ld_b128_sc1(rws, va, t * 4096); af[t % 10][1] = ld_b128_sc1(rws, va + 68 * 1024, t * 4096); };
  f32x16 P;
  const float psc = 1.4426950408889634f * rstd;
  {
    issueB(0);
    T[0] = MFMA32(cs_frag(3), bm, T[0]);
    T[1] = MFMA32(cs_frag(4), bm, T[1]);
    const unsigned wpl = L_WPF + ch * 4096 + l * 16;
    const s8 A0 = lds_b128(wpl), A1 = lds_b128(wpl + 1024), A2 = lds_b128(wpl + 2048), A3 = lds_b128(wpl + 3072);
    s8 E0, E1;
    { const int k0 = l31 - 8 * h; const unsigned val = (k0 & 1) ? 0x3f800000u : 0x3f80u; u32x4 u0, u1;
#pragma unroll
      for (int d2 = 0; d2 < 4; ++d2) { u0[d2] = (k0 >> 1) == d2 ? val : 0u; u1[d2] = ((k0 - 16) >> 1) == d2 ? val : 0u; }
      E0 = __builtin_bit_cast(s8, u0); E1 = __builtin_bit_cast(s8, u1); }
    SB(); lda(0); lda(1); SB();
    const s8 t01 = pack_step(T[0], 1), t10u = pack_step(T[1], 0), t00 = pack_step(T[0], 0);
    f32x16 z16;
#pragma unroll
    for (int r = 0; r < 16; ++r) z16[r] = 0.f;
    f32x16 Pk = MFMA32(A2, t01, z16); Pk = MFMA32(A3, t10u, Pk);
#pragma unroll
    for (int r = 0; r < 8; ++r) { const f32x2 v = (f32x2){T[1][2 * r], T[1][2 * r + 1]} * (f32x2){rstd, rstd}; T[1][2 * r] = v[0]; T[1][2 * r + 1] = v[1]; }
    const s8 t10 = pack_step(T[1], 0);
    s8 t11 = pack_step(T[1], 1);
    { u32x4 u = __builtin_bit_cast(u32x4, t11); if (h) u[2] = (u[2] & 0xffff0000u) | 0x3f80u; t11 = __builtin_bit_cast(s8, u); }
    f32x16 V = MFMA32(t10, E0, z16); V = MFMA32(t11, E1, V);
    SB(); issueB(1); SB();
    unsigned phk[10];
    { f32x2 ep[5], en[5]; phi_map1(Pk, psc, ep, en); SB(); lda(2); lda(3); SB(); phi_map2(ep, en, phk); }
    SB(); issueB(2); SB();
    const s8 v0 = pack_step(V, 0), v1 = pack_step(V, 1);
    const s8 F0 = __builtin_bit_cast(s8, (u32x4){phk[0], phk[1], phk[2], phk[3]}), F1 = __builtin_bit_cast(s8, (u32x4){phk[4], phk[5], phk[6], phk[7]}), F2 = __builtin_bit_cast(s8, (u32x4){phk[8], phk[9], 0u, 0u});
    f32x16 Z0 = MFMA32(F0, E0, z16); Z0 = MFMA32(F1, E1, Z0);
    f32x16 Z1 = MFMA32(F2, E0, z16);
    f32x16 KV0 = MFMA32(pack_step(Z0, 0), v0, z16); KV0 = MFMA32(pack_step(Z0, 1), v1, KV0);
    f32x16 KV1 = MFMA32(pack_step(Z1, 0), v0, z16);
    const int rho = 16 * (l31 >> 4) + 8 * ((l31 & 7) >> 2) + 4 * ((l31 >> 3) & 1) + (l31 & 3);
    const int d = rho == 28 ? 20 : rho - 8;
    KV1 = MFMA32(pack_step(Z1, 1), v1, KV1);
    P = MFMA32(A0, t00, z16); P = MFMA32(A1, t01, P);
    if (d >= 0 && d <= 20) {
      const unsigned kb = L_KVR + w * 3360 + d * 160;
#pragma unroll
      for (int g = 0; g < 4; ++g) *(LAS f32x4*)(size_t)(kb + (8 * g + 4 * h) * 4) = (f32x4){KV0[4 * g], KV0[4 * g + 1], KV0[4 * g + 2], KV0[4 * g + 3]};
      if (h == 0) { *(LAS f32x4*)(size_t)(kb + 128) = (f32x4){KV1[0], KV1[1], KV1[2], KV1[3]}; *(LAS f32x4*)(size_t)(kb + 144) = (f32x4){KV1[4], KV1[5], KV1[6], KV1[7]}; }
    }
  }
  STAMPL(29);
  LBAR();
  {
    const int c = tid & 255, hc = tid >> 8;
    u32x4 o = {0u, 0u, 0u, 0u};
    if (c < 105) {
      float a[8];
#pragma unroll
      for (int e = 0; e < 8; ++e) a[e] = 0.f;
#pragma unroll
      for (int g = 0; g < 4; ++g) {
        const unsigned ad = L_KVR + (hc * 4 + g) * 3360 + c * 32;
        const f32x4 u0 = *(LAS f32x4*)(size_t)ad, u1 = *(LAS f32x4*)(size_t)(ad + 16);
#pragma unroll
        for (int e = 0; e < 4; ++e) { a[e] += u0[e]; a[4 + e] += u1[e]; }
      }
      o = (u32x4){pk2(a[0], a[1]), pk2(a[2], a[3]), pk2(a[4], a[5]), pk2(a[6], a[7])};
    }
    st_b128_sc1_m(o, rws, c < 105 ? WS_KVP + ((cg * 2 + hc) * 16 + rb) * KVP_STRIDE + c * 16 : OOB, tg == 0 ? M_ALL : (tg == 1 ? 0x1ffffffffffull : M_ONE));
  }
  STAMPL(30);
  const rsrc_t rout = mk_rsrc(out, S * D * 2);
  unsigned phq[10];
  lda(4); lda(5); issueB(3); SB();
  { f32x2 ep[5], en[5]; phi_map1(P, psc, ep, en); SB(); lda(6); lda(7); issueB(4); SB(); phi_map2(ep, en, phq); }
#pragma unroll
  for (int r = 0; r < 10; ++r) TIE(phq[r]);
  SB(); lda(8); lda(9);
  STAMPL(31);
  WAITV_BAR(18);
  st_b64_sc1_m(tag, rws, w == 0 ? WS_FKV + (cg * 16 + rb) * 8 : OOB, M_ONE);
  issueB(5);
  STAMP(5);
  STAMPL(32);
  f32x4 a2[2][5];
#pragma unroll
  for (int a = 0; a < 2; ++a)
#pragma unroll
    for (int c = 0; c < 5; ++c) a2[a][c] = (f32x4){0.f, 0.f, 0.f, 0.f};
  u32x4 kp[8]; u32x2 xr[3]; unsigned go[3]; s8 bfr[2][5];
  const int kc = (64 * tg + l) >> 1; const bool kact = kc < 105;
  static_for<0, 17>([&](auto T_) {
    constexpr int t = decltype(T_)::v;
    if constexpr (t == 4) STAMPF(16);
    if constexpr (t == 7) STAMPF(17);
    if constexpr (t == 11) STAMPF(18);
    if constexpr (t == 12) STAMPF(19);
    constexpr int T_PKV = 3, T_KVL = 4, T_SUM = 6, T_O = 7, T_FO = 10, T_PO = 11, T_AO = 12;
    constexpr int allow_tab[17] = {30, 23, 20, 18, 3, 20, 6, 17, 11, 11, 9, 10, 3, 6, 4, 5, 3};
    STAMPL(33 + t);
    asm volatile("s_waitcnt vmcnt(%0) lgkmcnt(0)" ::"i"(allow_tab[t]) : "memory");
    if constexpr (t == T_KVL) { TIE(pf); if (w == 0) { if (__builtin_expect(!flags_ok(pf, tag, l < 16), 0)) wait_flags(rws, WS_FKV + cg * 128, 8, 16, tag, l); } }
    if constexpr (t == T_AO) { TIE(pf); if (w == 0) { if (__builtin_expect(!flags_ok(pf, tag, l < 16), 0)) wait_flags(rws, WS_FO + (r2 >> 1) * 128, 8, 16, tag, l); } }
    asm volatile("s_barrier" ::: "memory");
    if constexpr (t == T_PKV) pf = POLL16(WS_FKV + cg * 128 + l * 8);
    if constexpr (t == T_KVL) {
#pragma unroll
      for (int r = 0; r < 8; ++r) kp[r] = ld_b128_sc1_m(rws, kact ? WS_KVP + ((cg * 2 + ch) * 16 + 8 * (l & 1)) * KVP_STRIDE + kc * 16 : OOB, r * KVP_STRIDE, tg < 3 ? M_ALL : 0x3ffffull);
    }
    if constexpr (t == T_SUM) {
      float a[8];
#pragma unroll
      for (int r = 0; r < 8; ++r) { TIE(kp[r]);
#pragma unroll
        for (int e = 0; e < 4; ++e) { a[2 * e] = r == 0 ? bf_lo(kp[r][e]) : sadd(a[2 * e], bf_lo(kp[r][e])); a[2 * e + 1] = r == 0 ? bf_hi(kp[r][e]) : sadd(a[2 * e + 1], bf_hi(kp[r][e])); } }
      asm volatile("s_nop 1" : "+v"(a[0]), "+v"(a[1]), "+v"(a[2]), "+v"(a[3]), "+v"(a[4]), "+v"(a[5]), "+v"(a[6]), "+v"(a[7]));
#pragma unroll
      for (int e = 0; e < 8; ++e) a[e] = dpp_add(a[e], 0);
      if (kact && (l & 1) == 0) { const int d = kc / 5, cc = kc % 5; *(LAS u32x4*)(size_t)(L_KVS + ch * 2016 + d * 96 + cc * 16) = (u32x4){pk2(a[0], a[1]), pk2(a[2], a[3]), pk2(a[4], a[5]), pk2(a[6], a[7])}; }
    }
    if constexpr (t == T_O) {
      const int reg = (l31 & 3) + 4 * (l31 >> 3), hh = (l31 >> 2) & 1;
      int d;
      if (reg >= 12) d = 20; else if (ch == 0) d = reg < 8 ? 8 * hh + reg : (hh == 0 ? 8 + reg : 20); else d = reg < 8 ? (hh == 0 ? 4 + reg : 12 + reg) : (hh == 0 ? reg - 8 : 20);
      const unsigned ka = L_KVS + ch * 2016 + d * 96;
      const s8 A0 = lds_b128(ka + h * 16), A1 = lds_b128(ka + 32 + h * 16);
      const u32x2 a2l = *(LAS u32x2*)(size_t)(ka + 64 + h * 8);
      const s8 A2 = __builtin_bit_cast(s8, (u32x4){a2l[0], a2l[1], 0u, 0u});
      f32x16 O;
#pragma unroll
      for (int r = 0; r < 16; ++r) O[r] = 0.f;
      O = MFMA32(A0, __builtin_bit_cast(s8, (u32x4){phq[0], phq[1], phq[2], phq[3]}), O);
      O = MFMA32(A1, __builtin_bit_cast(s8, (u32x4){phq[4], phq[5], phq[6], phq[7]}), O);
      O = MFMA32(A2, __builtin_bit_cast(s8, (u32x4){phq[8], phq[9], 0u, 0u}), O);
      float rc = __builtin_amdgcn_rcpf(O[12] + 1e-6f);
      asm volatile("s_nop 1" : "+v"(rc));
      const unsigned G0 = 192 + 5 * cg + (ch == 0 ? h : 3 + h);
      st_b128_sc1((u32x4){pk2(smul(O[0], rc), smul(O[1], rc)), pk2(smul(O[2], rc), smul(O[3], rc)), pk2(smul(O[4], rc), smul(O[5], rc)), pk2(smul(O[6], rc), smul(O[7], rc))}, rws, actbase + (G0 >> 2) * 1024 + (G0 & 3) * 256);
      const unsigned G2 = 192 + 5 * cg + 2;
      st_b64_sc1_m((u32x2){pk2(smul(O[8], rc), smul(O[9], rc)), pk2(smul(O[10], rc), smul(O[11], rc))}, rws, actbase + (G2 >> 2) * 1024 + (G2 & 3) * 256 + ch * 8, 0xffffffffull);
    }
    if constexpr (t == T_FO) st_b64_sc1_m(tag, rws, w == 0 ? WS_FO + (rb * 16 + cg) * 8 : OOB, M_ONE);
    if constexpr (t == T_PO) pf = POLL16(WS_FO + (r2 >> 1) * 128 + l * 8);
    if constexpr (t == T_AO) { lda(12); lda(13); lda(14); lda(15); lda(16); }
    if constexpr (t == 1) lda(10);
    if constexpr (t == 2) lda(11);
    if constexpr (t >= 1 && t + 5 < 17) issueB(t + 5);
    if constexpr (t == 14) {
#pragma unroll
      for (int z = 0; z < 3; ++z) { const int ix = kq + 4 * z; const bool v = ix < 10; const int a = ix >= 5 ? 1 : 0, c = ix - 5 * a;
        go[z] = v ? (unsigned)((64 * r2 + 32 * rh + 16 * a + (l & 15)) * 1280 + (80 * nb + 16 * c + 4 * (l >> 4)) * 2) : OOB; xr[z] = ld_b64_m(rx, go[z], v ? M_ALL : M_ONE); }
    }
    if constexpr (t == 12) WAITV(8);
    TIE(af[t % 10][0]); TIE(af[t % 10][1]);
    if constexpr (t == 0) {
#pragma unroll
      for (int c = 0; c < 5; ++c) bfr[0][c] = lds_b128(L_BRING + kq * 5120 + l * 16 + c * 1024);
    }
    SB();
#pragma unroll
    for (int c = 0; c < 5; ++c) {
      a2[0][c] = MFMA16(bfr[t & 1][c], af[t % 10][0], a2[0][c]);
      a2[1][c] = MFMA16(bfr[t & 1][c], af[t % 10][1], a2[1][c]);
      if constexpr (t + 1 < 17) bfr[(t + 1) & 1][c] = lds_b128(L_BRING + ((t + 1) % 6) * B_STAGE + kq * 5120 + l * 16 + c * 1024);
      SB();
    }
  });
  STAMP(6);
  STAMPL(50);
  LBAR();
  static_for<0, 10>([&](auto I_) { constexpr int ix = decltype(I_)::v;
    if ((ix & 3) != kq) *(LAS f32x4*)(size_t)(L_RED + ((rh * 10 + ix) * 3 + ((kq - ix - 1) & 3)) * 1024 + l * 16) = a2[ix / 5][ix % 5]; });
  asm volatile("s_waitcnt vmcnt(0) lgkmcnt(0)\n\ts_barrier" ::: "memory");
  auto fin = [&](auto I_, int z) { constexpr int ix = decltype(I_)::v;
    const unsigned ad = L_RED + (rh * 10 + ix) * 3 * 1024 + l * 16;
    const f32x4 v = (a2[ix / 5][ix % 5] + *(LAS f32x4*)(size_t)ad) + (*(LAS f32x4*)(size_t)(ad + 1024) + *(LAS f32x4*)(size_t)(ad + 2048));
    st_b64((u32x2){pk2(v[0] + bf_lo(xr[z][0]), v[1] + bf_hi(xr[z][0])), pk2(v[2] + bf_lo(xr[z][1]), v[3] + bf_hi(xr[z][1]))}, rout, go[z]); };
#pragma unroll
  for (int z = 0; z < 3; ++z) TIE(xr[z]);
  if (kq == 0) { fin(IC<0>{}, 0); fin(IC<4>{}, 1); fin(IC<8>{}, 2); }
  else if (kq == 1) { fin(IC<1>{}, 0); fin(IC<5>{}, 1); fin(IC<9>{}, 2); }
  else if (kq == 2) { fin(IC<2>{}, 0); fin(IC<6>{}, 1); }
  else { fin(IC<3>{}, 0); fin(IC<7>{}, 1); }
  asm volatile(".Lfk_end:");
  if (b == 0 && tid == 0) st_b64_sc1(tag, rws, WS_EPOCH);
  STAMPL(51);
  STAMP(7);
}

extern "C" void kernel_launch(void* const* d_in, const int* in_sizes, int n_in, void* d_out, int out_size, void* d_ws, size_t ws_size, hipStream_t stream) {
  (void)in_sizes; (void)n_in; (void)out_size; (void)ws_size;
  hipFuncSetAttribute((const void*)fused_k, hipFuncAttributeMaxDynamicSharedMemorySize, LDS_BYTES);
  fused_k<<<256, 512, LDS_BYTES, stream>>>((const u16*)d_in[0], (const u16*)d_in[1], (const u16*)d_in[2], (const u16*)d_in[3], (const u16*)d_in[5], (const u16*)d_in[6], (const u16*)d_in[7], (u16*)d_out, (char*)d_ws);
}
```

```cpp
#include <hip/hip_runtime.h>
#include <hip/hip_bf16.h>
typedef unsigned short u16;
typedef unsigned long long u64;
typedef short s4 __attribute__((ext_vector_type(4)));
typedef short s8 __attribute__((ext_vector_type(8)));
typedef __bf16 b8 __attribute__((ext_vector_type(8)));
typedef __bf16 b2 __attribute__((ext_vector_type(2)));
typedef float f32x16 __attribute__((ext_vector_type(16)));
typedef float f32x4 __attribute__((ext_vector_type(4)));
typedef float f32x2 __attribute__((ext_vector_type(2)));
typedef unsigned u32x4 __attribute__((ext_vector_type(4)));
typedef unsigned u32x2 __attribute__((ext_vector_type(2)));
#define LAS __attribute__((address_space(3)))
#define DI __device__ __forceinline__
typedef __amdgpu_buffer_rsrc_t rsrc_t;

constexpr int S = 2048, D = 640, F = 1536;
constexpr unsigned WS_EPOCH = 0;
constexpr unsigned WS_FP0 = 1024, WS_FP1 = 3072, WS_FH = 5120, WS_FO = 7168, WS_FKV = 9216, WS_FCS = 11264;
constexpr unsigned WS_STATS = 16384;
constexpr unsigned WS_KVP = 32768;
constexpr unsigned KVP_STRIDE = 1792;
constexpr unsigned WS_WQP = 1u << 20;
constexpr unsigned WS_W2P = 4u << 20;
constexpr unsigned WS_CSP = 7u << 20;
constexpr unsigned WS_ACT = 8u << 20;
constexpr unsigned WS_USED = 17u << 20;
constexpr unsigned OOB = 0x80000000u;
constexpr unsigned L_X = 0;
constexpr unsigned L_W = 49152;
constexpr unsigned W_STAGE = 24832, W_UOFF = 12320;
constexpr unsigned L_MISC = L_W + 4 * W_STAGE;
constexpr unsigned L_W2S = L_MISC;
constexpr unsigned L_CS = L_W2S;
constexpr unsigned L_WPF = L_W2S + 7168;
constexpr unsigned L_QRAW = L_W + 3 * W_STAGE;
constexpr unsigned L_WPR = L_QRAW + 12288;
constexpr unsigned L_W2A = L_QRAW + 14336;
constexpr unsigned L_BRING = 0;
constexpr unsigned B_STAGE = 20480;
constexpr unsigned L_KVR = 122880;
constexpr unsigned L_KVS = 149760;
constexpr unsigned L_DUMMY1 = L_QRAW + 23552;
constexpr unsigned L_DUMMY = L_W2S + 6144;
constexpr unsigned L_RED = 0;
constexpr unsigned LDS_BYTES = 160 * 1024;

DI rsrc_t mk_rsrc(const void* p, unsigned bytes) { return __builtin_amdgcn_make_buffer_rsrc((void*)p, 0, bytes, 0x00020000); }
DI void dma16(unsigned lds_addr, unsigned voff, rsrc_t r, unsigned soff) {
  asm volatile("s_mov_b32 m0, %0\n\ts_nop 0\n\tbuffer_load_dwordx4 %1, %2, %3 offen lds" ::"s"(lds_addr), "v"(voff), "s"(r), "s"(soff) : "memory");
}
DI void dma16_sc1(unsigned lds_addr, unsigned voff, rsrc_t r, unsigned soff) {
  asm volatile("s_mov_b32 m0, %0\n\ts_nop 0\n\tbuffer_load_dwordx4 %1, %2, %3 offen sc1 lds" ::"s"(lds_addr), "v"(voff), "s"(r), "s"(soff) : "memory");
}
DI unsigned ld_u16(rsrc_t r, unsigned voff, unsigned soff) { unsigned v; asm volatile("buffer_load_ushort %0, %1, %2, %3 offen" : "=v"(v) : "v"(voff), "s"(r), "s"(soff) : "memory"); return v; }
DI unsigned ld_b32(rsrc_t r, unsigned voff, unsigned soff) { unsigned v; asm volatile("buffer_load_dword %0, %1, %2, %3 offen" : "=v"(v) : "v"(voff), "s"(r), "s"(soff) : "memory"); return v; }
DI u32x2 ld_b64_sc1(rsrc_t r, unsigned voff, unsigned soff) { u32x2 v; asm volatile("buffer_load_dwordx2 %0, %1, %2, %3 offen sc1" : "=v"(v) : "v"(voff), "s"(r), "s"(soff) : "memory"); return v; }
DI u32x4 ld_b128_sc1(rsrc_t r, unsigned voff, unsigned soff) { u32x4 v; asm volatile("buffer_load_dwordx4 %0, %1, %2, %3 offen sc1" : "=v"(v) : "v"(voff), "s"(r), "s"(soff) : "memory"); return v; }
DI u32x2 ld_b64_m(rsrc_t r, unsigned voff, u64 mask) { u32x2 v; asm volatile("s_mov_b64 exec, %3\n\tbuffer_load_dwordx2 %0, %1, %2, 0 offen\n\ts_mov_b64 exec, -1" : "=v"(v) : "v"(voff), "s"(r), "s"(mask) : "memory"); return v; }
DI void st_b64(u32x2 v, rsrc_t r, unsigned voff) { asm volatile("buffer_store_dwordx2 %0, %1, %2, 0 offen" ::"v"(v), "v"(voff), "s"(r) : "memory"); }
DI u32x4 ld_b128(rsrc_t r, unsigned voff, unsigned soff) { u32x4 v; asm volatile("buffer_load_dwordx4 %0, %1, %2, %3 offen" : "=v"(v) : "v"(voff), "s"(r), "s"(soff) : "memory"); return v; }
DI void st_b128_sc1(u32x4 v, rsrc_t r, unsigned voff) {
 asm volatile("buffer_store_dwordx4 %0, %1, %2, 0 offen sc1\n\ts_nop 1" ::"v"(v), "v"(voff), "s"(r) : "memory"); }
DI void st_b64_sc1(u32x2 v, rsrc_t r, unsigned voff) { asm volatile("buffer_store_dwordx2 %0, %1, %2, 0 offen sc1" ::"v"(v), "v"(voff), "s"(r) : "memory"); }
DI void st_b128(u32x4 v, rsrc_t r, unsigned voff) { asm volatile("buffer_store_dwordx4 %0, %1, %2, 0 offen\n\ts_nop 1" ::"v"(v), "v"(voff), "s"(r) : "memory"); }
DI void st_b64_sc1_m(u32x2 v, rsrc_t r, unsigned voff, u64 mask) {
 asm volatile("s_mov_b64 exec, %3\n\tbuffer_store_dwordx2 %0, %1, %2, 0 offen sc1\n\ts_mov_b64 exec, -1" ::"v"(v), "v"(voff), "s"(r), "s"(mask) : "memory"); }
DI void st_b32_sc1_m(unsigned v, rsrc_t r, unsigned voff, u64 mask) { asm volatile("s_mov_b64 exec, %3\n\tbuffer_store_dword %0, %1, %2, 0 offen sc1\n\ts_mov_b64 exec, -1" ::"v"(v), "v"(voff), "s"(r), "s"(mask) : "memory"); }
DI u32x2 ld_b64_sc1_m(rsrc_t r, unsigned voff, u64 mask) { u32x2 v; asm volatile("s_mov_b64 exec, %3\n\tbuffer_load_dwordx2 %0, %1, %2, 0 offen sc1\n\ts_mov_b64 exec, -1" : "=v"(v) : "v"(voff), "s"(r), "s"(mask) : "memory"); return v; }
DI unsigned ld_b32_sc1_m(rsrc_t r, unsigned voff, unsigned soff, u64 mask) { unsigned v; asm volatile("s_mov_b64 exec, %4\n\tbuffer_load_dword %0, %1, %2, %3 offen sc1\n\ts_mov_b64 exec, -1" : "=v"(v) : "v"(voff), "s"(r), "s"(soff), "s"(mask) : "memory"); return v; }
DI u32x4 ld_b128_sc1_m(rsrc_t r, unsigned voff, unsigned soff, u64 mask) { u32x4 v; asm volatile("s_mov_b64 exec, %4\n\tbuffer_load_dwordx4 %0, %1, %2, %3 offen sc1\n\ts_mov_b64 exec, -1" : "=v"(v) : "v"(voff), "s"(r), "s"(soff), "s"(mask) : "memory"); return v; }
DI void st_b128_sc1_m(u32x4 v, rsrc_t r, unsigned voff, u64 mask) {
 asm volatile("s_mov_b64 exec, %3\n\tbuffer_store_dwordx4 %0, %1, %2, 0 offen sc1\n\ts_mov_b64 exec, -1\n\ts_nop 0" ::"v"(v), "v"(voff), "s"(r), "s"(mask) : "memory"); }
DI void dma16_m(unsigned lds_addr, unsigned voff, rsrc_t r, unsigned soff, u64 mask) {
  asm volatile("s_mov_b32 m0, %0\n\ts_mov_b64 exec, %4\n\tbuffer_load_dwordx4 %1, %2, %3 offen lds\n\ts_mov_b64 exec, -1" ::"s"(lds_addr), "v"(voff), "s"(r), "s"(soff), "s"(mask) : "memory");
}
#define POLL16(off) ld_b64_sc1_m(rws, w == 0 ? (off) : OOB, w == 0 ? 0xffffull : M_ONE)
#define TIE(x) asm volatile("" : "+v"(x))
#define WAITV(n) asm volatile("s_waitcnt vmcnt(" #n ")" ::: "memory")
#define WAITV_BAR(n) asm volatile("s_waitcnt vmcnt(" #n ")\n\ts_barrier" ::: "memory")
#define WAITVL_BAR(n) asm volatile("s_waitcnt vmcnt(" #n ") lgkmcnt(0)\n\ts_barrier" ::: "memory")
#define LBAR() asm volatile("s_waitcnt lgkmcnt(0)\n\ts_barrier" ::: "memory")
#define SB() __builtin_amdgcn_sched_barrier(0)
#define MFMA32(a, b, c) __builtin_amdgcn_mfma_f32_32x32x16_bf16(__builtin_bit_cast(b8, (a)), __builtin_bit_cast(b8, (b)), (c), 0, 0, 0)
#define MFMA16(a, b, c) __builtin_amdgcn_mfma_f32_16x16x32_bf16(__builtin_bit_cast(b8, (a)), __builtin_bit_cast(b8, (b)), (c), 0, 0, 0)
DI s8 lds_b128(unsigned addr) { return *(LAS s8*)(size_t)addr; }
DI s8 tr_frag(unsigned addr, unsigned off2) {
  s4 a = __builtin_amdgcn_ds_read_tr16_b64_v4i16((LAS s4*)(size_t)addr);
  s4 b = __builtin_amdgcn_ds_read_tr16_b64_v4i16((LAS s4*)(size_t)(addr + off2));
  return __builtin_shufflevector(a, b, 0, 1, 2, 3, 4, 5, 6, 7);
}
DI float dot_ones(s8 f, float acc) {
  u32x4 u = __builtin_bit_cast(u32x4, f);
#pragma unroll
  for (int i = 0; i < 4; ++i) { const unsigned t = u[i]; acc = __builtin_amdgcn_fdot2_f32_bf16(__builtin_bit_cast(b2, t), __builtin_bit_cast(b2, 0x3f803f80u), acc, false); }
  asm volatile("" : "+v"(acc));
  return acc;
}
DI unsigned pk2(float lo, float hi) { b2 c; c[0] = (__bf16)lo; c[1] = (__bf16)hi; return __builtin_bit_cast(unsigned, c); }
DI float bf_lo(unsigned u) { return __uint_as_float(u << 16); }
DI float bf_hi(unsigned u) { return __uint_as_float(u & 0xffff0000u); }
DI float swap_add(float v) { unsigned u = __float_as_uint(v); auto r = __builtin_amdgcn_permlane32_swap(u, u, false, false); return __uint_as_float(r[0]) + __uint_as_float(r[1]); }
DI float swap_max(float v) { unsigned u = __float_as_uint(v); auto r = __builtin_amdgcn_permlane32_swap(u, u, false, false); return fmaxf(__uint_as_float(r[0]), __uint_as_float(r[1])); }
DI float swap_min(float v) { unsigned u = __float_as_uint(v); auto r = __builtin_amdgcn_permlane32_swap(u, u, false, false); return fminf(__uint_as_float(r[0]), __uint_as_float(r[1])); }
DI float sadd(float a, float b) { float r; asm("v_add_f32 %0, %1, %2" : "=v"(r) : "v"(a), "v"(b)); return r; }
DI float smul(float a, float b) { float r; asm("v_mul_f32 %0, %1, %2" : "=v"(r) : "v"(a), "v"(b)); return r; }
DI float sfma(float a, float b, float c) { float r; asm("v_fma_f32 %0, %1, %2, %3" : "=v"(r) : "v"(a), "v"(b), "v"(c)); return r; }
DI float dpp_add(float v, const int ctrl_sel) {
  const int u = __float_as_int(v); int r;
  if (ctrl_sel == 0) r = __builtin_amdgcn_update_dpp(0, u, 0xB1, 0xf, 0xf, true);
  else if (ctrl_sel == 1) r = __builtin_amdgcn_update_dpp(0, u, 0x4E, 0xf, 0xf, true);
  else if (ctrl_sel == 2) r = __builtin_amdgcn_update_dpp(0, u, 0x141, 0xf, 0xf, true);
  else r = __builtin_amdgcn_update_dpp(0, u, 0x140, 0xf, 0xf, true);
  return v + __int_as_float(r);
}
DI float wave_sum(float v) {
  v = dpp_add(v, 0); v = dpp_add(v, 1); v = dpp_add(v, 2); v = dpp_add(v, 3);
  { unsigned u = __float_as_uint(v); auto r = __builtin_amdgcn_permlane16_swap(u, u, false, false); v = __uint_as_float(r[0]) + __uint_as_float(r[1]); }
  return swap_add(v);
}
DI float sum8(s8 f, float acc) {
  const u32x4 u = __builtin_bit_cast(u32x4, f);
#pragma unroll
  for (int i = 0; i < 4; ++i) { const unsigned t = u[i]; acc += __uint_as_float(t << 16); acc += __uint_as_float(t & 0xffff0000u); }
  asm volatile("" : "+v"(acc));
  return acc;
}
DI s8 pack_step(const f32x16& x, int s) {
  return __builtin_bit_cast(s8, (u32x4){pk2(x[8 * s], x[8 * s + 1]), pk2(x[8 * s + 2], x[8 * s + 3]), pk2(x[8 * s + 4], x[8 * s + 5]), pk2(x[8 * s + 6], x[8 * s + 7])});
}
template <int I> struct IC { static constexpr int v = I; };
template <int B, int E, class Fn> DI void static_for(Fn&& f) { if constexpr (B < E) { f(IC<B>{}); static_for<B + 1, E>(f); } }
DI bool flags_ok(u32x2 v, u32x2 tag, bool act) { return __all(!act || (v[0] == tag[0] && v[1] == tag[1])); }
DI void wait_flags(rsrc_t rws, unsigned base, unsigned stride, int n, u32x2 tag, int l) {

  const bool act = l < n; const unsigned off = act ? base + l * stride : OOB;
#pragma nounroll
  for (int it = 0; it < (1 << 17); ++it) {
    u32x2 v; asm volatile("buffer_load_dwordx2 %0, %1, %2, 0 offen sc1\n\ts_waitcnt vmcnt(0)" : "=&v"(v) : "v"(off), "s"(rws) : "memory");
    if (flags_ok(v, tag, act)) return;
    __builtin_amdgcn_s_sleep(1);
  }
}
DI void wait_flags256(rsrc_t rws, unsigned base, u32x2 tag, int l) {
#pragma nounroll
  for (int it = 0; it < (1 << 17); ++it) {
    u32x4 a, b;
    asm volatile("buffer_load_dwordx4 %0, %2, %3, 0 offen sc1\n\tbuffer_load_dwordx4 %1, %2, %3, 0 offen offset:16 sc1\n\ts_waitcnt vmcnt(0)" : "=&v"(a), "=&v"(b) : "v"(base + l * 32), "s"(rws) : "memory");
    const bool ok = a[0] == tag[0] && a[1] == tag[1] && a[2] == tag[0] && a[3] == tag[1] && b[0] == tag[0] && b[1] == tag[1] && b[2] == tag[0] && b[3] == tag[1];
    if (__all(ok)) return;
    __builtin_amdgcn_s_sleep(1);
  }
}
DI bool flags256_ok(u32x4 a, u32x4 b, u32x2 tag) {
  return __all(a[0] == tag[0] && a[1] == tag[1] && a[2] == tag[0] && a[3] == tag[1] && b[0] == tag[0] && b[1] == tag[1] && b[2] == tag[0] && b[3] == tag[1]);
}
DI float max3(float a, float b, float c) { float r; asm("v_max3_f32 %0, %1, %2, %3" : "=v"(r) : "v"(a), "v"(b), "v"(c)); return r; }
DI float min3(float a, float b, float c) { float r; asm("v_min3_f32 %0, %1, %2, %3" : "=v"(r) : "v"(a), "v"(b), "v"(c)); return r; }
DI f32x2 exp2_2(f32x2 a) { f32x2 r; r[0] = __builtin_amdgcn_exp2f(a[0]); r[1] = __builtin_amdgcn_exp2f(a[1]); return r; }
DI unsigned pk2v(f32x2 a) { return pk2(a[0], a[1]); }
DI void phi_map1(const f32x16& pin, float sc, f32x2 (&ep)[5], f32x2 (&en)[5]) {
  float p[10];
#pragma unroll
  for (int r = 0; r < 10; ++r) p[r] = pin[r];
  asm volatile("s_nop 7\n\ts_nop 3" : "+v"(p[0]), "+v"(p[1]), "+v"(p[2]), "+v"(p[3]), "+v"(p[4]), "+v"(p[5]), "+v"(p[6]), "+v"(p[7]), "+v"(p[8]), "+v"(p[9]));
  float mx = max3(p[0], p[1], p[2]), mn = min3(p[0], p[1], p[2]);
#pragma unroll
  for (int r = 3; r < 9; r += 2) { mx = max3(mx, p[r], p[r + 1]); mn = min3(mn, p[r], p[r + 1]); }
  mx = max3(mx, p[9], p[9]); mn = min3(mn, p[9], p[9]);
  { unsigned u = __float_as_uint(mx); auto r = __builtin_amdgcn_permlane32_swap(u, u, false, false); mx = max3(__uint_as_float(r[0]), __uint_as_float(r[1]), __uint_as_float(r[1])); }
  { unsigned u = __float_as_uint(mn); auto r = __builtin_amdgcn_permlane32_swap(u, u, false, false); mn = min3(__uint_as_float(r[0]), __uint_as_float(r[1]), __uint_as_float(r[1])); }
  const f32x2 s2 = {sc, sc}, bp = {-mx * sc, -mx * sc}, bn = {mn * sc, mn * sc};
#pragma unroll
  for (int r = 0; r < 5; ++r) { const f32x2 v = {p[2 * r], p[2 * r + 1]}; ep[r] = exp2_2(__builtin_elementwise_fma(v, s2, bp)); en[r] = exp2_2(__builtin_elementwise_fma(v, -s2, bn)); }
}
DI void phi_map2(const f32x2 (&ep)[5], const f32x2 (&en)[5], unsigned (&o)[10]) {
  const f32x2 sp2 = (ep[0] + ep[1]) + (ep[2] + ep[3]) + ep[4], sn2 = (en[0] + en[1]) + (en[2] + en[3]) + en[4];
  const float rp = __builtin_amdgcn_rcpf(swap_add(sp2[0] + sp2[1])), rn = __builtin_amdgcn_rcpf(swap_add(sn2[0] + sn2[1]));
  const f32x2 rp2 = {rp, rp}, rn2 = {rn, rn};
#pragma unroll
  for (int r = 0; r < 5; ++r) { o[r] = pk2v(ep[r] * rp2); o[5 + r] = pk2v(en[r] * rn2); }
}

#define STAMP(i)
#define STAMPL(k)
#define STAMPC(i)
#define STAMPF(i)

__global__ __launch_bounds__(512, 2) void fused_k(const u16* __restrict__ x, const u16* __restrict__ wqkv, const u16* __restrict__ wphi, const u16* __restrict__ wo,
                                                   const u16* __restrict__ wg, const u16* __restrict__ wu, const u16* __restrict__ wd, u16* __restrict__ out, char* __restrict__ ws) {
  extern __shared__ __attribute__((aligned(256))) char smem[];
  const int tid = threadIdx.x; const int l = tid & 63; const int w = __builtin_amdgcn_readfirstlane(tid >> 6);
  const int tg = w & 3, ch = w >> 2; const int l31 = l & 31, h = l >> 5;
  const int b = blockIdx.x, xcd = b & 7, idx = b >> 3;
  const int rb = (xcd & 1) * 8 + (idx & 7), cg = (xcd >> 1) * 4 + (idx >> 3);
  const int r2 = (xcd & 3) * 8 + (idx & 7), nb = (xcd >> 2) * 4 + (idx >> 3);
  const int klo = (5 * rb) >> 1, kcnt = ((5 * (rb + 1)) >> 1) - klo, rot = klo >> 2, kloc = klo - 4 * rot;
  STAMP(0);
  STAMPL(0);
  const rsrc_t rx = mk_rsrc(x, S * D * 2);
  const rsrc_t rw = mk_rsrc(w < 4 ? wg : wu, D * F * 2);
  const rsrc_t rws = mk_rsrc(ws, WS_USED);
  const u64 M_ALL = ~0ull, M_ONE = 1ull;
  auto sig = [&](int s) -> unsigned { const int t = s + rot; return t >= 10 ? t - 10 : t; };
  unsigned vx[2], vw[3];
#pragma unroll
  for (int i = 0; i < 2; ++i) { const int r = 8 * (2 * w + i) + (l >> 3); const int c = (l & 7) ^ ((r >> 1) & 7); vx[i] = (128 * rb + r) * 1280 + c * 16; }
#pragma unroll
  for (int i = 0; i < 3; ++i) { const int p = 64 * (3 * (w & 3) + i) + l; const int k = p / 12, c = p % 12; vw[i] = k * 3072 + cg * 192 + c * 16; }
  const unsigned lx0 = L_X + 2 * w * 1024;
  const unsigned lw0 = L_W + (w < 4 ? 0 : W_UOFF) + 3 * (w & 3) * 1024;
  auto dmax = [&](int s, int i) { dma16(lx0 + (s % 3) * 16384 + i * 1024, vx[i], rx, sig(s) * 128); };
  auto dmaw = [&](int s, int i) { dma16(lw0 + (s & 3) * W_STAGE + i * 1024, vw[i], rw, sig(s) * (64 * 3072)); };
  auto issue1 = [&](int s) { dmax(s, 0); dmax(s, 1); dmaw(s, 0); dmaw(s, 1); dmaw(s, 2); };
  issue1(0);
  u32x2 ep = ld_b64_sc1_m(rws, WS_EPOCH, M_ONE);
  const int srow = 128 * rb + 8 * cg + w;
  const bool v3 = kcnt == 3 && w < 4;
  u32x4 sx0, sx1;
  {
    const rsrc_t rq = mk_rsrc(wqkv, D * 1920 * 2);
    const int p = 64 * (w & 3) + l; const int k = p / 15, c = p % 15;
    const unsigned vo = p < 240 ? (unsigned)(k * 3840 + (c / 5) * 1280 + cg * 80 + (c % 5) * 16) : OOB;
    dma16(L_QRAW + w * 1024, vo, rq, (16 * (klo + (w >> 2))) * 3840);
    dma16_m(v3 ? L_QRAW + 8192 + w * 1024 : L_DUMMY1, v3 ? vo : OOB, rq, v3 ? (16 * (klo + 2)) * 3840 : 0, v3 ? M_ALL : M_ONE);
    const rsrc_t rp = mk_rsrc(wphi, 32 * 400 * 2);
    dma16_m(w < 2 ? L_WPR + w * 1024 : L_DUMMY1, (w < 2 && w * 64 + l < 100) ? (unsigned)(cg * 1600 + (w * 64 + l) * 16) : OOB, rp, 0, w == 0 ? M_ALL : (w == 1 ? 0xfffffffffull : M_ONE));
    sx0 = ld_b128(rx, srow * 1280 + l * 16, 0);
    sx1 = ld_b128(rx, l < 16 ? (unsigned)(srow * 1280 + 1024 + l * 16) : OOB, 0);
    const int kst = b / 5, cb = b % 5; const bool isd = kst < 48;
    const rsrc_t rr = mk_rsrc(isd ? wd : wo, isd ? F * D * 2 : D * D * 2);
    dma16(L_W2A + w * 1024, (unsigned)((l >> 4) * 1280 + (l & 15) * 16), rr, ((isd ? 32 * kst : 32 * (kst - 48)) + 4 * w) * 1280 + cb * 256);
  }
  const int gi = l >> 4, q4 = (l >> 2) & 3, p4 = l & 3;
  issue1(1); issue1(2);
  {
    u64 pc; unsigned len;
    asm volatile("s_getpc_b64 %0\n.Lfk_here:\n\ts_mov_b32 %1, .Lfk_end-.Lfk_here" : "=s"(pc), "=s"(len));
    const rsrc_t rc = mk_rsrc((const void*)pc, len);
    const unsigned off = (idx * 8 + w) * 256;
    asm volatile("s_mov_b32 m0, %0\n\ts_mov_b64 exec, 0xffff\n\tbuffer_load_dwordx4 %1, %2, 0 offen lds\n\ts_mov_b64 exec, -1" ::"s"(L_DUMMY1), "v"(off + l * 16), "s"(rc) : "memory");
  }
  WAITV_BAR(11);
  STAMP(1);
  STAMPL(1);
  TIE(ep);
  u32x2 tag;
  { const unsigned lo = __builtin_amdgcn_readfirstlane(ep[0]), hi = __builtin_amdgcn_readfirstlane(ep[1]); tag[0] = lo + 1u; tag[1] = hi + (lo == 0xffffffffu ? 1u : 0u); }
  const int shc = (w >> 1) & 1, sht = w & 1;
  s8 sf0, sf1, sf2; unsigned se[8]; unsigned smA, smB;
  {
    const int Q = 4 * (gi & 1) + p4;
    int seg, qd;
    if (sht == 0) { seg = Q < 5 ? 0 : 1; qd = Q < 5 ? Q : Q - 5; } else { seg = Q < 2 ? 1 : 2; qd = Q < 2 ? 3 + Q : (Q < 7 ? Q - 2 : 4); }
    const unsigned lo = L_QRAW + (8 * h + q4) * 240 + seg * 80 + shc * 40 + qd * 8;
    sf0 = tr_frag(lo + (w >> 2) * 4096, 960); sf1 = tr_frag(lo + 8192, 960);
    sf2 = tr_frag(L_W2A + (8 * gi + q4) * 256 + w * 32 + p4 * 8, 1024);
    const int reg = (l31 & 3) + 4 * (l31 >> 3), hh = (l31 >> 2) & 1;
    const int d0 = (tg == 0 ? 0 : tg == 1 ? 16 : tg == 2 ? -4 : 12) + 4 * h;
    const unsigned wa = L_WPR + ch * 800 + (10 * hh + reg) * 2 + d0 * 40;
#pragma unroll
    for (int j = 0; j < 8; ++j) se[j] = *(LAS u16*)(size_t)(wa + (8 * (j >> 2) + (j & 3)) * 40);
    smA = (reg < 10 && (unsigned)d0 < 20u) ? ~0u : 0u; smB = (reg < 10 && (unsigned)(d0 + 8) < 20u) ? ~0u : 0u;
  }
  auto side = [&](int part) {
    if (part == 0) {
      TIE(sx0); TIE(sx1);
      float sm = 0.f, sq = 0.f;
#pragma unroll
      for (int i = 0; i < 4; ++i) { const unsigned u0 = sx0[i], u1 = sx1[i];
        sm = __builtin_amdgcn_fdot2_f32_bf16(__builtin_bit_cast(b2, u0), __builtin_bit_cast(b2, 0x3f803f80u), sm, false); sq = __builtin_amdgcn_fdot2_f32_bf16(__builtin_bit_cast(b2, u0), __builtin_bit_cast(b2, u0), sq, false);
        sm = __builtin_amdgcn_fdot2_f32_bf16(__builtin_bit_cast(b2, u1), __builtin_bit_cast(b2, 0x3f803f80u), sm, false); sq = __builtin_amdgcn_fdot2_f32_bf16(__builtin_bit_cast(b2, u1), __builtin_bit_cast(b2, u1), sq, false); }
      const float m = wave_sum(sm) * (1.f / D);
      const float var = fmaxf(wave_sum(sq) * (1.f / D) - m * m, 0.f);
      st_b64_sc1_m((u32x2){__float_as_uint(m), __float_as_uint(__builtin_amdgcn_rsqf(var + 1e-5f))}, rws, WS_STATS + srow * 8, M_ONE);
    } else if (part == 1) {
      st_b128_sc1(__builtin_bit_cast(u32x4, sf0), rws, WS_WQP + (((2 * cg + shc) * 40 + klo + (w >> 2)) * 2 + sht) * 1024 + l * 16);
      st_b128_sc1_m(__builtin_bit_cast(u32x4, sf1), rws, v3 ? WS_WQP + (((2 * cg + shc) * 40 + klo + 2) * 2 + sht) * 1024 + l * 16 : OOB, v3 ? M_ALL : M_ONE);
      const int kst = b / 5, cb = b % 5, ct = 8 * cb + w;
      st_b128_sc1(__builtin_bit_cast(u32x4, sf2), rws, WS_W2P + (((ct / 5) * 68 + kst) * 5 + ct % 5) * 1024 + l * 16);
      const float c0 = swap_add(dot_ones(sf0, 0.f)), c1 = swap_add(dot_ones(sf1, 0.f));
      if (h == 0) { const unsigned qa = L_W2S + ((shc * 2 + sht) * 32 + l31) * 16 + (w >> 2) * 4; *(LAS float*)(size_t)qa = c0; *(LAS float*)(size_t)(qa + 8) = v3 ? c1 : 0.f; }
    } else {
      *(LAS u32x4*)(size_t)(L_WPF + w * 1024 + l * 16) = (u32x4){(se[0] | (se[1] << 16)) & smA, (se[2] | (se[3] << 16)) & smA, (se[4] | (se[5] << 16)) & smB, (se[6] | (se[7] << 16)) & smB};
    }
  };
  STAMPF(8);
  const unsigned qoff = (p4 == 1) ? 8 : (p4 == 2) ? 4 : (p4 == 3) ? 12 : 0;
  const unsigned wl = L_W + ((gi & 1) ? W_UOFF : 0) + (8 * h + q4) * 192 + 2 * qoff + 96 * ch;
  const unsigned wl2 = wl + 2 * W_STAGE;
  const int xrow = 32 * tg + l31;
  unsigned xa[4];
#pragma unroll
  for (int j = 0; j < 4; ++j) { xa[j] = (L_X + xrow * 128 + ((h ^ ((xrow >> 1) & 7)) << 4)) ^ (j * 32); asm volatile("" : "+v"(xa[j])); }
  s8 XF[40];
  f32x16 acc[3]; float cs[3];
#pragma unroll
  for (int i = 0; i < 3; ++i) { cs[i] = 0.f;
#pragma unroll
    for (int r = 0; r < 16; ++r) acc[i][r] = 0.f; }
  s8 wf[2][3];
  auto rdx = [&](int s, int j) { XF[4 * s + j] = lds_b128(xa[j] + (s % 3) * 16384); };
  auto rdw = [&](int n, int i) { const int s = n >> 2, j = n & 3; wf[n & 1][i] = tr_frag(((s & 2) ? wl2 : wl) + (s & 1) * W_STAGE + j * 3072 + i * 32, 768); };
  const unsigned vq2 = WS_WQP + ((2 * cg + ch) * 40 + tg) * 2048 + l * 16;
  auto slot2 = [&](int q) -> unsigned { return q == 0 || q == 7 ? 16384u : q == 1 || q == 8 ? L_W + 2 * W_STAGE : q == 2 || q == 9 ? 32768u : q == 3 ? L_W + 3 * W_STAGE : q == 4 ? 0u : q == 5 ? L_W : L_W + W_STAGE; };
  auto issue2 = [&](int q) { const unsigned so = sig(q) * 8192; dma16_sc1(slot2(q) + w * 2048, vq2, rws, so); dma16_sc1(slot2(q) + w * 2048 + 1024, vq2, rws, so + 1024); };
  const bool hv = b < 168 && w < 4; const int hu = 256 + (b >> 1), hkst = hu / 5, hcb = hu % 5;
  u32x2 pf, pf2, st; u32x4 pl0, pl1, cg4[4]; unsigned cg1[4]; f32x4 qs0, qs1;
  rdx(0, 0); rdw(0, 0); rdw(0, 1); rdw(0, 2); rdx(0, 1); rdx(0, 2); rdx(0, 3);
  static_for<0, 40>([&](auto N_) {
    constexpr int n = decltype(N_)::v;
    constexpr int s = n >> 2, j = n & 3;
    if constexpr (n == 3) STAMPF(9);
    if constexpr (n == 11) STAMPF(10);
    if constexpr (n == 19) STAMPF(11);
    if constexpr (n == 27) STAMPF(12);
    if constexpr (n == 35) STAMPF(13);
    if constexpr (j == 3 && s < 9) STAMPL(2 + 2 * s);
    if constexpr (n == 3) WAITV_BAR(10);
    if constexpr (n == 7) { WAITV_BAR(9); qs0 = *(LAS f32x4*)(size_t)(L_W2S + ((ch * 2) * 32 + l31) * 16); qs1 = *(LAS f32x4*)(size_t)(L_W2S + ((ch * 2 + 1) * 32 + l31) * 16); }
    if constexpr (n == 11) { WAITV_BAR(0); st_b64_sc1_m(tag, rws, w == 0 ? WS_FP0 + (cg * 16 + rb) * 8 : OOB, M_ONE); }
    if constexpr (n == 15) WAITV_BAR(6);
    if constexpr (n == 19) WAITV_BAR(6);
    if constexpr (n == 23) { WAITV_BAR(5); pf = POLL16(WS_FP0 + cg * 128 + l * 8); pf2 = POLL16(WS_FP0 + rb * 8 + (l & 15) * 128); }
    if constexpr (n == 27) {
      WAITV(6); TIE(pf); TIE(pf2);
      if (w == 0) {
        if (__builtin_expect(!flags_ok(pf, tag, l < 16), 0)) wait_flags(rws, WS_FP0 + cg * 128, 8, 16, tag, l);
        if (__builtin_expect(!flags_ok(pf2, tag, l < 16), 0)) wait_flags(rws, WS_FP0 + rb * 8, 128, 16, tag, l);
      }
      asm volatile("s_barrier" ::: "memory");
      st = ld_b64_sc1(rws, WS_STATS + (128 * rb + xrow) * 8, 0);
      {
        const unsigned vo = w < 4 ? WS_CSP + (cg * 16 + 4 * w) * 1280 + l * 16 : OOB, vo1 = w < 4 ? WS_CSP + (cg * 16 + 4 * w) * 1280 + 1024 + l * 4 : OOB;
#pragma unroll
        for (int r = 0; r < 4; ++r) { cg4[r] = ld_b128_sc1_m(rws, vo, r * 1280, w < 4 ? M_ALL : M_ONE); cg1[r] = ld_b32_sc1_m(rws, vo1, r * 1280, w < 4 ? M_ALL : M_ONE); } }
    }
    if constexpr (n == 31) {
      WAITV_BAR(14);
      st_b64_sc1_m(tag, rws, w == 0 ? WS_FP1 + b * 8 : OOB, M_ONE);
      issue2(0); issue2(1);
    }
    if constexpr (n == 35) {
      WAITV_BAR(5);
#pragma unroll
      for (int r = 0; r < 4; ++r) { TIE(cg4[r]); TIE(cg1[r]); }
      if (w < 4) { const unsigned ad = L_CS + ((h * 5) * 32 + l31) * 16 + w * 4;
#pragma unroll
        for (int e = 0; e < 4; ++e) *(LAS float*)(size_t)(ad + e * 512) = (__uint_as_float(cg4[0][e]) + __uint_as_float(cg4[1][e])) + (__uint_as_float(cg4[2][e]) + __uint_as_float(cg4[3][e]));
        *(LAS float*)(size_t)(ad + 4 * 512) = (__uint_as_float(cg1[0]) + __uint_as_float(cg1[1])) + (__uint_as_float(cg1[2]) + __uint_as_float(cg1[3])); }
      issue2(2); issue2(3);
    }
    if constexpr (j == 3 && s < 9) STAMPL(3 + 2 * s);
    SB();
#pragma unroll
    for (int i = 0; i < 3; ++i) {
      acc[i] = MFMA32(wf[n & 1][i], XF[n], acc[i]);
      SB();
      if (i == 0 && j == 3 && s < 9) rdx(s + 1, 0);
      if (n + 1 < 40) rdw(n + 1, i);
      if (j == 0 && s > 0) rdx(s, i + 1);
      if (n < 3 && i == 2) side(n);
      if (n < 8 && i == 2) { if ((unsigned)(n - kloc) < (unsigned)kcnt && tg == 0) { cs[0] = dot_ones(wf[n & 1][0], cs[0]); cs[1] = dot_ones(wf[n & 1][1], cs[1]); cs[2] = dot_ones(wf[n & 1][2], cs[2]); } }
      if (n == 8 && i == 0) {
        const unsigned dst = tg == 0 ? WS_CSP + (cg * 16 + rb) * 1280 + (ch * 32 + l31) * 16 : OOB; const u64 mk = tg == 0 ? 0xffffffffull : M_ONE;
        st_b128_sc1_m((u32x4){__float_as_uint(swap_add(cs[0])), __float_as_uint(swap_add(cs[1])), __float_as_uint(swap_add(cs[2])), __float_as_uint((qs0[0] + qs0[1]) + (qs0[2] + qs0[3]))}, rws, dst, mk);
        st_b32_sc1_m(__float_as_uint((qs1[0] + qs1[1]) + (qs1[2] + qs1[3])), rws, tg == 0 ? WS_CSP + (cg * 16 + rb) * 1280 + 1024 + (ch * 32 + l31) * 4 : OOB, mk);
      }
      if (j == 3 && s + 3 < 10) {
        if (i == 0) { dmax(s + 3, 0); dmax(s + 3, 1); }
        if (i == 1) { dmaw(s + 3, 0); dmaw(s + 3, 1); }
        if (i == 2) dmaw(s + 3, 2);
      }
      if constexpr (n == 15) { if (i == 2) {
        const rsrc_t rr = mk_rsrc(wo, D * D * 2);
        dma16_m(hv ? L_W2S + w * 1024 : L_DUMMY, hv ? (unsigned)((l >> 3) * 1280 + (l & 7) * 16) : OOB, rr, hv ? (32 * (hkst - 48) + 8 * w) * 1280 + hcb * 256 + (b & 1) * 128 : 0, hv ? M_ALL : M_ONE);
      } }
      if constexpr (n == 23) { if (i == 2) {
        const int ct = 8 * hcb + 4 * (b & 1) + (w & 3);
        const s8 f = tr_frag(L_W2S + (8 * gi + q4) * 128 + (w & 3) * 32 + p4 * 8, 512);
        st_b128_sc1_m(__builtin_bit_cast(u32x4, f), rws, hv ? WS_W2P + (((ct / 5) * 68 + hkst) * 5 + ct % 5) * 1024 + l * 16 : OOB, hv ? M_ALL : M_ONE);
      } }
      SB();
    }
  });
  STAMP(2);
  STAMPL(20);
  WAITVL_BAR(0);
  TIE(st);
  const float mean = __uint_as_float(st[0]), rstd = __uint_as_float(st[1]);
  s8 bm;
  { const float nm = -mean; const unsigned hi = pk2(nm, 0.f) & 0xffffu; const float lo = nm - __uint_as_float(hi << 16); unsigned mpk = hi | (pk2(lo, 0.f) << 16); if (h) mpk = 0; bm = __builtin_bit_cast(s8, (u32x4){mpk, mpk, 0u, 0u}); }
  auto cs_frag = [&](int tile) -> s8 {
    const f32x4 pp = *(LAS f32x4*)(size_t)(L_CS + ((ch * 5 + tile) * 32 + l31) * 16);
    const float c = (pp[0] + pp[1]) + (pp[2] + pp[3]);
    unsigned hi = pk2(c, c); const float lo = c - __uint_as_float(hi << 16); unsigned lo2 = pk2(lo, lo);
    if (h) { hi = 0; lo2 = 0; }
    return __builtin_bit_cast(s8, (u32x4){hi, lo2, 0u, 0u});
  };
  const unsigned tok = 128 * rb + xrow;
  const unsigned actbase = WS_ACT + (tok >> 4) * 68 * 1024 + (l & 15) * 16;
  {
    const float gs = -1.4426950408889634f * rstd, irs = __builtin_amdgcn_rcpf(rstd);
#pragma unroll
    for (int i = 0; i < 3; ++i) acc[i] = MFMA32(cs_frag(i), bm, acc[i]);
#pragma unroll
    for (int i = 0; i < 3; ++i) {
      issue2(4 + i);
      const f32x2 gs2 = {gs, gs}, irs2 = {irs, irs};
      u32x4 ho;
#pragma unroll
      for (int r = 0; r < 4; ++r) {
        const f32x2 g2 = {acc[i][2 * r], acc[i][2 * r + 1]}, u2 = {acc[i][8 + 2 * r], acc[i][9 + 2 * r]};
        const f32x2 dn = __builtin_elementwise_fma(exp2_2(g2 * gs2), irs2, irs2);
        const f32x2 rc = {__builtin_amdgcn_rcpf(dn[0]), __builtin_amdgcn_rcpf(dn[1])};
        ho[r] = pk2v(u2 * rc);
      }
      const unsigned G = 12 * cg + 6 * ch + 2 * i + h;
      st_b128_sc1(ho, rws, actbase + (G >> 2) * 1024 + (G & 3) * 256);
      SB();
    }
  }
  STAMP(3);
  STAMPL(21);
  f32x16 T[2];
#pragma unroll
  for (int i = 0; i < 2; ++i)
#pragma unroll
    for (int r = 0; r < 16; ++r) T[i][r] = 0.f;
  s8 qf[3][2];
  const unsigned ql = ch * 8192 + l * 16, ql2 = ql + L_W;
  auto rdq = [&](int m, int t) { const int q = m >> 2, j = m & 3; const unsigned sl = slot2(q); qf[m % 3][t] = lds_b128((sl >= L_W ? ql2 + (sl - L_W) : ql + sl) + j * 2048 + t * 1024); };
  rdq(0, 0); rdq(0, 1); rdq(1, 0); rdq(1, 1);
  static_for<0, 40>([&](auto M_) {
    constexpr int m = decltype(M_)::v;
    if constexpr (m == 6) STAMPL(22);
    if constexpr (m == 14) STAMPL(23);
    if constexpr (m == 15) STAMPL(24);
    if constexpr (m == 26) STAMPL(25);
    if constexpr (m == 34) STAMPL(26);
    if constexpr (m == 14) { WAITV_BAR(0); st_b64_sc1_m(tag, rws, w == 0 ? WS_FH + (rb * 16 + cg) * 8 : OOB, M_ONE); issue2(7); issue2(8); issue2(9); }
    if constexpr (m == 26) {
      WAITV_BAR(0);
      pl0 = ld_b128_sc1_m(rws, WS_FP1 + l * 32, 0, w == 0 ? M_ALL : M_ONE); pl1 = ld_b128_sc1_m(rws, WS_FP1 + l * 32 + 16, 0, w == 0 ? M_ALL : M_ONE);
      pf = POLL16(WS_FH + (r2 >> 1) * 128 + l * 8);
    }
    if constexpr (m == 38) {
      WAITV(0); TIE(pl0); TIE(pl1); TIE(pf);
      if (w == 0) {
        if (__builtin_expect(!flags256_ok(pl0, pl1, tag), 0)) wait_flags256(rws, WS_FP1, tag, l);
        if (__builtin_expect(!flags_ok(pf, tag, l < 16), 0)) wait_flags(rws, WS_FH + (r2 >> 1) * 128, 8, 16, tag, l);
      }
    }
    SB();
#pragma unroll
    for (int t = 0; t < 2; ++t) {
      T[t] = MFMA32(qf[m % 3][t], XF[m], T[t]);
      SB();
      if (m + 2 < 40) rdq(m + 2, t);
      SB();
    }
  });
  STAMP(4);
  STAMPL(27);
  LBAR();
  STAMPL(28);
  const unsigned vb = WS_W2P + nb * 68 * 5 * 1024 + l * 16;
  const unsigned bf0 = w < 4 ? 3 * w : 12 + 2 * (w - 4);
  auto issueB = [&](int t) {
    const unsigned base = L_BRING + (t % 6) * B_STAGE;
    dma16_sc1(base + bf0 * 1024, vb, rws, t * B_STAGE + bf0 * 1024);
    dma16_sc1(base + (bf0 + 1) * 1024, vb, rws, t * B_STAGE + (bf0 + 1) * 1024);
    asm volatile("s_mov_b32 m0, %0\n\ts_mov_b64 exec, %4\n\tbuffer_load_dwordx4 %1, %2, %3 offen sc1 lds\n\ts_mov_b64 exec, -1" ::"s"(w < 4 ? base + (bf0 + 2) * 1024 : L_DUMMY), "v"(w < 4 ? vb : OOB), "s"(rws), "s"(t * B_STAGE + (bf0 + 2) * 1024), "s"(w < 4 ? M_ALL : M_ONE) : "memory");
  };
  const int kq = w & 3, rh = w >> 2;
  const unsigned va = WS_ACT + (4 * r2 + 2 * rh) * 68 * 1024 + kq * 1024 + l * 16;
  u32x4 af[10][2];
  auto lda = [&](int t) { af[t % 10][0] = ld_b128_sc1(rws, va, t * 4096); af[t % 10][1] = ld_b128_sc1(rws, va + 68 * 1024, t * 4096); };
  f32x16 P;
  const float psc = 1.4426950408889634f * rstd;
  {
    issueB(0);
    T[0] = MFMA32(cs_frag(3), bm, T[0]);
    T[1] = MFMA32(cs_frag(4), bm, T[1]);
    const unsigned wpl = L_WPF + ch * 4096 + l * 16;
    const s8 A0 = lds_b128(wpl), A1 = lds_b128(wpl + 1024), A2 = lds_b128(wpl + 2048), A3 = lds_b128(wpl + 3072);
    s8 E0, E1;
    { const int k0 = l31 - 8 * h; const unsigned val = (k0 & 1) ? 0x3f800000u : 0x3f80u; u32x4 u0, u1;
#pragma unroll
      for (int d2 = 0; d2 < 4; ++d2) { u0[d2] = (k0 >> 1) == d2 ? val : 0u; u1[d2] = ((k0 - 16) >> 1) == d2 ? val : 0u; }
      E0 = __builtin_bit_cast(s8, u0); E1 = __builtin_bit_cast(s8, u1); }
    SB(); lda(0); lda(1); SB();
    const s8 t01 = pack_step(T[0], 1), t10u = pack_step(T[1], 0), t00 = pack_step(T[0], 0);
    f32x16 z16;
#pragma unroll
    for (int r = 0; r < 16; ++r) z16[r] = 0.f;
    f32x16 Pk = MFMA32(A2, t01, z16); Pk = MFMA32(A3, t10u, Pk);
#pragma unroll
    for (int r = 0; r < 8; ++r) { const f32x2 v = (f32x2){T[1][2 * r], T[1][2 * r + 1]} * (f32x2){rstd, rstd}; T[1][2 * r] = v[0]; T[1][2 * r + 1] = v[1]; }
    const s8 t10 = pack_step(T[1], 0);
    s8 t11 = pack_step(T[1], 1);
    { u32x4 u = __builtin_bit_cast(u32x4, t11); if (h) u[2] = (u[2] & 0xffff0000u) | 0x3f80u; t11 = __builtin_bit_cast(s8, u); }
    f32x16 V = MFMA32(t10, E0, z16); V = MFMA32(t11, E1, V);
    SB(); issueB(1); SB();
    unsigned phk[10];
    { f32x2 ep[5], en[5]; phi_map1(Pk, psc, ep, en); SB(); lda(2); lda(3); SB(); phi_map2(ep, en, phk); }
    SB(); issueB(2); SB();
    const s8 v0 = pack_step(V, 0), v1 = pack_step(V, 1);
    const s8 F0 = __builtin_bit_cast(s8, (u32x4){phk[0], phk[1], phk[2], phk[3]}), F1 = __builtin_bit_cast(s8, (u32x4){phk[4], phk[5], phk[6], phk[7]}), F2 = __builtin_bit_cast(s8, (u32x4){phk[8], phk[9], 0u, 0u});
    f32x16 Z0 = MFMA32(F0, E0, z16); Z0 = MFMA32(F1, E1, Z0);
    f32x16 Z1 = MFMA32(F2, E0, z16);
    f32x16 KV0 = MFMA32(pack_step(Z0, 0), v0, z16); KV0 = MFMA32(pack_step(Z0, 1), v1, KV0);
    f32x16 KV1 = MFMA32(pack_step(Z1, 0), v0, z16);
    const int rho = 16 * (l31 >> 4) + 8 * ((l31 & 7) >> 2) + 4 * ((l31 >> 3) & 1) + (l31 & 3);
    const int d = rho == 28 ? 20 : rho - 8;
    KV1 = MFMA32(pack_step(Z1, 1), v1, KV1);
    P = MFMA32(A0, t00, z16); P = MFMA32(A1, t01, P);
    if (d >= 0 && d <= 20) {
      const unsigned kb = L_KVR + w * 3360 + d * 160;
#pragma unroll
      for (int g = 0; g < 4; ++g) *(LAS f32x4*)(size_t)(kb + (8 * g + 4 * h) * 4) = (f32x4){KV0[4 * g], KV0[4 * g + 1], KV0[4 * g + 2], KV0[4 * g + 3]};
      if (h == 0) { *(LAS f32x4*)(size_t)(kb + 128) = (f32x4){KV1[0], KV1[1], KV1[2], KV1[3]}; *(LAS f32x4*)(size_t)(kb + 144) = (f32x4){KV1[4], KV1[5], KV1[6], KV1[7]}; }
    }
  }
  STAMPL(29);
  LBAR();
  {
    const int c = tid & 255, hc = tid >> 8;
    u32x4 o = {0u, 0u, 0u, 0u};
    if (c < 105) {
      float a[8];
#pragma unroll
      for (int e = 0; e < 8; ++e) a[e] = 0.f;
#pragma unroll
      for (int g = 0; g < 4; ++g) {
        const unsigned ad = L_KVR + (hc * 4 + g) * 3360 + c * 32;
        const f32x4 u0 = *(LAS f32x4*)(size_t)ad, u1 = *(LAS f32x4*)(size_t)(ad + 16);
#pragma unroll
        for (int e = 0; e < 4; ++e) { a[e] += u0[e]; a[4 + e] += u1[e]; }
      }
      o = (u32x4){pk2(a[0], a[1]), pk2(a[2], a[3]), pk2(a[4], a[5]), pk2(a[6], a[7])};
    }
    st_b128_sc1_m(o, rws, c < 105 ? WS_KVP + ((cg * 2 + hc) * 16 + rb) * KVP_STRIDE + c * 16 : OOB, tg == 0 ? M_ALL : (tg == 1 ? 0x1ffffffffffull : M_ONE));
  }
  STAMPL(30);
  const rsrc_t rout = mk_rsrc(out, S * D * 2);
  unsigned phq[10];
  lda(4); lda(5); SB();
  { f32x2 ep[5], en[5]; phi_map1(P, psc, ep, en); SB(); phi_map2(ep, en, phq); }
#pragma unroll
  for (int r = 0; r < 10; ++r) TIE(phq[r]);
  STAMPL(31);
  WAITV_BAR(4);
  st_b64_sc1_m(tag, rws, w == 0 ? WS_FKV + (cg * 16 + rb) * 8 : OOB, M_ONE);
  issueB(3); lda(6); lda(7); issueB(4);
  STAMP(5);
  STAMPL(32);
  f32x4 a2[2][5];
#pragma unroll
  for (int a = 0; a < 2; ++a)
#pragma unroll
    for (int c = 0; c < 5; ++c) a2[a][c] = (f32x4){0.f, 0.f, 0.f, 0.f};
  u32x4 kp[8]; u32x2 xr[3]; unsigned go[3]; s8 bfr[2][5];
  const int kc = (64 * tg + l) >> 1; const bool kact = kc < 105;
  static_for<0, 17>([&](auto T_) {
    constexpr int t = decltype(T_)::v;
    if constexpr (t == 4) STAMPF(16);
    if constexpr (t == 7) STAMPF(17);
    if constexpr (t == 11) STAMPF(18);
    if constexpr (t == 12) STAMPF(19);
    constexpr int T_PKV = 2, T_KVL = 3, T_SUM = 5, T_O = 6, T_FO = 9, T_PO = 11, T_AO = 12;
    constexpr int allow_tab[17] = {23, 23, 19, 5, 22, 6, 17, 11, 11, 9, 10, 10, 3, 6, 4, 5, 3};
    STAMPL(33 + t);
    asm volatile("s_waitcnt vmcnt(%0) lgkmcnt(0)" ::"i"(allow_tab[t]) : "memory");
    if constexpr (t == T_KVL) { TIE(pf); if (w == 0) { if (__builtin_expect(!flags_ok(pf, tag, l < 16), 0)) wait_flags(rws, WS_FKV + cg * 128, 8, 16, tag, l); } }
    if constexpr (t == T_AO) { TIE(pf); if (w == 0) { if (__builtin_expect(!flags_ok(pf, tag, l < 16), 0)) wait_flags(rws, WS_FO + (r2 >> 1) * 128, 8, 16, tag, l); } }
    asm volatile("s_barrier" ::: "memory");
    if constexpr (t == T_PKV) pf = POLL16(WS_FKV + cg * 128 + l * 8);
    if constexpr (t == T_KVL) {
#pragma unroll
      for (int r = 0; r < 8; ++r) kp[r] = ld_b128_sc1_m(rws, kact ? WS_KVP + ((cg * 2 + ch) * 16 + 8 * (l & 1)) * KVP_STRIDE + kc * 16 : OOB, r * KVP_STRIDE, tg < 3 ? M_ALL : 0x3ffffull);
    }
    if constexpr (t == T_SUM) {
      float a[8];
#pragma unroll
      for (int r = 0; r < 8; ++r) { TIE(kp[r]);
#pragma unroll
        for (int e = 0; e < 4; ++e) { a[2 * e] = r == 0 ? bf_lo(kp[r][e]) : sadd(a[2 * e], bf_lo(kp[r][e])); a[2 * e + 1] = r == 0 ? bf_hi(kp[r][e]) : sadd(a[2 * e + 1], bf_hi(kp[r][e])); } }
      asm volatile("s_nop 1" : "+v"(a[0]), "+v"(a[1]), "+v"(a[2]), "+v"(a[3]), "+v"(a[4]), "+v"(a[5]), "+v"(a[6]), "+v"(a[7]));
#pragma unroll
      for (int e = 0; e < 8; ++e) a[e] = dpp_add(a[e], 0);
      if (kact && (l & 1) == 0) { const int d = kc / 5, cc = kc % 5; *(LAS u32x4*)(size_t)(L_KVS + ch * 2016 + d * 96 + cc * 16) = (u32x4){pk2(a[0], a[1]), pk2(a[2], a[3]), pk2(a[4], a[5]), pk2(a[6], a[7])}; }
    }
    if constexpr (t == T_O) {
      const int reg = (l31 & 3) + 4 * (l31 >> 3), hh = (l31 >> 2) & 1;
      int d;
      if (reg >= 12) d = 20; else if (ch == 0) d = reg < 8 ? 8 * hh + reg : (hh == 0 ? 8 + reg : 20); else d = reg < 8 ? (hh == 0 ? 4 + reg : 12 + reg) : (hh == 0 ? reg - 8 : 20);
      const unsigned ka = L_KVS + ch * 2016 + d * 96;
      const s8 A0 = lds_b128(ka + h * 16), A1 = lds_b128(ka + 32 + h * 16);
      const u32x2 a2l = *(LAS u32x2*)(size_t)(ka + 64 + h * 8);
      const s8 A2 = __builtin_bit_cast(s8, (u32x4){a2l[0], a2l[1], 0u, 0u});
      f32x16 O;
#pragma unroll
      for (int r = 0; r < 16; ++r) O[r] = 0.f;
      O = MFMA32(A0, __builtin_bit_cast(s8, (u32x4){phq[0], phq[1], phq[2], phq[3]}), O);
      O = MFMA32(A1, __builtin_bit_cast(s8, (u32x4){phq[4], phq[5], phq[6], phq[7]}), O);
      O = MFMA32(A2, __builtin_bit_cast(s8, (u32x4){phq[8], phq[9], 0u, 0u}), O);
      float rc = __builtin_amdgcn_rcpf(O[12] + 1e-6f);
      asm volatile("s_nop 1" : "+v"(rc));
      const unsigned G0 = 192 + 5 * cg + (ch == 0 ? h : 3 + h);
      st_b128_sc1((u32x4){pk2(smul(O[0], rc), smul(O[1], rc)), pk2(smul(O[2], rc), smul(O[3], rc)), pk2(smul(O[4], rc), smul(O[5], rc)), pk2(smul(O[6], rc), smul(O[7], rc))}, rws, actbase + (G0 >> 2) * 1024 + (G0 & 3) * 256);
      const unsigned G2 = 192 + 5 * cg + 2;
      st_b64_sc1_m((u32x2){pk2(smul(O[8], rc), smul(O[9], rc)), pk2(smul(O[10], rc), smul(O[11], rc))}, rws, actbase + (G2 >> 2) * 1024 + (G2 & 3) * 256 + ch * 8, 0xffffffffull);
    }
    if constexpr (t == T_FO) st_b64_sc1_m(tag, rws, w == 0 ? WS_FO + (rb * 16 + cg) * 8 : OOB, M_ONE);
    if constexpr (t == T_PO) pf = POLL16(WS_FO + (r2 >> 1) * 128 + l * 8);
    if constexpr (t == T_AO) { lda(12); lda(13); lda(14); lda(15); lda(16); }
    if constexpr (t == 0) { lda(8); lda(9); issueB(5); }
    if constexpr (t == 1) lda(10);
    if constexpr (t == 2) lda(11);
    if constexpr (t >= 1 && t + 5 < 17) issueB(t + 5);
    if constexpr (t == 14) {
#pragma unroll
      for (int z = 0; z < 3; ++z) { const int ix = kq + 4 * z; const bool v = ix < 10; const int a = ix >= 5 ? 1 : 0, c = ix - 5 * a;
        go[z] = v ? (unsigned)((64 * r2 + 32 * rh + 16 * a + (l & 15)) * 1280 + (80 * nb + 16 * c + 4 * (l >> 4)) * 2) : OOB; xr[z] = ld_b64_m(rx, go[z], v ? M_ALL : M_ONE); }
    }
    if constexpr (t == 12) WAITV(8);
    TIE(af[t % 10][0]); TIE(af[t % 10][1]);
    if constexpr (t == 0) {
#pragma unroll
      for (int c = 0; c < 5; ++c) bfr[0][c] = lds_b128(L_BRING + kq * 5120 + l * 16 + c * 1024);
    }
    SB();
#pragma unroll
    for (int c = 0; c < 5; ++c) {
      a2[0][c] = MFMA16(bfr[t & 1][c], af[t % 10][0], a2[0][c]);
      a2[1][c] = MFMA16(bfr[t & 1][c], af[t % 10][1], a2[1][c]);
      if constexpr (t + 1 < 17) bfr[(t + 1) & 1][c] = lds_b128(L_BRING + ((t + 1) % 6) * B_STAGE + kq * 5120 + l * 16 + c * 1024);
      SB();
    }
  });
  STAMP(6);
  STAMPL(50);
  LBAR();
  static_for<0, 10>([&](auto I_) { constexpr int ix = decltype(I_)::v;
    if ((ix & 3) != kq) *(LAS f32x4*)(size_t)(L_RED + ((rh * 10 + ix) * 3 + ((kq - ix - 1) & 3)) * 1024 + l * 16) = a2[ix / 5][ix % 5]; });
  asm volatile("s_waitcnt vmcnt(0) lgkmcnt(0)\n\ts_barrier" ::: "memory");
  auto fin = [&](auto I_, int z) { constexpr int ix = decltype(I_)::v;
    const unsigned ad = L_RED + (rh * 10 + ix) * 3 * 1024 + l * 16;
    const f32x4 v = (a2[ix / 5][ix % 5] + *(LAS f32x4*)(size_t)ad) + (*(LAS f32x4*)(size_t)(ad + 1024) + *(LAS f32x4*)(size_t)(ad + 2048));
    st_b64((u32x2){pk2(v[0] + bf_lo(xr[z][0]), v[1] + bf_hi(xr[z][0])), pk2(v[2] + bf_lo(xr[z][1]), v[3] + bf_hi(xr[z][1]))}, rout, go[z]); };
#pragma unroll
  for (int z = 0; z < 3; ++z) TIE(xr[z]);
  if (kq == 0) { fin(IC<0>{}, 0); fin(IC<4>{}, 1); fin(IC<8>{}, 2); }
  else if (kq == 1) { fin(IC<1>{}, 0); fin(IC<5>{}, 1); fin(IC<9>{}, 2); }
  else if (kq == 2) { fin(IC<2>{}, 0); fin(IC<6>{}, 1); }
  else { fin(IC<3>{}, 0); fin(IC<7>{}, 1); }
  asm volatile(".Lfk_end:");
  if (b == 0 && tid == 0) st_b64_sc1(tag, rws, WS_EPOCH);
  STAMPL(51);
  STAMP(7);
}

extern "C" void kernel_launch(void* const* d_in, const int* in_sizes, int n_in, void* d_out, int out_size, void* d_ws, size_t ws_size, hipStream_t stream) {
  (void)in_sizes; (void)n_in; (void)out_size; (void)ws_size;
  hipFuncSetAttribute((const void*)fused_k, hipFuncAttributeMaxDynamicSharedMemorySize, LDS_BYTES);
  fused_k<<<256, 512, LDS_BYTES, stream>>>((const u16*)d_in[0], (const u16*)d_in[1], (const u16*)d_in[2], (const u16*)d_in[3], (const u16*)d_in[5], (const u16*)d_in[6], (const u16*)d_in[7], (u16*)d_out, (char*)d_ws);
}
```

```cpp
#include <hip/hip_runtime.h>
#include <hip/hip_bf16.h>
typedef unsigned short u16;
typedef unsigned long long u64;
typedef short s4 __attribute__((ext_vector_type(4)));
typedef short s8 __attribute__((ext_vector_type(8)));
typedef __bf16 b8 __attribute__((ext_vector_type(8)));
typedef __bf16 b2 __attribute__((ext_vector_type(2)));
typedef float f32x16 __attribute__((ext_vector_type(16)));
typedef float f32x4 __attribute__((ext_vector_type(4)));
typedef float f32x2 __attribute__((ext_vector_type(2)));
typedef unsigned u32x4 __attribute__((ext_vector_type(4)));
typedef unsigned u32x2 __attribute__((ext_vector_type(2)));
#define LAS __attribute__((address_space(3)))
#define DI __device__ __forceinline__
typedef __amdgpu_buffer_rsrc_t rsrc_t;

constexpr int S = 2048, D = 640, F = 1536;
constexpr unsigned WS_EPOCH = 0;
constexpr unsigned WS_FP0 = 1024, WS_FP1 = 3072, WS_FH = 5120, WS_FO = 7168, WS_FKV = 9216, WS_FCS = 11264;
constexpr unsigned WS_STATS = 16384;
constexpr unsigned WS_KVP = 32768;
constexpr unsigned KVP_STRIDE = 1792;
constexpr unsigned WS_WQP = 1u << 20;
constexpr unsigned WS_W2P = 4u << 20;
constexpr unsigned WS_CSP = 7u << 20;
constexpr unsigned WS_ACT = 8u << 20;
constexpr unsigned WS_USED = 17u << 20;
constexpr unsigned OOB = 0x80000000u;
constexpr unsigned L_X = 0;
constexpr unsigned L_W = 49152;
constexpr unsigned W_STAGE = 24832, W_UOFF = 12320;
constexpr unsigned L_MISC = L_W + 4 * W_STAGE;
constexpr unsigned L_W2S = L_MISC;
constexpr unsigned L_CS = L_W2S;
constexpr unsigned L_WPF = L_W2S + 7168;
constexpr unsigned L_QRAW = L_W + 3 * W_STAGE;
constexpr unsigned L_WPR = L_QRAW + 12288;
constexpr unsigned L_W2A = L_QRAW + 14336;
constexpr unsigned L_BRING = 0;
constexpr unsigned B_STAGE = 20480;
constexpr unsigned L_KVR = 122880;
constexpr unsigned L_KVS = 149760;
constexpr unsigned L_DUMMY1 = L_QRAW + 23552;
constexpr unsigned L_DUMMY = L_W2S + 6144;
constexpr unsigned L_RED = 0;
constexpr unsigned LDS_BYTES = 160 * 1024;

DI rsrc_t mk_rsrc(const void* p, unsigned bytes) { return __builtin_amdgcn_make_buffer_rsrc((void*)p, 0, bytes, 0x00020000); }
DI void dma16(unsigned lds_addr, unsigned voff, rsrc_t r, unsigned soff) {
  asm volatile("s_mov_b32 m0, %0\n\ts_nop 0\n\tbuffer_load_dwordx4 %1, %2, %3 offen lds" ::"s"(lds_addr), "v"(voff), "s"(r), "s"(soff) : "memory");
}
DI void dma16_sc1(unsigned lds_addr, unsigned voff, rsrc_t r, unsigned soff) {
  asm volatile("s_mov_b32 m0, %0\n\ts_nop 0\n\tbuffer_load_dwordx4 %1, %2, %3 offen sc1 lds" ::"s"(lds_addr), "v"(voff), "s"(r), "s"(soff) : "memory");
}
DI unsigned ld_u16(rsrc_t r, unsigned voff, unsigned soff) { unsigned v; asm volatile("buffer_load_ushort %0, %1, %2, %3 offen" : "=v"(v) : "v"(voff), "s"(r), "s"(soff) : "memory"); return v; }
DI unsigned ld_b32(rsrc_t r, unsigned voff, unsigned soff) { unsigned v; asm volatile("buffer_load_dword %0, %1, %2, %3 offen" : "=v"(v) : "v"(voff), "s"(r), "s"(soff) : "memory"); return v; }
DI u32x2 ld_b64_sc1(rsrc_t r, unsigned voff, unsigned soff) { u32x2 v; asm volatile("buffer_load_dwordx2 %0, %1, %2, %3 offen sc1" : "=v"(v) : "v"(voff), "s"(r), "s"(soff) : "memory"); return v; }
DI u32x4 ld_b128_sc1(rsrc_t r, unsigned voff, unsigned soff) { u32x4 v; asm volatile("buffer_load_dwordx4 %0, %1, %2, %3 offen sc1" : "=v"(v) : "v"(voff), "s"(r), "s"(soff) : "memory"); return v; }
DI u32x2 ld_b64_m(rsrc_t r, unsigned voff, u64 mask) { u32x2 v; asm volatile("s_mov_b64 exec, %3\n\tbuffer_load_dwordx2 %0, %1, %2, 0 offen\n\ts_mov_b64 exec, -1" : "=v"(v) : "v"(voff), "s"(r), "s"(mask) : "memory"); return v; }
DI void st_b64(u32x2 v, rsrc_t r, unsigned voff) { asm volatile("buffer_store_dwordx2 %0, %1, %2, 0 offen" ::"v"(v), "v"(voff), "s"(r) : "memory"); }
DI u32x4 ld_b128(rsrc_t r, unsigned voff, unsigned soff) { u32x4 v; asm volatile("buffer_load_dwordx4 %0, %1, %2, %3 offen" : "=v"(v) : "v"(voff), "s"(r), "s"(soff) : "memory"); return v; }
DI void st_b128_sc1(u32x4 v, rsrc_t r, unsigned voff) {
 asm volatile("buffer_store_dwordx4 %0, %1, %2, 0 offen sc1\n\ts_nop 1" ::"v"(v), "v"(voff), "s"(r) : "memory"); }
DI void st_b64_sc1(u32x2 v, rsrc_t r, unsigned voff) { asm volatile("buffer_store_dwordx2 %0, %1, %2, 0 offen sc1" ::"v"(v), "v"(voff), "s"(r) : "memory"); }
DI void st_b128(u32x4 v, rsrc_t r, unsigned voff) { asm volatile("buffer_store_dwordx4 %0, %1, %2, 0 offen\n\ts_nop 1" ::"v"(v), "v"(voff), "s"(r) : "memory"); }
DI void st_b64_sc1_m(u32x2 v, rsrc_t r, unsigned voff, u64 mask) {
 asm volatile("s_mov_b64 exec, %3\n\tbuffer_store_dwordx2 %0, %1, %2, 0 offen sc1\n\ts_mov_b64 exec, -1" ::"v"(v), "v"(voff), "s"(r), "s"(mask) : "memory"); }
DI void st_b32_sc1_m(unsigned v, rsrc_t r, unsigned voff, u64 mask) { asm volatile("s_mov_b64 exec, %3\n\tbuffer_store_dword %0, %1, %2, 0 offen sc1\n\ts_mov_b64 exec, -1" ::"v"(v), "v"(voff), "s"(r), "s"(mask) : "memory"); }
DI u32x2 ld_b64_sc1_m(rsrc_t r, unsigned voff, u64 mask) { u32x2 v; asm volatile("s_mov_b64 exec, %3\n\tbuffer_load_dwordx2 %0, %1, %2, 0 offen sc1\n\ts_mov_b64 exec, -1" : "=v"(v) : "v"(voff), "s"(r), "s"(mask) : "memory"); return v; }
DI unsigned ld_b32_sc1_m(rsrc_t r, unsigned voff, unsigned soff, u64 mask) { unsigned v; asm volatile("s_mov_b64 exec, %4\n\tbuffer_load_dword %0, %1, %2, %3 offen sc1\n\ts_mov_b64 exec, -1" : "=v"(v) : "v"(voff), "s"(r), "s"(soff), "s"(mask) : "memory"); return v; }
DI u32x4 ld_b128_sc1_m(rsrc_t r, unsigned voff, unsigned soff, u64 mask) { u32x4 v; asm volatile("s_mov_b64 exec, %4\n\tbuffer_load_dwordx4 %0, %1, %2, %3 offen sc1\n\ts_mov_b64 exec, -1" : "=v"(v) : "v"(voff), "s"(r), "s"(soff), "s"(mask) : "memory"); return v; }
DI void st_b128_sc1_m(u32x4 v, rsrc_t r, unsigned voff, u64 mask) {
 asm volatile("s_mov_b64 exec, %3\n\tbuffer_store_dwordx4 %0, %1, %2, 0 offen sc1\n\ts_mov_b64 exec, -1\n\ts_nop 0" ::"v"(v), "v"(voff), "s"(r), "s"(mask) : "memory"); }
DI void dma16_m(unsigned lds_addr, unsigned voff, rsrc_t r, unsigned soff, u64 mask) {
  asm volatile("s_mov_b32 m0, %0\n\ts_mov_b64 exec, %4\n\tbuffer_load_dwordx4 %1, %2, %3 offen lds\n\ts_mov_b64 exec, -1" ::"s"(lds_addr), "v"(voff), "s"(r), "s"(soff), "s"(mask) : "memory");
}
#define POLL16(off) ld_b64_sc1_m(rws, w == 0 ? (off) : OOB, w == 0 ? 0xffffull : M_ONE)
#define TIE(x) asm volatile("" : "+v"(x))
#define WAITV(n) asm volatile("s_waitcnt vmcnt(" #n ")" ::: "memory")
#define WAITV_BAR(n) asm volatile("s_waitcnt vmcnt(" #n ")\n\ts_barrier" ::: "memory")
#define WAITVL_BAR(n) asm volatile("s_waitcnt vmcnt(" #n ") lgkmcnt(0)\n\ts_barrier" ::: "memory")
#define LBAR() asm volatile("s_waitcnt lgkmcnt(0)\n\ts_barrier" ::: "memory")
#define SB() __builtin_amdgcn_sched_barrier(0)
#define MFMA32(a, b, c) __builtin_amdgcn_mfma_f32_32x32x16_bf16(__builtin_bit_cast(b8, (a)), __builtin_bit_cast(b8, (b)), (c), 0, 0, 0)
#define MFMA16(a, b, c) __builtin_amdgcn_mfma_f32_16x16x32_bf16(__builtin_bit_cast(b8, (a)), __builtin_bit_cast(b8, (b)), (c), 0, 0, 0)
DI s8 lds_b128(unsigned addr) { return *(LAS s8*)(size_t)addr; }
DI s8 tr_frag(unsigned addr, unsigned off2) {
  s4 a = __builtin_amdgcn_ds_read_tr16_b64_v4i16((LAS s4*)(size_t)addr);
  s4 b = __builtin_amdgcn_ds_read_tr16_b64_v4i16((LAS s4*)(size_t)(addr + off2));
  return __builtin_shufflevector(a, b, 0, 1, 2, 3, 4, 5, 6, 7);
}
DI float dot_ones(s8 f, float acc) {
  u32x4 u = __builtin_bit_cast(u32x4, f);
#pragma unroll
  for (int i = 0; i < 4; ++i) { const unsigned t = u[i]; acc = __builtin_amdgcn_fdot2_f32_bf16(__builtin_bit_cast(b2, t), __builtin_bit_cast(b2, 0x3f803f80u), acc, false); }
  asm volatile("" : "+v"(acc));
  return acc;
}
DI unsigned pk2(float lo, float hi) { b2 c; c[0] = (__bf16)lo; c[1] = (__bf16)hi; return __builtin_bit_cast(unsigned, c); }
DI float bf_lo(unsigned u) { return __uint_as_float(u << 16); }
DI float bf_hi(unsigned u) { return __uint_as_float(u & 0xffff0000u); }
DI float swap_add(float v) { unsigned u = __float_as_uint(v); auto r = __builtin_amdgcn_permlane32_swap(u, u, false, false); return __uint_as_float(r[0]) + __uint_as_float(r[1]); }
DI float swap_max(float v) { unsigned u = __float_as_uint(v); auto r = __builtin_amdgcn_permlane32_swap(u, u, false, false); return fmaxf(__uint_as_float(r[0]), __uint_as_float(r[1])); }
DI float swap_min(float v) { unsigned u = __float_as_uint(v); auto r = __builtin_amdgcn_permlane32_swap(u, u, false, false); return fminf(__uint_as_float(r[0]), __uint_as_float(r[1])); }
DI float sadd(float a, float b) { float r; asm("v_add_f32 %0, %1, %2" : "=v"(r) : "v"(a), "v"(b)); return r; }
DI float smul(float a, float b) { float r; asm("v_mul_f32 %0, %1, %2" : "=v"(r) : "v"(a), "v"(b)); return r; }
DI float sfma(float a, float b, float c) { float r; asm("v_fma_f32 %0, %1, %2, %3" : "=v"(r) : "v"(a), "v"(b), "v"(c)); return r; }
DI float dpp_add(float v, const int ctrl_sel) {
  const int u = __float_as_int(v); int r;
  if (ctrl_sel == 0) r = __builtin_amdgcn_update_dpp(0, u, 0xB1, 0xf, 0xf, true);
  else if (ctrl_sel == 1) r = __builtin_amdgcn_update_dpp(0, u, 0x4E, 0xf, 0xf, true);
  else if (ctrl_sel == 2) r = __builtin_amdgcn_update_dpp(0, u, 0x141, 0xf, 0xf, true);
  else r = __builtin_amdgcn_update_dpp(0, u, 0x140, 0xf, 0xf, true);
  return v + __int_as_float(r);
}
DI float wave_sum(float v) {
  v = dpp_add(v, 0); v = dpp_add(v, 1); v = dpp_add(v, 2); v = dpp_add(v, 3);
  { unsigned u = __float_as_uint(v); auto r = __builtin_amdgcn_permlane16_swap(u, u, false, false); v = __uint_as_float(r[0]) + __uint_as_float(r[1]); }
  return swap_add(v);
}
DI float sum8(s8 f, float acc) {
  const u32x4 u = __builtin_bit_cast(u32x4, f);
#pragma unroll
  for (int i = 0; i < 4; ++i) { const unsigned t = u[i]; acc += __uint_as_float(t << 16); acc += __uint_as_float(t & 0xffff0000u); }
  asm volatile("" : "+v"(acc));
  return acc;
}
DI s8 pack_step(const f32x16& x, int s) {
  return __builtin_bit_cast(s8, (u32x4){pk2(x[8 * s], x[8 * s + 1]), pk2(x[8 * s + 2], x[8 * s + 3]), pk2(x[8 * s + 4], x[8 * s + 5]), pk2(x[8 * s + 6], x[8 * s + 7])});
}
template <int I> struct IC { static constexpr int v = I; };
template <int B, int E, class Fn> DI void static_for(Fn&& f) { if constexpr (B < E) { f(IC<B>{}); static_for<B + 1, E>(f); } }
DI bool flags_ok(u32x2 v, u32x2 tag, bool act) { return __all(!act || (v[0] == tag[0] && v[1] == tag[1])); }
DI void wait_flags(rsrc_t rws, unsigned base, unsigned stride, int n, u32x2 tag, int l) {

  const bool act = l < n; const unsigned off = act ? base + l * stride : OOB;
#pragma nounroll
  for (int it = 0; it < (1 << 17); ++it) {
    u32x2 v; asm volatile("buffer_load_dwordx2 %0, %1, %2, 0 offen sc1\n\ts_waitcnt vmcnt(0)" : "=&v"(v) : "v"(off), "s"(rws) : "memory");
    if (flags_ok(v, tag, act)) return;
    __builtin_amdgcn_s_sleep(1);
  }
}
DI void wait_flags256(rsrc_t rws, unsigned base, u32x2 tag, int l) {
#pragma nounroll
  for (int it = 0; it < (1 << 17); ++it) {
    u32x4 a, b;
    asm volatile("buffer_load_dwordx4 %0, %2, %3, 0 offen sc1\n\tbuffer_load_dwordx4 %1, %2, %3, 0 offen offset:16 sc1\n\ts_waitcnt vmcnt(0)" : "=&v"(a), "=&v"(b) : "v"(base + l * 32), "s"(rws) : "memory");
    const bool ok = a[0] == tag[0] && a[1] == tag[1] && a[2] == tag[0] && a[3] == tag[1] && b[0] == tag[0] && b[1] == tag[1] && b[2] == tag[0] && b[3] == tag[1];
    if (__all(ok)) return;
    __builtin_amdgcn_s_sleep(1);
  }
}
DI bool flags256_ok(u32x4 a, u32x4 b, u32x2 tag) {
  return __all(a[0] == tag[0] && a[1] == tag[1] && a[2] == tag[0] && a[3] == tag[1] && b[0] == tag[0] && b[1] == tag[1] && b[2] == tag[0] && b[3] == tag[1]);
}
DI float max3(float a, float b, float c) { float r; asm("v_max3_f32 %0, %1, %2, %3" : "=v"(r) : "v"(a), "v"(b), "v"(c)); return r; }
DI float min3(float a, float b, float c) { float r; asm("v_min3_f32 %0, %1, %2, %3" : "=v"(r) : "v"(a), "v"(b), "v"(c)); return r; }
DI f32x2 exp2_2(f32x2 a) { f32x2 r; r[0] = __builtin_amdgcn_exp2f(a[0]); r[1] = __builtin_amdgcn_exp2f(a[1]); return r; }
DI unsigned pk2v(f32x2 a) { return pk2(a[0], a[1]); }
DI void phi_map1(const f32x16& pin, float sc, f32x2 (&ep)[5], f32x2 (&en)[5]) {
  float p[10];
#pragma unroll
  for (int r = 0; r < 10; ++r) p[r] = pin[r];
  asm volatile("s_nop 7\n\ts_nop 3" : "+v"(p[0]), "+v"(p[1]), "+v"(p[2]), "+v"(p[3]), "+v"(p[4]), "+v"(p[5]), "+v"(p[6]), "+v"(p[7]), "+v"(p[8]), "+v"(p[9]));
  float mx = max3(p[0], p[1], p[2]), mn = min3(p[0], p[1], p[2]);
#pragma unroll
  for (int r = 3; r < 9; r += 2) { mx = max3(mx, p[r], p[r + 1]); mn = min3(mn, p[r], p[r + 1]); }
  mx = max3(mx, p[9], p[9]); mn = min3(mn, p[9], p[9]);
  { unsigned u = __float_as_uint(mx); auto r = __builtin_amdgcn_permlane32_swap(u, u, false, false); mx = max3(__uint_as_float(r[0]), __uint_as_float(r[1]), __uint_as_float(r[1])); }
  { unsigned u = __float_as_uint(mn); auto r = __builtin_amdgcn_permlane32_swap(u, u, false, false); mn = min3(__uint_as_float(r[0]), __uint_as_float(r[1]), __uint_as_float(r[1])); }
  const f32x2 s2 = {sc, sc}, bp = {-mx * sc, -mx * sc}, bn = {mn * sc, mn * sc};
#pragma unroll
  for (int r = 0; r < 5; ++r) { const f32x2 v = {p[2 * r], p[2 * r + 1]}; ep[r] = exp2_2(__builtin_elementwise_fma(v, s2, bp)); en[r] = exp2_2(__builtin_elementwise_fma(v, -s2, bn)); }
}
DI void phi_map2(const f32x2 (&ep)[5], const f32x2 (&en)[5], unsigned (&o)[10]) {
  const f32x2 sp2 = (ep[0] + ep[1]) + (ep[2] + ep[3]) + ep[4], sn2 = (en[0] + en[1]) + (en[2] + en[3]) + en[4];
  const float rp = __builtin_amdgcn_rcpf(swap_add(sp2[0] + sp2[1])), rn = __builtin_amdgcn_rcpf(swap_add(sn2[0] + sn2[1]));
  const f32x2 rp2 = {rp, rp}, rn2 = {rn, rn};
#pragma unroll
  for (int r = 0; r < 5; ++r) { o[r] = pk2v(ep[r] * rp2); o[5 + r] = pk2v(en[r] * rn2); }
}

#define STAMP(i)
#define STAMPL(k)
#define STAMPC(i)
#define STAMPF(i)

__global__ __launch_bounds__(512, 2) void fused_k(const u16* __restrict__ x, const u16* __restrict__ wqkv, const u16* __restrict__ wphi, const u16* __restrict__ wo,
                                                   const u16* __restrict__ wg, const u16* __restrict__ wu, const u16* __restrict__ wd, u16* __restrict__ out, char* __restrict__ ws) {
  extern __shared__ __attribute__((aligned(256))) char smem[];
  const int tid = threadIdx.x; const int l = tid & 63; const int w = __builtin_amdgcn_readfirstlane(tid >> 6);
  const int tg = w & 3, ch = w >> 2; const int l31 = l & 31, h = l >> 5;
  const int b = blockIdx.x, xcd = b & 7, idx = b >> 3;
  const int rb = (xcd & 1) * 8 + (idx & 7), cg = (xcd >> 1) * 4 + (idx >> 3);
  const int r2 = (xcd & 3) * 8 + (idx & 7), nb = (xcd >> 2) * 4 + (idx >> 3);
  const int klo = (5 * rb) >> 1, kcnt = ((5 * (rb + 1)) >> 1) - klo, rot = klo >> 2, kloc = klo - 4 * rot;
  STAMP(0);
  {
    const unsigned long long c0 = __builtin_amdgcn_s_memtime();
    const u16* ptrs[7] = {x, wqkv, wg, wu, wd, wo, wphi}; const unsigned szs[7] = {S * D * 2, D * 1920 * 2, D * F * 2, D * F * 2, F * D * 2, D * D * 2, 25600};
    for (int q = 0; q < 7; ++q) { const rsrc_t r = mk_rsrc(ptrs[q], szs[q]); const unsigned per = (szs[q] / 256 + 1023) / 1024;
      for (unsigned i = w; i < per; i += 8) dma16(L_X + w * 1024, (b * per + i) * 1024 + l * 16, r, 0); }
    asm volatile("s_waitcnt vmcnt(0)" ::: "memory");
    while (__builtin_amdgcn_s_memtime() - c0 < 40000ull) __builtin_amdgcn_s_sleep(1);
    __syncthreads(); }
  STAMPL(0);
  const rsrc_t rx = mk_rsrc(x, S * D * 2);
  const rsrc_t rw = mk_rsrc(w < 4 ? wg : wu, D * F * 2);
  const rsrc_t rws = mk_rsrc(ws, WS_USED);
  const u64 M_ALL = ~0ull, M_ONE = 1ull;
  auto sig = [&](int s) -> unsigned { const int t = s + rot; return t >= 10 ? t - 10 : t; };
  unsigned vx[2], vw[3];
#pragma unroll
  for (int i = 0; i < 2; ++i) { const int r = 8 * (2 * w + i) + (l >> 3); const int c = (l & 7) ^ ((r >> 1) & 7); vx[i] = (128 * rb + r) * 1280 + c * 16; }
#pragma unroll
  for (int i = 0; i < 3; ++i) { const int p = 64 * (3 * (w & 3) + i) + l; const int k = p / 12, c = p % 12; vw[i] = k * 3072 + cg * 192 + c * 16; }
  const unsigned lx0 = L_X + 2 * w * 1024;
  const unsigned lw0 = L_W + (w < 4 ? 0 : W_UOFF) + 3 * (w & 3) * 1024;
  auto dmax = [&](int s, int i) { dma16(lx0 + (s % 3) * 16384 + i * 1024, vx[i], rx, sig(s) * 128); };
  auto dmaw = [&](int s, int i) { dma16(lw0 + (s & 3) * W_STAGE + i * 1024, vw[i], rw, sig(s) * (64 * 3072)); };
  auto issue1 = [&](int s) { dmax(s, 0); dmax(s, 1); dmaw(s, 0); dmaw(s, 1); dmaw(s, 2); };
  issue1(0);
  u32x2 ep = ld_b64_sc1_m(rws, WS_EPOCH, M_ONE);
  const int srow = 128 * rb + 8 * cg + w;
  const bool v3 = kcnt == 3 && w < 4;
  u32x4 sx0, sx1;
  {
    const rsrc_t rq = mk_rsrc(wqkv, D * 1920 * 2);
    const int p = 64 * (w & 3) + l; const int k = p / 15, c = p % 15;
    const unsigned vo = p < 240 ? (unsigned)(k * 3840 + (c / 5) * 1280 + cg * 80 + (c % 5) * 16) : OOB;
    dma16(L_QRAW + w * 1024, vo, rq, (16 * (klo + (w >> 2))) * 3840);
    dma16_m(v3 ? L_QRAW + 8192 + w * 1024 : L_DUMMY1, v3 ? vo : OOB, rq, v3 ? (16 * (klo + 2)) * 3840 : 0, v3 ? M_ALL : M_ONE);
    const rsrc_t rp = mk_rsrc(wphi, 32 * 400 * 2);
    dma16_m(w < 2 ? L_WPR + w * 1024 : L_DUMMY1, (w < 2 && w * 64 + l < 100) ? (unsigned)(cg * 1600 + (w * 64 + l) * 16) : OOB, rp, 0, w == 0 ? M_ALL : (w == 1 ? 0xfffffffffull : M_ONE));
    sx0 = ld_b128(rx, srow * 1280 + l * 16, 0);
    sx1 = ld_b128(rx, l < 16 ? (unsigned)(srow * 1280 + 1024 + l * 16) : OOB, 0);
    const int kst = b / 5, cb = b % 5; const bool isd = kst < 48;
    const rsrc_t rr = mk_rsrc(isd ? wd : wo, isd ? F * D * 2 : D * D * 2);
    dma16(L_W2A + w * 1024, (unsigned)((l >> 4) * 1280 + (l & 15) * 16), rr, ((isd ? 32 * kst : 32 * (kst - 48)) + 4 * w) * 1280 + cb * 256);
  }
  const int gi = l >> 4, q4 = (l >> 2) & 3, p4 = l & 3;
  issue1(1); issue1(2);
  {
    u64 pc; unsigned len;
    asm volatile("s_getpc_b64 %0\n.Lfk_here:\n\ts_mov_b32 %1, .Lfk_end-.Lfk_here" : "=s"(pc), "=s"(len));
    const rsrc_t rc = mk_rsrc((const void*)pc, len);
    const unsigned off = (idx * 8 + w) * 256;
    asm volatile("s_mov_b32 m0, %0\n\ts_mov_b64 exec, 0xffff\n\tbuffer_load_dwordx4 %1, %2, 0 offen lds\n\ts_mov_b64 exec, -1" ::"s"(L_DUMMY1), "v"(off + l * 16), "s"(rc) : "memory");
  }
  WAITV_BAR(11);
  STAMP(1);
  STAMPL(1);
  TIE(ep);
  u32x2 tag;
  { const unsigned lo = __builtin_amdgcn_readfirstlane(ep[0]), hi = __builtin_amdgcn_readfirstlane(ep[1]); tag[0] = lo + 1u; tag[1] = hi + (lo == 0xffffffffu ? 1u : 0u); }
  const int shc = (w >> 1) & 1, sht = w & 1;
  s8 sf0, sf1, sf2; unsigned se[8]; unsigned smA, smB;
  {
    const int Q = 4 * (gi & 1) + p4;
    int seg, qd;
    if (sht == 0) { seg = Q < 5 ? 0 : 1; qd = Q < 5 ? Q : Q - 5; } else { seg = Q < 2 ? 1 : 2; qd = Q < 2 ? 3 + Q : (Q < 7 ? Q - 2 : 4); }
    const unsigned lo = L_QRAW + (8 * h + q4) * 240 + seg * 80 + shc * 40 + qd * 8;
    sf0 = tr_frag(lo + (w >> 2) * 4096, 960); sf1 = tr_frag(lo + 8192, 960);
    sf2 = tr_frag(L_W2A + (8 * gi + q4) * 256 + w * 32 + p4 * 8, 1024);
    const int reg = (l31 & 3) + 4 * (l31 >> 3), hh = (l31 >> 2) & 1;
    const int d0 = (tg == 0 ? 0 : tg == 1 ? 16 : tg == 2 ? -4 : 12) + 4 * h;
    const unsigned wa = L_WPR + ch * 800 + (10 * hh + reg) * 2 + d0 * 40;
#pragma unroll
    for (int j = 0; j < 8; ++j) se[j] = *(LAS u16*)(size_t)(wa + (8 * (j >> 2) + (j & 3)) * 40);
    smA = (reg < 10 && (unsigned)d0 < 20u) ? ~0u : 0u; smB = (reg < 10 && (unsigned)(d0 + 8) < 20u) ? ~0u : 0u;
  }
  auto side = [&](int part) {
    if (part == 0) {
      TIE(sx0); TIE(sx1);
      float sm = 0.f, sq = 0.f;
#pragma unroll
      for (int i = 0; i < 4; ++i) { const unsigned u0 = sx0[i], u1 = sx1[i];
        sm = __builtin_amdgcn_fdot2_f32_bf16(__builtin_bit_cast(b2, u0), __builtin_bit_cast(b2, 0x3f803f80u), sm, false); sq = __builtin_amdgcn_fdot2_f32_bf16(__builtin_bit_cast(b2, u0), __builtin_bit_cast(b2, u0), sq, false);
        sm = __builtin_amdgcn_fdot2_f32_bf16(__builtin_bit_cast(b2, u1), __builtin_bit_cast(b2, 0x3f803f80u), sm, false); sq = __builtin_amdgcn_fdot2_f32_bf16(__builtin_bit_cast(b2, u1), __builtin_bit_cast(b2, u1), sq, false); }
      const float m = wave_sum(sm) * (1.f / D);
      const float var = fmaxf(wave_sum(sq) * (1.f / D) - m * m, 0.f);
      st_b64_sc1_m((u32x2){__float_as_uint(m), __float_as_uint(__builtin_amdgcn_rsqf(var + 1e-5f))}, rws, WS_STATS + srow * 8, M_ONE);
    } else if (part == 1) {
      st_b128_sc1(__builtin_bit_cast(u32x4, sf0), rws, WS_WQP + (((2 * cg + shc) * 40 + klo + (w >> 2)) * 2 + sht) * 1024 + l * 16);
      st_b128_sc1_m(__builtin_bit_cast(u32x4, sf1), rws, v3 ? WS_WQP + (((2 * cg + shc) * 40 + klo + 2) * 2 + sht) * 1024 + l * 16 : OOB, v3 ? M_ALL : M_ONE);
      const int kst = b / 5, cb = b % 5, ct = 8 * cb + w;
      st_b128_sc1(__builtin_bit_cast(u32x4, sf2), rws, WS_W2P + (((ct / 5) * 68 + kst) * 5 + ct % 5) * 1024 + l * 16);
      const float c0 = swap_add(dot_ones(sf0, 0.f)), c1 = swap_add(dot_ones(sf1, 0.f));
      if (h == 0) { const unsigned qa = L_W2S + ((shc * 2 + sht) * 32 + l31) * 16 + (w >> 2) * 4; *(LAS float*)(size_t)qa = c0; *(LAS float*)(size_t)(qa + 8) = v3 ? c1 : 0.f; }
    } else {
      *(LAS u32x4*)(size_t)(L_WPF + w * 1024 + l * 16) = (u32x4){(se[0] | (se[1] << 16)) & smA, (se[2] | (se[3] << 16)) & smA, (se[4] | (se[5] << 16)) & smB, (se[6] | (se[7] << 16)) & smB};
    }
  };
  STAMPF(8);
  const unsigned qoff = (p4 == 1) ? 8 : (p4 == 2) ? 4 : (p4 == 3) ? 12 : 0;
  const unsigned wl = L_W + ((gi & 1) ? W_UOFF : 0) + (8 * h + q4) * 192 + 2 * qoff + 96 * ch;
  const unsigned wl2 = wl + 2 * W_STAGE;
  const int xrow = 32 * tg + l31;
  unsigned xa[4];
#pragma unroll
  for (int j = 0; j < 4; ++j) { xa[j] = (L_X + xrow * 128 + ((h ^ ((xrow >> 1) & 7)) << 4)) ^ (j * 32); asm volatile("" : "+v"(xa[j])); }
  s8 XF[40];
  f32x16 acc[3]; float cs[3];
#pragma unroll
  for (int i = 0; i < 3; ++i) { cs[i] = 0.f;
#pragma unroll
    for (int r = 0; r < 16; ++r) acc[i][r] = 0.f; }
  s8 wf[2][3];
  auto rdx = [&](int s, int j) { XF[4 * s + j] = lds_b128(xa[j] + (s % 3) * 16384); };
  auto rdw = [&](int n, int i) { const int s = n >> 2, j = n & 3; wf[n & 1][i] = tr_frag(((s & 2) ? wl2 : wl) + (s & 1) * W_STAGE + j * 3072 + i * 32, 768); };
  const unsigned vq2 = WS_WQP + ((2 * cg + ch) * 40 + tg) * 2048 + l * 16;
  auto slot2 = [&](int q) -> unsigned { return q == 0 || q == 7 ? 16384u : q == 1 || q == 8 ? L_W + 2 * W_STAGE : q == 2 || q == 9 ? 32768u : q == 3 ? L_W + 3 * W_STAGE : q == 4 ? 0u : q == 5 ? L_W : L_W + W_STAGE; };
  auto issue2 = [&](int q) { const unsigned so = sig(q) * 8192; dma16_sc1(slot2(q) + w * 2048, vq2, rws, so); dma16_sc1(slot2(q) + w * 2048 + 1024, vq2, rws, so + 1024); };
  const bool hv = b < 168 && w < 4; const int hu = 256 + (b >> 1), hkst = hu / 5, hcb = hu % 5;
  u32x2 pf, pf2, st; u32x4 pl0, pl1, cg4[4]; unsigned cg1[4]; f32x4 qs0, qs1;
  rdx(0, 0); rdw(0, 0); rdw(0, 1); rdw(0, 2); rdx(0, 1); rdx(0, 2); rdx(0, 3);
  static_for<0, 40>([&](auto N_) {
    constexpr int n = decltype(N_)::v;
    constexpr int s = n >> 2, j = n & 3;
    if constexpr (n == 3) STAMPF(9);
    if constexpr (n == 11) STAMPF(10);
    if constexpr (n == 19) STAMPF(11);
    if constexpr (n == 27) STAMPF(12);
    if constexpr (n == 35) STAMPF(13);
    if constexpr (j == 3 && s < 9) STAMPL(2 + 2 * s);
    if constexpr (n == 3) WAITV_BAR(10);
    if constexpr (n == 7) { WAITV_BAR(9); qs0 = *(LAS f32x4*)(size_t)(L_W2S + ((ch * 2) * 32 + l31) * 16); qs1 = *(LAS f32x4*)(size_t)(L_W2S + ((ch * 2 + 1) * 32 + l31) * 16); }
    if constexpr (n == 11) { WAITV_BAR(0); st_b64_sc1_m(tag, rws, w == 0 ? WS_FP0 + (cg * 16 + rb) * 8 : OOB, M_ONE); }
    if constexpr (n == 15) WAITV_BAR(6);
    if constexpr (n == 19) WAITV_BAR(6);
    if constexpr (n == 23) { WAITV_BAR(5); pf = POLL16(WS_FP0 + cg * 128 + l * 8); pf2 = POLL16(WS_FP0 + rb * 8 + (l & 15) * 128); }
    if constexpr (n == 27) {
      WAITV(6); TIE(pf); TIE(pf2);
      if (w == 0) {
        if (__builtin_expect(!flags_ok(pf, tag, l < 16), 0)) wait_flags(rws, WS_FP0 + cg * 128, 8, 16, tag, l);
        if (__builtin_expect(!flags_ok(pf2, tag, l < 16), 0)) wait_flags(rws, WS_FP0 + rb * 8, 128, 16, tag, l);
      }
      asm volatile("s_barrier" ::: "memory");
      st = ld_b64_sc1(rws, WS_STATS + (128 * rb + xrow) * 8, 0);
      {
        const unsigned vo = w < 4 ? WS_CSP + (cg * 16 + 4 * w) * 1280 + l * 16 : OOB, vo1 = w < 4 ? WS_CSP + (cg * 16 + 4 * w) * 1280 + 1024 + l * 4 : OOB;
#pragma unroll
        for (int r = 0; r < 4; ++r) { cg4[r] = ld_b128_sc1_m(rws, vo, r * 1280, w < 4 ? M_ALL : M_ONE); cg1[r] = ld_b32_sc1_m(rws, vo1, r * 1280, w < 4 ? M_ALL : M_ONE); } }
    }
    if constexpr (n == 31) {
      WAITV_BAR(14);
      st_b64_sc1_m(tag, rws, w == 0 ? WS_FP1 + b * 8 : OOB, M_ONE);
      issue2(0); issue2(1);
    }
    if constexpr (n == 35) {
      WAITV_BAR(5);
#pragma unroll
      for (int r = 0; r < 4; ++r) { TIE(cg4[r]); TIE(cg1[r]); }
      if (w < 4) { const unsigned ad = L_CS + ((h * 5) * 32 + l31) * 16 + w * 4;
#pragma unroll
        for (int e = 0; e < 4; ++e) *(LAS float*)(size_t)(ad + e * 512) = (__uint_as_float(cg4[0][e]) + __uint_as_float(cg4[1][e])) + (__uint_as_float(cg4[2][e]) + __uint_as_float(cg4[3][e]));
        *(LAS float*)(size_t)(ad + 4 * 512) = (__uint_as_float(cg1[0]) + __uint_as_float(cg1[1])) + (__uint_as_float(cg1[2]) + __uint_as_float(cg1[3])); }
      issue2(2); issue2(3);
    }
    if constexpr (j == 3 && s < 9) STAMPL(3 + 2 * s);
    SB();
#pragma unroll
    for (int i = 0; i < 3; ++i) {
      acc[i] = MFMA32(wf[n & 1][i], XF[n], acc[i]);
      SB();
      if (i == 0 && j == 3 && s < 9) rdx(s + 1, 0);
      if (n + 1 < 40) rdw(n + 1, i);
      if (j == 0 && s > 0) rdx(s, i + 1);
      if (n < 3 && i == 2) side(n);
      if (n < 8 && i == 2) { if ((unsigned)(n - kloc) < (unsigned)kcnt && tg == 0) { cs[0] = dot_ones(wf[n & 1][0], cs[0]); cs[1] = dot_ones(wf[n & 1][1], cs[1]); cs[2] = dot_ones(wf[n & 1][2], cs[2]); } }
      if (n == 8 && i == 0) {
        const unsigned dst = tg == 0 ? WS_CSP + (cg * 16 + rb) * 1280 + (ch * 32 + l31) * 16 : OOB; const u64 mk = tg == 0 ? 0xffffffffull : M_ONE;
        st_b128_sc1_m((u32x4){__float_as_uint(swap_add(cs[0])), __float_as_uint(swap_add(cs[1])), __float_as_uint(swap_add(cs[2])), __float_as_uint((qs0[0] + qs0[1]) + (qs0[2] + qs0[3]))}, rws, dst, mk);
        st_b32_sc1_m(__float_as_uint((qs1[0] + qs1[1]) + (qs1[2] + qs1[3])), rws, tg == 0 ? WS_CSP + (cg * 16 + rb) * 1280 + 1024 + (ch * 32 + l31) * 4 : OOB, mk);
      }
      if (j == 3 && s + 3 < 10) {
        if (i == 0) { dmax(s + 3, 0); dmax(s + 3, 1); }
        if (i == 1) { dmaw(s + 3, 0); dmaw(s + 3, 1); }
        if (i == 2) dmaw(s + 3, 2);
      }
      if constexpr (n == 15) { if (i == 2) {
        const rsrc_t rr = mk_rsrc(wo, D * D * 2);
        dma16_m(hv ? L_W2S + w * 1024 : L_DUMMY, hv ? (unsigned)((l >> 3) * 1280 + (l & 7) * 16) : OOB, rr, hv ? (32 * (hkst - 48) + 8 * w) * 1280 + hcb * 256 + (b & 1) * 128 : 0, hv ? M_ALL : M_ONE);
      } }
      if constexpr (n == 23) { if (i == 2) {
        const int ct = 8 * hcb + 4 * (b & 1) + (w & 3);
        const s8 f = tr_frag(L_W2S + (8 * gi + q4) * 128 + (w & 3) * 32 + p4 * 8, 512);
        st_b128_sc1_m(__builtin_bit_cast(u32x4, f), rws, hv ? WS_W2P + (((ct / 5) * 68 + hkst) * 5 + ct % 5) * 1024 + l * 16 : OOB, hv ? M_ALL : M_ONE);
      } }
      SB();
    }
  });
  STAMP(2);
  STAMPL(20);
  WAITVL_BAR(0);
  TIE(st);
  const float mean = __uint_as_float(st[0]), rstd = __uint_as_float(st[1]);
  s8 bm;
  { const float nm = -mean; const unsigned hi = pk2(nm, 0.f) & 0xffffu; const float lo = nm - __uint_as_float(hi << 16); unsigned mpk = hi | (pk2(lo, 0.f) << 16); if (h) mpk = 0; bm = __builtin_bit_cast(s8, (u32x4){mpk, mpk, 0u, 0u}); }
  auto cs_frag = [&](int tile) -> s8 {
    const f32x4 pp = *(LAS f32x4*)(size_t)(L_CS + ((ch * 5 + tile) * 32 + l31) * 16);
    const float c = (pp[0] + pp[1]) + (pp[2] + pp[3]);
    unsigned hi = pk2(c, c); const float lo = c - __uint_as_float(hi << 16); unsigned lo2 = pk2(lo, lo);
    if (h) { hi = 0; lo2 = 0; }
    return __builtin_bit_cast(s8, (u32x4){hi, lo2, 0u, 0u});
  };
  const unsigned tok = 128 * rb + xrow;
  const unsigned actbase = WS_ACT + (tok >> 4) * 68 * 1024 + (l & 15) * 16;
  {
    const float gs = -1.4426950408889634f * rstd, irs = __builtin_amdgcn_rcpf(rstd);
#pragma unroll
    for (int i = 0; i < 3; ++i) acc[i] = MFMA32(cs_frag(i), bm, acc[i]);
#pragma unroll
    for (int i = 0; i < 3; ++i) {
      issue2(4 + i);
      const f32x2 gs2 = {gs, gs}, irs2 = {irs, irs};
      u32x4 ho;
#pragma unroll
      for (int r = 0; r < 4; ++r) {
        const f32x2 g2 = {acc[i][2 * r], acc[i][2 * r + 1]}, u2 = {acc[i][8 + 2 * r], acc[i][9 + 2 * r]};
        const f32x2 dn = __builtin_elementwise_fma(exp2_2(g2 * gs2), irs2, irs2);
        const f32x2 rc = {__builtin_amdgcn_rcpf(dn[0]), __builtin_amdgcn_rcpf(dn[1])};
        ho[r] = pk2v(u2 * rc);
      }
      const unsigned G = 12 * cg + 6 * ch + 2 * i + h;
      st_b128_sc1(ho, rws, actbase + (G >> 2) * 1024 + (G & 3) * 256);
      SB();
    }
  }
  STAMP(3);
  STAMPL(21);
  f32x16 T[2];
#pragma unroll
  for (int i = 0; i < 2; ++i)
#pragma unroll
    for (int r = 0; r < 16; ++r) T[i][r] = 0.f;
  s8 qf[3][2];
  const unsigned ql = ch * 8192 + l * 16, ql2 = ql + L_W;
  auto rdq = [&](int m, int t) { const int q = m >> 2, j = m & 3; const unsigned sl = slot2(q); qf[m % 3][t] = lds_b128((sl >= L_W ? ql2 + (sl - L_W) : ql + sl) + j * 2048 + t * 1024); };
  rdq(0, 0); rdq(0, 1); rdq(1, 0); rdq(1, 1);
  static_for<0, 40>([&](auto M_) {
    constexpr int m = decltype(M_)::v;
    if constexpr (m == 6) STAMPL(22);
    if constexpr (m == 14) STAMPL(23);
    if constexpr (m == 15) STAMPL(24);
    if constexpr (m == 26) STAMPL(25);
    if constexpr (m == 34) STAMPL(26);
    if constexpr (m == 14) { WAITV_BAR(0); st_b64_sc1_m(tag, rws, w == 0 ? WS_FH + (rb * 16 + cg) * 8 : OOB, M_ONE); issue2(7); issue2(8); issue2(9); }
    if constexpr (m == 26) {
      WAITV_BAR(0);
      pl0 = ld_b128_sc1_m(rws, WS_FP1 + l * 32, 0, w == 0 ? M_ALL : M_ONE); pl1 = ld_b128_sc1_m(rws, WS_FP1 + l * 32 + 16, 0, w == 0 ? M_ALL : M_ONE);
      pf = POLL16(WS_FH + (r2 >> 1) * 128 + l * 8);
    }
    if constexpr (m == 38) {
      WAITV(0); TIE(pl0); TIE(pl1); TIE(pf);
      if (w == 0) {
        if (__builtin_expect(!flags256_ok(pl0, pl1, tag), 0)) wait_flags256(rws, WS_FP1, tag, l);
        if (__builtin_expect(!flags_ok(pf, tag, l < 16), 0)) wait_flags(rws, WS_FH + (r2 >> 1) * 128, 8, 16, tag, l);
      }
    }
    SB();
#pragma unroll
    for (int t = 0; t < 2; ++t) {
      T[t] = MFMA32(qf[m % 3][t], XF[m], T[t]);
      SB();
      if (m + 2 < 40) rdq(m + 2, t);
      SB();
    }
  });
  STAMP(4);
  STAMPL(27);
  LBAR();
  STAMPL(28);
  const unsigned vb = WS_W2P + nb * 68 * 5 * 1024 + l * 16;
  const unsigned bf0 = w < 4 ? 3 * w : 12 + 2 * (w - 4);
  auto issueB = [&](int t) {
    const unsigned base = L_BRING + (t % 6) * B_STAGE;
    dma16_sc1(base + bf0 * 1024, vb, rws, t * B_STAGE + bf0 * 1024);
    dma16_sc1(base + (bf0 + 1) * 1024, vb, rws, t * B_STAGE + (bf0 + 1) * 1024);
    asm volatile("s_mov_b32 m0, %0\n\ts_mov_b64 exec, %4\n\tbuffer_load_dwordx4 %1, %2, %3 offen sc1 lds\n\ts_mov_b64 exec, -1" ::"s"(w < 4 ? base + (bf0 + 2) * 1024 : L_DUMMY), "v"(w < 4 ? vb : OOB), "s"(rws), "s"(t * B_STAGE + (bf0 + 2) * 1024), "s"(w < 4 ? M_ALL : M_ONE) : "memory");
  };
  const int kq = w & 3, rh = w >> 2;
  const unsigned va = WS_ACT + (4 * r2 + 2 * rh) * 68 * 1024 + kq * 1024 + l * 16;
  u32x4 af[10][2];
  auto lda = [&](int t) { af[t % 10][0] = ld_b128_sc1(rws, va, t * 4096); af[t % 10][1] = ld_b128_sc1(rws, va + 68 * 1024, t * 4096); };
  f32x16 P;
  const float psc = 1.4426950408889634f * rstd;
  {
    issueB(0);
    T[0] = MFMA32(cs_frag(3), bm, T[0]);
    T[1] = MFMA32(cs_frag(4), bm, T[1]);
    const unsigned wpl = L_WPF + ch * 4096 + l * 16;
    const s8 A0 = lds_b128(wpl), A1 = lds_b128(wpl + 1024), A2 = lds_b128(wpl + 2048), A3 = lds_b128(wpl + 3072);
    s8 E0, E1;
    { const int k0 = l31 - 8 * h; const unsigned val = (k0 & 1) ? 0x3f800000u : 0x3f80u; u32x4 u0, u1;
#pragma unroll
      for (int d2 = 0; d2 < 4; ++d2) { u0[d2] = (k0 >> 1) == d2 ? val : 0u; u1[d2] = ((k0 - 16) >> 1) == d2 ? val : 0u; }
      E0 = __builtin_bit_cast(s8, u0); E1 = __builtin_bit_cast(s8, u1); }
    SB(); lda(0); lda(1); SB();
    const s8 t01 = pack_step(T[0], 1), t10u = pack_step(T[1], 0), t00 = pack_step(T[0], 0);
    f32x16 z16;
#pragma unroll
    for (int r = 0; r < 16; ++r) z16[r] = 0.f;
    f32x16 Pk = MFMA32(A2, t01, z16); Pk = MFMA32(A3, t10u, Pk);
#pragma unroll
    for (int r = 0; r < 8; ++r) { const f32x2 v = (f32x2){T[1][2 * r], T[1][2 * r + 1]} * (f32x2){rstd, rstd}; T[1][2 * r] = v[0]; T[1][2 * r + 1] = v[1]; }
    const s8 t10 = pack_step(T[1], 0);
    s8 t11 = pack_step(T[1], 1);
    { u32x4 u = __builtin_bit_cast(u32x4, t11); if (h) u[2] = (u[2] & 0xffff0000u) | 0x3f80u; t11 = __builtin_bit_cast(s8, u); }
    f32x16 V = MFMA32(t10, E0, z16); V = MFMA32(t11, E1, V);
    SB(); issueB(1); SB();
    unsigned phk[10];
    { f32x2 ep[5], en[5]; phi_map1(Pk, psc, ep, en); SB(); lda(2); lda(3); SB(); phi_map2(ep, en, phk); }
    SB(); issueB(2); SB();
    const s8 v0 = pack_step(V, 0), v1 = pack_step(V, 1);
    const s8 F0 = __builtin_bit_cast(s8, (u32x4){phk[0], phk[1], phk[2], phk[3]}), F1 = __builtin_bit_cast(s8, (u32x4){phk[4], phk[5], phk[6], phk[7]}), F2 = __builtin_bit_cast(s8, (u32x4){phk[8], phk[9], 0u, 0u});
    f32x16 Z0 = MFMA32(F0, E0, z16); Z0 = MFMA32(F1, E1, Z0);
    f32x16 Z1 = MFMA32(F2, E0, z16);
    f32x16 KV0 = MFMA32(pack_step(Z0, 0), v0, z16); KV0 = MFMA32(pack_step(Z0, 1), v1, KV0);
    f32x16 KV1 = MFMA32(pack_step(Z1, 0), v0, z16);
    const int rho = 16 * (l31 >> 4) + 8 * ((l31 & 7) >> 2) + 4 * ((l31 >> 3) & 1) + (l31 & 3);
    const int d = rho == 28 ? 20 : rho - 8;
    KV1 = MFMA32(pack_step(Z1, 1), v1, KV1);
    P = MFMA32(A0, t00, z16); P = MFMA32(A1, t01, P);
    if (d >= 0 && d <= 20) {
      const unsigned kb = L_KVR + w * 3360 + d * 160;
#pragma unroll
      for (int g = 0; g < 4; ++g) *(LAS f32x4*)(size_t)(kb + (8 * g + 4 * h) * 4) = (f32x4){KV0[4 * g], KV0[4 * g + 1], KV0[4 * g + 2], KV0[4 * g + 3]};
      if (h == 0) { *(LAS f32x4*)(size_t)(kb + 128) = (f32x4){KV1[0], KV1[1], KV1[2], KV1[3]}; *(LAS f32x4*)(size_t)(kb + 144) = (f32x4){KV1[4], KV1[5], KV1[6], KV1[7]}; }
    }
  }
  STAMPL(29);
  LBAR();
  {
    const int c = tid & 255, hc = tid >> 8;
    u32x4 o = {0u, 0u, 0u, 0u};
    if (c < 105) {
      float a[8];
#pragma unroll
      for (int e = 0; e < 8; ++e) a[e] = 0.f;
#pragma unroll
      for (int g = 0; g < 4; ++g) {
        const unsigned ad = L_KVR + (hc * 4 + g) * 3360 + c * 32;
        const f32x4 u0 = *(LAS f32x4*)(size_t)ad, u1 = *(LAS f32x4*)(size_t)(ad + 16);
#pragma unroll
        for (int e = 0; e < 4; ++e) { a[e] += u0[e]; a[4 + e] += u1[e]; }
      }
      o = (u32x4){pk2(a[0], a[1]), pk2(a[2], a[3]), pk2(a[4], a[5]), pk2(a[6], a[7])};
    }
    st_b128_sc1_m(o, rws, c < 105 ? WS_KVP + ((cg * 2 + hc) * 16 + rb) * KVP_STRIDE + c * 16 : OOB, tg == 0 ? M_ALL : (tg == 1 ? 0x1ffffffffffull : M_ONE));
  }
  STAMPL(30);
  const rsrc_t rout = mk_rsrc(out, S * D * 2);
  unsigned phq[10];
  lda(4); lda(5); SB();
  { f32x2 ep[5], en[5]; phi_map1(P, psc, ep, en); SB(); phi_map2(ep, en, phq); }
#pragma unroll
  for (int r = 0; r < 10; ++r) TIE(phq[r]);
  STAMPL(31);
  WAITV_BAR(4);
  st_b64_sc1_m(tag, rws, w == 0 ? WS_FKV + (cg * 16 + rb) * 8 : OOB, M_ONE);
  issueB(3); lda(6); lda(7); issueB(4);
  STAMP(5);
  STAMPL(32);
  f32x4 a2[2][5];
#pragma unroll
  for (int a = 0; a < 2; ++a)
#pragma unroll
    for (int c = 0; c < 5; ++c) a2[a][c] = (f32x4){0.f, 0.f, 0.f, 0.f};
  u32x4 kp[8]; u32x2 xr[3]; unsigned go[3]; s8 bfr[2][5];
  const int kc = (64 * tg + l) >> 1; const bool kact = kc < 105;
  static_for<0, 17>([&](auto T_) {
    constexpr int t = decltype(T_)::v;
    if constexpr (t == 4) STAMPF(16);
    if constexpr (t == 7) STAMPF(17);
    if constexpr (t == 11) STAMPF(18);
    if constexpr (t == 12) STAMPF(19);
    constexpr int T_PKV = 2, T_KVL = 3, T_SUM = 5, T_O = 6, T_FO = 9, T_PO = 11, T_AO = 12;
    constexpr int allow_tab[17] = {23, 23, 19, 5, 22, 6, 17, 11, 11, 9, 10, 10, 3, 6, 4, 5, 3};
    STAMPL(33 + t);
    asm volatile("s_waitcnt vmcnt(%0) lgkmcnt(0)" ::"i"(allow_tab[t]) : "memory");
    if constexpr (t == T_KVL) { TIE(pf); if (w == 0) { if (__builtin_expect(!flags_ok(pf, tag, l < 16), 0)) wait_flags(rws, WS_FKV + cg * 128, 8, 16, tag, l); } }
    if constexpr (t == T_AO) { TIE(pf); if (w == 0) { if (__builtin_expect(!flags_ok(pf, tag, l < 16), 0)) wait_flags(rws, WS_FO + (r2 >> 1) * 128, 8, 16, tag, l); } }
    asm volatile("s_barrier" ::: "memory");
    if constexpr (t == T_PKV) pf = POLL16(WS_FKV + cg * 128 + l * 8);
    if constexpr (t == T_KVL) {
#pragma unroll
      for (int r = 0; r < 8; ++r) kp[r] = ld_b128_sc1_m(rws, kact ? WS_KVP + ((cg * 2 + ch) * 16 + 8 * (l & 1)) * KVP_STRIDE + kc * 16 : OOB, r * KVP_STRIDE, tg < 3 ? M_ALL : 0x3ffffull);
    }
    if constexpr (t == T_SUM) {
      float a[8];
#pragma unroll
      for (int r = 0; r < 8; ++r) { TIE(kp[r]);
#pragma unroll
        for (int e = 0; e < 4; ++e) { a[2 * e] = r == 0 ? bf_lo(kp[r][e]) : sadd(a[2 * e], bf_lo(kp[r][e])); a[2 * e + 1] = r == 0 ? bf_hi(kp[r][e]) : sadd(a[2 * e + 1], bf_hi(kp[r][e])); } }
      asm volatile("s_nop 1" : "+v"(a[0]), "+v"(a[1]), "+v"(a[2]), "+v"(a[3]), "+v"(a[4]), "+v"(a[5]), "+v"(a[6]), "+v"(a[7]));
#pragma unroll
      for (int e = 0; e < 8; ++e) a[e] = dpp_add(a[e], 0);
      if (kact && (l & 1) == 0) { const int d = kc / 5, cc = kc % 5; *(LAS u32x4*)(size_t)(L_KVS + ch * 2016 + d * 96 + cc * 16) = (u32x4){pk2(a[0], a[1]), pk2(a[2], a[3]), pk2(a[4], a[5]), pk2(a[6], a[7])}; }
    }
    if constexpr (t == T_O) {
      const int reg = (l31 & 3) + 4 * (l31 >> 3), hh = (l31 >> 2) & 1;
      int d;
      if (reg >= 12) d = 20; else if (ch == 0) d = reg < 8 ? 8 * hh + reg : (hh == 0 ? 8 + reg : 20); else d = reg < 8 ? (hh == 0 ? 4 + reg : 12 + reg) : (hh == 0 ? reg - 8 : 20);
      const unsigned ka = L_KVS + ch * 2016 + d * 96;
      const s8 A0 = lds_b128(ka + h * 16), A1 = lds_b128(ka + 32 + h * 16);
      const u32x2 a2l = *(LAS u32x2*)(size_t)(ka + 64 + h * 8);
      const s8 A2 = __builtin_bit_cast(s8, (u32x4){a2l[0], a2l[1], 0u, 0u});
      f32x16 O;
#pragma unroll
      for (int r = 0; r < 16; ++r) O[r] = 0.f;
      O = MFMA32(A0, __builtin_bit_cast(s8, (u32x4){phq[0], phq[1], phq[2], phq[3]}), O);
      O = MFMA32(A1, __builtin_bit_cast(s8, (u32x4){phq[4], phq[5], phq[6], phq[7]}), O);
      O = MFMA32(A2, __builtin_bit_cast(s8, (u32x4){phq[8], phq[9], 0u, 0u}), O);
      float rc = __builtin_amdgcn_rcpf(O[12] + 1e-6f);
      asm volatile("s_nop 1" : "+v"(rc));
      const unsigned G0 = 192 + 5 * cg + (ch == 0 ? h : 3 + h);
      st_b128_sc1((u32x4){pk2(smul(O[0], rc), smul(O[1], rc)), pk2(smul(O[2], rc), smul(O[3], rc)), pk2(smul(O[4], rc), smul(O[5], rc)), pk2(smul(O[6], rc), smul(O[7], rc))}, rws, actbase + (G0 >> 2) * 1024 + (G0 & 3) * 256);
      const unsigned G2 = 192 + 5 * cg + 2;
      st_b64_sc1_m((u32x2){pk2(smul(O[8], rc), smul(O[9], rc)), pk2(smul(O[10], rc), smul(O[11], rc))}, rws, actbase + (G2 >> 2) * 1024 + (G2 & 3) * 256 + ch * 8, 0xffffffffull);
    }
    if constexpr (t == T_FO) st_b64_sc1_m(tag, rws, w == 0 ? WS_FO + (rb * 16 + cg) * 8 : OOB, M_ONE);
    if constexpr (t == T_PO) pf = POLL16(WS_FO + (r2 >> 1) * 128 + l * 8);
    if constexpr (t == T_AO) { lda(12); lda(13); lda(14); lda(15); lda(16); }
    if constexpr (t == 0) { lda(8); lda(9); issueB(5); }
    if constexpr (t == 1) lda(10);
    if constexpr (t == 2) lda(11);
    if constexpr (t >= 1 && t + 5 < 17) issueB(t + 5);
    if constexpr (t == 14) {
#pragma unroll
      for (int z = 0; z < 3; ++z) { const int ix = kq + 4 * z; const bool v = ix < 10; const int a = ix >= 5 ? 1 : 0, c = ix - 5 * a;
        go[z] = v ? (unsigned)((64 * r2 + 32 * rh + 16 * a + (l & 15)) * 1280 + (80 * nb + 16 * c + 4 * (l >> 4)) * 2) : OOB; xr[z] = ld_b64_m(rx, go[z], v ? M_ALL : M_ONE); }
    }
    if constexpr (t == 12) WAITV(8);
    TIE(af[t % 10][0]); TIE(af[t % 10][1]);
    if constexpr (t == 0) {
#pragma unroll
      for (int c = 0; c < 5; ++c) bfr[0][c] = lds_b128(L_BRING + kq * 5120 + l * 16 + c * 1024);
    }
    SB();
#pragma unroll
    for (int c = 0; c < 5; ++c) {
      a2[0][c] = MFMA16(bfr[t & 1][c], af[t % 10][0], a2[0][c]);
      a2[1][c] = MFMA16(bfr[t & 1][c], af[t % 10][1], a2[1][c]);
      if constexpr (t + 1 < 17) bfr[(t + 1) & 1][c] = lds_b128(L_BRING + ((t + 1) % 6) * B_STAGE + kq * 5120 + l * 16 + c * 1024);
      SB();
    }
  });
  STAMP(6);
  STAMPL(50);
  LBAR();
  static_for<0, 10>([&](auto I_) { constexpr int ix = decltype(I_)::v;
    if ((ix & 3) != kq) *(LAS f32x4*)(size_t)(L_RED + ((rh * 10 + ix) * 3 + ((kq - ix - 1) & 3)) * 1024 + l * 16) = a2[ix / 5][ix % 5]; });
  asm volatile("s_waitcnt vmcnt(0) lgkmcnt(0)\n\ts_barrier" ::: "memory");
  auto fin = [&](auto I_, int z) { constexpr int ix = decltype(I_)::v;
    const unsigned ad = L_RED + (rh * 10 + ix) * 3 * 1024 + l * 16;
    const f32x4 v = (a2[ix / 5][ix % 5] + *(LAS f32x4*)(size_t)ad) + (*(LAS f32x4*)(size_t)(ad + 1024) + *(LAS f32x4*)(size_t)(ad + 2048));
    st_b64((u32x2){pk2(v[0] + bf_lo(xr[z][0]), v[1] + bf_hi(xr[z][0])), pk2(v[2] + bf_lo(xr[z][1]), v[3] + bf_hi(xr[z][1]))}, rout, go[z]); };
#pragma unroll
  for (int z = 0; z < 3; ++z) TIE(xr[z]);
  if (kq == 0) { fin(IC<0>{}, 0); fin(IC<4>{}, 1); fin(IC<8>{}, 2); }
  else if (kq == 1) { fin(IC<1>{}, 0); fin(IC<5>{}, 1); fin(IC<9>{}, 2); }
  else if (kq == 2) { fin(IC<2>{}, 0); fin(IC<6>{}, 1); }
  else { fin(IC<3>{}, 0); fin(IC<7>{}, 1); }
  asm volatile(".Lfk_end:");
  if (b == 0 && tid == 0) st_b64_sc1(tag, rws, WS_EPOCH);
  STAMPL(51);
  STAMP(7);
}

extern "C" void kernel_launch(void* const* d_in, const int* in_sizes, int n_in, void* d_out, int out_size, void* d_ws, size_t ws_size, hipStream_t stream) {
  (void)in_sizes; (void)n_in; (void)out_size; (void)ws_size;
  hipFuncSetAttribute((const void*)fused_k, hipFuncAttributeMaxDynamicSharedMemorySize, LDS_BYTES);
  fused_k<<<256, 512, LDS_BYTES, stream>>>((const u16*)d_in[0], (const u16*)d_in[1], (const u16*)d_in[2], (const u16*)d_in[3], (const u16*)d_in[5], (const u16*)d_in[6], (const u16*)d_in[7], (u16*)d_out, (char*)d_ws);
}
```

```cpp
#include <hip/hip_runtime.h>
#include <hip/hip_bf16.h>
typedef unsigned short u16;
typedef unsigned long long u64;
typedef short s4 __attribute__((ext_vector_type(4)));
typedef short s8 __attribute__((ext_vector_type(8)));
typedef __bf16 b8 __attribute__((ext_vector_type(8)));
typedef __bf16 b2 __attribute__((ext_vector_type(2)));
typedef float f32x16 __attribute__((ext_vector_type(16)));
typedef float f32x4 __attribute__((ext_vector_type(4)));
typedef float f32x2 __attribute__((ext_vector_type(2)));
typedef unsigned u32x4 __attribute__((ext_vector_type(4)));
typedef unsigned u32x2 __attribute__((ext_vector_type(2)));
#define LAS __attribute__((address_space(3)))
#define DI __device__ __forceinline__
typedef __amdgpu_buffer_rsrc_t rsrc_t;

constexpr int S = 2048, D = 640, F = 1536;
constexpr unsigned WS_EPOCH = 0;
constexpr unsigned WS_FP0 = 1024, WS_FP1 = 3072, WS_FH = 5120, WS_FO = 7168, WS_FKV = 9216, WS_FCS = 11264;
constexpr unsigned WS_STATS = 16384;
constexpr unsigned WS_KVP = 32768;
constexpr unsigned KVP_STRIDE = 1792;
constexpr unsigned WS_WQP = 1u << 20;
constexpr unsigned WS_W2P = 4u << 20;
constexpr unsigned WS_CSP = 7u << 20;
constexpr unsigned WS_ACT = 8u << 20;
constexpr unsigned WS_USED = 17u << 20;
constexpr unsigned OOB = 0x80000000u;
constexpr unsigned L_X = 0;
constexpr unsigned L_W = 49152;
constexpr unsigned W_STAGE = 24832, W_UOFF = 12320;
constexpr unsigned L_MISC = L_W + 4 * W_STAGE;
constexpr unsigned L_W2S = L_MISC;
constexpr unsigned L_CS = L_W2S;
constexpr unsigned L_WPF = L_W2S + 7168;
constexpr unsigned L_QRAW = L_W + 3 * W_STAGE;
constexpr unsigned L_WPR = L_QRAW + 12288;
constexpr unsigned L_W2A = L_QRAW + 14336;
constexpr unsigned L_BRING = 0;
constexpr unsigned B_STAGE = 20480;
constexpr unsigned L_KVR = 122880;
constexpr unsigned L_KVS = 149760;
constexpr unsigned L_DUMMY1 = L_QRAW + 23552;
constexpr unsigned L_DUMMY = L_W2S + 6144;
constexpr unsigned L_RED = 0;
constexpr unsigned LDS_BYTES = 160 * 1024;

DI rsrc_t mk_rsrc(const void* p, unsigned bytes) { return __builtin_amdgcn_make_buffer_rsrc((void*)p, 0, bytes, 0x00020000); }
DI void dma16(unsigned lds_addr, unsigned voff, rsrc_t r, unsigned soff) {
  asm volatile("s_mov_b32 m0, %0\n\ts_nop 0\n\tbuffer_load_dwordx4 %1, %2, %3 offen lds" ::"s"(lds_addr), "v"(voff), "s"(r), "s"(soff) : "memory");
}
DI void dma16_sc1(unsigned lds_addr, unsigned voff, rsrc_t r, unsigned soff) {
  asm volatile("s_mov_b32 m0, %0\n\ts_nop 0\n\tbuffer_load_dwordx4 %1, %2, %3 offen sc1 lds" ::"s"(lds_addr), "v"(voff), "s"(r), "s"(soff) : "memory");
}
DI unsigned ld_u16(rsrc_t r, unsigned voff, unsigned soff) { unsigned v; asm volatile("buffer_load_ushort %0, %1, %2, %3 offen" : "=v"(v) : "v"(voff), "s"(r), "s"(soff) : "memory"); return v; }
DI unsigned ld_b32(rsrc_t r, unsigned voff, unsigned soff) { unsigned v; asm volatile("buffer_load_dword %0, %1, %2, %3 offen" : "=v"(v) : "v"(voff), "s"(r), "s"(soff) : "memory"); return v; }
DI u32x2 ld_b64_sc1(rsrc_t r, unsigned voff, unsigned soff) { u32x2 v; asm volatile("buffer_load_dwordx2 %0, %1, %2, %3 offen sc1" : "=v"(v) : "v"(voff), "s"(r), "s"(soff) : "memory"); return v; }
DI u32x4 ld_b128_sc1(rsrc_t r, unsigned voff, unsigned soff) { u32x4 v; asm volatile("buffer_load_dwordx4 %0, %1, %2, %3 offen sc1" : "=v"(v) : "v"(voff), "s"(r), "s"(soff) : "memory"); return v; }
DI u32x2 ld_b64_m(rsrc_t r, unsigned voff, u64 mask) { u32x2 v; asm volatile("s_mov_b64 exec, %3\n\tbuffer_load_dwordx2 %0, %1, %2, 0 offen\n\ts_mov_b64 exec, -1" : "=v"(v) : "v"(voff), "s"(r), "s"(mask) : "memory"); return v; }
DI void st_b64(u32x2 v, rsrc_t r, unsigned voff) { asm volatile("buffer_store_dwordx2 %0, %1, %2, 0 offen" ::"v"(v), "v"(voff), "s"(r) : "memory"); }
DI u32x4 ld_b128(rsrc_t r, unsigned voff, unsigned soff) { u32x4 v; asm volatile("buffer_load_dwordx4 %0, %1, %2, %3 offen" : "=v"(v) : "v"(voff), "s"(r), "s"(soff) : "memory"); return v; }
DI void st_b128_sc1(u32x4 v, rsrc_t r, unsigned voff) {
 asm volatile("buffer_store_dwordx4 %0, %1, %2, 0 offen sc1\n\ts_nop 1" ::"v"(v), "v"(voff), "s"(r) : "memory"); }
DI void st_b64_sc1(u32x2 v, rsrc_t r, unsigned voff) { asm volatile("buffer_store_dwordx2 %0, %1, %2, 0 offen sc1" ::"v"(v), "v"(voff), "s"(r) : "memory"); }
DI void st_b128(u32x4 v, rsrc_t r, unsigned voff) { asm volatile("buffer_store_dwordx4 %0, %1, %2, 0 offen\n\ts_nop 1" ::"v"(v), "v"(voff), "s"(r) : "memory"); }
DI void st_b64_sc1_m(u32x2 v, rsrc_t r, unsigned voff, u64 mask) {
 asm volatile("s_mov_b64 exec, %3\n\tbuffer_store_dwordx2 %0, %1, %2, 0 offen sc1\n\ts_mov_b64 exec, -1" ::"v"(v), "v"(voff), "s"(r), "s"(mask) : "memory"); }
DI void st_b32_sc1_m(unsigned v, rsrc_t r, unsigned voff, u64 mask) { asm volatile("s_mov_b64 exec, %3\n\tbuffer_store_dword %0, %1, %2, 0 offen sc1\n\ts_mov_b64 exec, -1" ::"v"(v), "v"(voff), "s"(r), "s"(mask) : "memory"); }
DI u32x2 ld_b64_sc1_m(rsrc_t r, unsigned voff, u64 mask) { u32x2 v; asm volatile("s_mov_b64 exec, %3\n\tbuffer_load_dwordx2 %0, %1, %2, 0 offen sc1\n\ts_mov_b64 exec, -1" : "=v"(v) : "v"(voff), "s"(r), "s"(mask) : "memory"); return v; }
DI unsigned ld_b32_sc1_m(rsrc_t r, unsigned voff, unsigned soff, u64 mask) { unsigned v; asm volatile("s_mov_b64 exec, %4\n\tbuffer_load_dword %0, %1, %2, %3 offen sc1\n\ts_mov_b64 exec, -1" : "=v"(v) : "v"(voff), "s"(r), "s"(soff), "s"(mask) : "memory"); return v; }
DI u32x4 ld_b128_sc1_m(rsrc_t r, unsigned voff, unsigned soff, u64 mask) { u32x4 v; asm volatile("s_mov_b64 exec, %4\n\tbuffer_load_dwordx4 %0, %1, %2, %3 offen sc1\n\ts_mov_b64 exec, -1" : "=v"(v) : "v"(voff), "s"(r), "s"(soff), "s"(mask) : "memory"); return v; }
DI void st_b128_sc1_m(u32x4 v, rsrc_t r, unsigned voff, u64 mask) {
 asm volatile("s_mov_b64 exec, %3\n\tbuffer_store_dwordx4 %0, %1, %2, 0 offen sc1\n\ts_mov_b64 exec, -1\n\ts_nop 0" ::"v"(v), "v"(voff), "s"(r), "s"(mask) : "memory"); }
DI void dma16_m(unsigned lds_addr, unsigned voff, rsrc_t r, unsigned soff, u64 mask) {
  asm volatile("s_mov_b32 m0, %0\n\ts_mov_b64 exec, %4\n\tbuffer_load_dwordx4 %1, %2, %3 offen lds\n\ts_mov_b64 exec, -1" ::"s"(lds_addr), "v"(voff), "s"(r), "s"(soff), "s"(mask) : "memory");
}
#define POLL16(off) ld_b64_sc1_m(rws, w == 0 ? (off) : OOB, w == 0 ? 0xffffull : M_ONE)
#define TIE(x) asm volatile("" : "+v"(x))
#define WAITV(n) asm volatile("s_waitcnt vmcnt(" #n ")" ::: "memory")
#define WAITV_BAR(n) asm volatile("s_waitcnt vmcnt(" #n ")\n\ts_barrier" ::: "memory")
#define WAITVL_BAR(n) asm volatile("s_waitcnt vmcnt(" #n ") lgkmcnt(0)\n\ts_barrier" ::: "memory")
#define LBAR() asm volatile("s_waitcnt lgkmcnt(0)\n\ts_barrier" ::: "memory")
#define SB() __builtin_amdgcn_sched_barrier(0)
#define MFMA32(a, b, c) __builtin_amdgcn_mfma_f32_32x32x16_bf16(__builtin_bit_cast(b8, (a)), __builtin_bit_cast(b8, (b)), (c), 0, 0, 0)
#define MFMA16(a, b, c) __builtin_amdgcn_mfma_f32_16x16x32_bf16(__builtin_bit_cast(b8, (a)), __builtin_bit_cast(b8, (b)), (c), 0, 0, 0)
DI s8 lds_b128(unsigned addr) { return *(LAS s8*)(size_t)addr; }
DI s8 tr_frag(unsigned addr, unsigned off2) {
  s4 a = __builtin_amdgcn_ds_read_tr16_b64_v4i16((LAS s4*)(size_t)addr);
  s4 b = __builtin_amdgcn_ds_read_tr16_b64_v4i16((LAS s4*)(size_t)(addr + off2));
  return __builtin_shufflevector(a, b, 0, 1, 2, 3, 4, 5, 6, 7);
}
DI float dot_ones(s8 f, float acc) {
  u32x4 u = __builtin_bit_cast(u32x4, f);
#pragma unroll
  for (int i = 0; i < 4; ++i) { const unsigned t = u[i]; acc = __builtin_amdgcn_fdot2_f32_bf16(__builtin_bit_cast(b2, t), __builtin_bit_cast(b2, 0x3f803f80u), acc, false); }
  asm volatile("" : "+v"(acc));
  return acc;
}
DI unsigned pk2(float lo, float hi) { b2 c; c[0] = (__bf16)lo; c[1] = (__bf16)hi; return __builtin_bit_cast(unsigned, c); }
DI float bf_lo(unsigned u) { return __uint_as_float(u << 16); }
DI float bf_hi(unsigned u) { return __uint_as_float(u & 0xffff0000u); }
DI float swap_add(float v) { unsigned u = __float_as_uint(v); auto r = __builtin_amdgcn_permlane32_swap(u, u, false, false); return __uint_as_float(r[0]) + __uint_as_float(r[1]); }
DI float swap_max(float v) { unsigned u = __float_as_uint(v); auto r = __builtin_amdgcn_permlane32_swap(u, u, false, false); return fmaxf(__uint_as_float(r[0]), __uint_as_float(r[1])); }
DI float swap_min(float v) { unsigned u = __float_as_uint(v); auto r = __builtin_amdgcn_permlane32_swap(u, u, false, false); return fminf(__uint_as_float(r[0]), __uint_as_float(r[1])); }
DI float sadd(float a, float b) { float r; asm("v_add_f32 %0, %1, %2" : "=v"(r) : "v"(a), "v"(b)); return r; }
DI float smul(float a, float b) { float r; asm("v_mul_f32 %0, %1, %2" : "=v"(r) : "v"(a), "v"(b)); return r; }
DI float sfma(float a, float b, float c) { float r; asm("v_fma_f32 %0, %1, %2, %3" : "=v"(r) : "v"(a), "v"(b), "v"(c)); return r; }
DI float dpp_add(float v, const int ctrl_sel) {
  const int u = __float_as_int(v); int r;
  if (ctrl_sel == 0) r = __builtin_amdgcn_update_dpp(0, u, 0xB1, 0xf, 0xf, true);
  else if (ctrl_sel == 1) r = __builtin_amdgcn_update_dpp(0, u, 0x4E, 0xf, 0xf, true);
  else if (ctrl_sel == 2) r = __builtin_amdgcn_update_dpp(0, u, 0x141, 0xf, 0xf, true);
  else r = __builtin_amdgcn_update_dpp(0, u, 0x140, 0xf, 0xf, true);
  return v + __int_as_float(r);
}
DI float wave_sum(float v) {
  v = dpp_add(v, 0); v = dpp_add(v, 1); v = dpp_add(v, 2); v = dpp_add(v, 3);
  { unsigned u = __float_as_uint(v); auto r = __builtin_amdgcn_permlane16_swap(u, u, false, false); v = __uint_as_float(r[0]) + __uint_as_float(r[1]); }
  return swap_add(v);
}
DI float sum8(s8 f, float acc) {
  const u32x4 u = __builtin_bit_cast(u32x4, f);
#pragma unroll
  for (int i = 0; i < 4; ++i) { const unsigned t = u[i]; acc += __uint_as_float(t << 16); acc += __uint_as_float(t & 0xffff0000u); }
  asm volatile("" : "+v"(acc));
  return acc;
}
DI s8 pack_step(const f32x16& x, int s) {
  return __builtin_bit_cast(s8, (u32x4){pk2(x[8 * s], x[8 * s + 1]), pk2(x[8 * s + 2], x[8 * s + 3]), pk2(x[8 * s + 4], x[8 * s + 5]), pk2(x[8 * s + 6], x[8 * s + 7])});
}
template <int I> struct IC { static constexpr int v = I; };
template <int B, int E, class Fn> DI void static_for(Fn&& f) { if constexpr (B < E) { f(IC<B>{}); static_for<B + 1, E>(f); } }
DI bool flags_ok(u32x2 v, u32x2 tag, bool act) { return __all(!act || (v[0] == tag[0] && v[1] == tag[1])); }
DI void wait_flags(rsrc_t rws, unsigned base, unsigned stride, int n, u32x2 tag, int l) {

  const bool act = l < n; const unsigned off = act ? base + l * stride : OOB;
#pragma nounroll
  for (int it = 0; it < (1 << 17); ++it) {
    u32x2 v; asm volatile("buffer_load_dwordx2 %0, %1, %2, 0 offen sc1\n\ts_waitcnt vmcnt(0)" : "=&v"(v) : "v"(off), "s"(rws) : "memory");
    if (flags_ok(v, tag, act)) return;
    __builtin_amdgcn_s_sleep(1);
  }
}
DI void wait_flags256(rsrc_t rws, unsigned base, u32x2 tag, int l) {
#pragma nounroll
  for (int it = 0; it < (1 << 17); ++it) {
    u32x4 a, b;
    asm volatile("buffer_load_dwordx4 %0, %2, %3, 0 offen sc1\n\tbuffer_load_dwordx4 %1, %2, %3, 0 offen offset:16 sc1\n\ts_waitcnt vmcnt(0)" : "=&v"(a), "=&v"(b) : "v"(base + l * 32), "s"(rws) : "memory");
    const bool ok = a[0] == tag[0] && a[1] == tag[1] && a[2] == tag[0] && a[3] == tag[1] && b[0] == tag[0] && b[1] == tag[1] && b[2] == tag[0] && b[3] == tag[1];
    if (__all(ok)) return;
    __builtin_amdgcn_s_sleep(1);
  }
}
DI bool flags256_ok(u32x4 a, u32x4 b, u32x2 tag) {
  return __all(a[0] == tag[0] && a[1] == tag[1] && a[2] == tag[0] && a[3] == tag[1] && b[0] == tag[0] && b[1] == tag[1] && b[2] == tag[0] && b[3] == tag[1]);
}
DI float max3(float a, float b, float c) { float r; asm("v_max3_f32 %0, %1, %2, %3" : "=v"(r) : "v"(a), "v"(b), "v"(c)); return r; }
DI float min3(float a, float b, float c) { float r; asm("v_min3_f32 %0, %1, %2, %3" : "=v"(r) : "v"(a), "v"(b), "v"(c)); return r; }
DI f32x2 exp2_2(f32x2 a) { f32x2 r; r[0] = __builtin_amdgcn_exp2f(a[0]); r[1] = __builtin_amdgcn_exp2f(a[1]); return r; }
DI unsigned pk2v(f32x2 a) { return pk2(a[0], a[1]); }
DI void phi_map1(const f32x16& pin, float sc, f32x2 (&ep)[5], f32x2 (&en)[5]) {
  float p[10];
#pragma unroll
  for (int r = 0; r < 10; ++r) p[r] = pin[r];
  asm volatile("s_nop 7\n\ts_nop 3" : "+v"(p[0]), "+v"(p[1]), "+v"(p[2]), "+v"(p[3]), "+v"(p[4]), "+v"(p[5]), "+v"(p[6]), "+v"(p[7]), "+v"(p[8]), "+v"(p[9]));
  float mx = max3(p[0], p[1], p[2]), mn = min3(p[0], p[1], p[2]);
#pragma unroll
  for (int r = 3; r < 9; r += 2) { mx = max3(mx, p[r], p[r + 1]); mn = min3(mn, p[r], p[r + 1]); }
  mx = max3(mx, p[9], p[9]); mn = min3(mn, p[9], p[9]);
  { unsigned u = __float_as_uint(mx); auto r = __builtin_amdgcn_permlane32_swap(u, u, false, false); mx = max3(__uint_as_float(r[0]), __uint_as_float(r[1]), __uint_as_float(r[1])); }
  { unsigned u = __float_as_uint(mn); auto r = __builtin_amdgcn_permlane32_swap(u, u, false, false); mn = min3(__uint_as_float(r[0]), __uint_as_float(r[1]), __uint_as_float(r[1])); }
  const f32x2 s2 = {sc, sc}, bp = {-mx * sc, -mx * sc}, bn = {mn * sc, mn * sc};
#pragma unroll
  for (int r = 0; r < 5; ++r) { const f32x2 v = {p[2 * r], p[2 * r + 1]}; ep[r] = exp2_2(__builtin_elementwise_fma(v, s2, bp)); en[r] = exp2_2(__builtin_elementwise_fma(v, -s2, bn)); }
}
DI void phi_map2(const f32x2 (&ep)[5], const f32x2 (&en)[5], unsigned (&o)[10]) {
  const f32x2 sp2 = (ep[0] + ep[1]) + (ep[2] + ep[3]) + ep[4], sn2 = (en[0] + en[1]) + (en[2] + en[3]) + en[4];
  const float rp = __builtin_amdgcn_rcpf(swap_add(sp2[0] + sp2[1])), rn = __builtin_amdgcn_rcpf(swap_add(sn2[0] + sn2[1]));
  const f32x2 rp2 = {rp, rp}, rn2 = {rn, rn};
#pragma unroll
  for (int r = 0; r < 5; ++r) { o[r] = pk2v(ep[r] * rp2); o[5 + r] = pk2v(en[r] * rn2); }
}

#define STAMP(i)
#define STAMPL(k)
#define STAMPC(i)
#define STAMPF(i)

__global__ __launch_bounds__(512, 2) void fused_k(const u16* __restrict__ x, const u16* __restrict__ wqkv, const u16* __restrict__ wphi, const u16* __restrict__ wo,
                                                   const u16* __restrict__ wg, const u16* __restrict__ wu, const u16* __restrict__ wd, u16* __restrict__ out, char* __restrict__ ws) {
  extern __shared__ __attribute__((aligned(256))) char smem[];
  const int tid = threadIdx.x; const int l = tid & 63; const int w = __builtin_amdgcn_readfirstlane(tid >> 6);
  const int tg = w & 3, ch = w >> 2; const int l31 = l & 31, h = l >> 5;
  const int b = blockIdx.x, xcd = b & 7, idx = b >> 3;
  const int rb = (xcd & 1) * 8 + (idx & 7), cg = (xcd >> 1) * 4 + (idx >> 3);
  const int r2 = (xcd & 3) * 8 + (idx & 7), nb = (xcd >> 2) * 4 + (idx >> 3);
  const int klo = (5 * rb) >> 1, kcnt = ((5 * (rb + 1)) >> 1) - klo, rot = klo >> 2, kloc = klo - 4 * rot;
  STAMP(0);
  STAMPL(0);
  const rsrc_t rx = mk_rsrc(x, S * D * 2);
  const rsrc_t rw = mk_rsrc(w < 4 ? wg : wu, D * F * 2);
  const rsrc_t rws = mk_rsrc(ws, WS_USED);
  const u64 M_ALL = ~0ull, M_ONE = 1ull;
  auto sig = [&](int s) -> unsigned { const int t = s + rot; return t >= 10 ? t - 10 : t; };
  unsigned vx[2], vw[3];
#pragma unroll
  for (int i = 0; i < 2; ++i) { const int r = 8 * (2 * w + i) + (l >> 3); const int c = (l & 7) ^ ((r >> 1) & 7); vx[i] = (128 * rb + r) * 1280 + c * 16; }
#pragma unroll
  for (int i = 0; i < 3; ++i) { const int p = 64 * (3 * (w & 3) + i) + l; const int k = p / 12, c = p % 12; vw[i] = k * 3072 + cg * 192 + c * 16; }
  const unsigned lx0 = L_X + 2 * w * 1024;
  const unsigned lw0 = L_W + (w < 4 ? 0 : W_UOFF) + 3 * (w & 3) * 1024;
  auto dmax = [&](int s, int i) { dma16(lx0 + (s % 3) * 16384 + i * 1024, vx[i], rx, sig(s) * 128); };
  auto dmaw = [&](int s, int i) { dma16(lw0 + (s & 3) * W_STAGE + i * 1024, vw[i], rw, sig(s) * (64 * 3072)); };
  auto issue1 = [&](int s) { dmax(s, 0); dmax(s, 1); dmaw(s, 0); dmaw(s, 1); dmaw(s, 2); };
  issue1(0);
  u32x2 ep = ld_b64_sc1_m(rws, WS_EPOCH, M_ONE);
  const int srow = 128 * rb + 8 * cg + w;
  const bool v3 = kcnt == 3 && w < 4;
  u32x4 sx0, sx1;
  {
    const rsrc_t rq = mk_rsrc(wqkv, D * 1920 * 2);
    const int p = 64 * (w & 3) + l; const int k = p / 15, c = p % 15;
    const unsigned vo = p < 240 ? (unsigned)(k * 3840 + (c / 5) * 1280 + cg * 80 + (c % 5) * 16) : OOB;
    dma16(L_QRAW + w * 1024, vo, rq, (16 * (klo + (w >> 2))) * 3840);
    dma16_m(v3 ? L_QRAW + 8192 + w * 1024 : L_DUMMY1, v3 ? vo : OOB, rq, v3 ? (16 * (klo + 2)) * 3840 : 0, v3 ? M_ALL : M_ONE);
    const rsrc_t rp = mk_rsrc(wphi, 32 * 400 * 2);
    dma16_m(w < 2 ? L_WPR + w * 1024 : L_DUMMY1, (w < 2 && w * 64 + l < 100) ? (unsigned)(cg * 1600 + (w * 64 + l) * 16) : OOB, rp, 0, w == 0 ? M_ALL : (w == 1 ? 0xfffffffffull : M_ONE));
    sx0 = ld_b128(rx, srow * 1280 + l * 16, 0);
    sx1 = ld_b128(rx, l < 16 ? (unsigned)(srow * 1280 + 1024 + l * 16) : OOB, 0);
    const int kst = b / 5, cb = b % 5; const bool isd = kst < 48;
    const rsrc_t rr = mk_rsrc(isd ? wd : wo, isd ? F * D * 2 : D * D * 2);
    dma16(L_W2A + w * 1024, (unsigned)((l >> 4) * 1280 + (l & 15) * 16), rr, ((isd ? 32 * kst : 32 * (kst - 48)) + 4 * w) * 1280 + cb * 256);
  }
  const int gi = l >> 4, q4 = (l >> 2) & 3, p4 = l & 3;
  issue1(1); issue1(2);
  {
    u64 pc; unsigned len;
    asm volatile("s_getpc_b64 %0\n.Lfk_here:\n\ts_mov_b32 %1, .Lfk_end-.Lfk_here" : "=s"(pc), "=s"(len));
    const rsrc_t rc = mk_rsrc((const void*)pc, len);
    const unsigned off = (idx * 8 + w) * 256;
    asm volatile("s_mov_b32 m0, %0\n\ts_mov_b64 exec, 0xffff\n\tbuffer_load_dwordx4 %1, %2, 0 offen lds\n\ts_mov_b64 exec, -1" ::"s"(L_DUMMY1), "v"(off + l * 16), "s"(rc) : "memory");
  }
  WAITV_BAR(11);
  STAMP(1);
  STAMPL(1);
  TIE(ep);
  u32x2 tag;
  { const unsigned lo = __builtin_amdgcn_readfirstlane(ep[0]), hi = __builtin_amdgcn_readfirstlane(ep[1]); tag[0] = lo + 1u; tag[1] = hi + (lo == 0xffffffffu ? 1u : 0u); }
  const int shc = (w >> 1) & 1, sht = w & 1;
  s8 sf0, sf1, sf2; unsigned se[8]; unsigned smA, smB;
  {
    const int Q = 4 * (gi & 1) + p4;
    int seg, qd;
    if (sht == 0) { seg = Q < 5 ? 0 : 1; qd = Q < 5 ? Q : Q - 5; } else { seg = Q < 2 ? 1 : 2; qd = Q < 2 ? 3 + Q : (Q < 7 ? Q - 2 : 4); }
    const unsigned lo = L_QRAW + (8 * h + q4) * 240 + seg * 80 + shc * 40 + qd * 8;
    sf0 = tr_frag(lo + (w >> 2) * 4096, 960); sf1 = tr_frag(lo + 8192, 960);
    sf2 = tr_frag(L_W2A + (8 * gi + q4) * 256 + w * 32 + p4 * 8, 1024);
    const int reg = (l31 & 3) + 4 * (l31 >> 3), hh = (l31 >> 2) & 1;
    const int d0 = (tg == 0 ? 0 : tg == 1 ? 16 : tg == 2 ? -4 : 12) + 4 * h;
    const unsigned wa = L_WPR + ch * 800 + (10 * hh + reg) * 2 + d0 * 40;
#pragma unroll
    for (int j = 0; j < 8; ++j) se[j] = *(LAS u16*)(size_t)(wa + (8 * (j >> 2) + (j & 3)) * 40);
    smA = (reg < 10 && (unsigned)d0 < 20u) ? ~0u : 0u; smB = (reg < 10 && (unsigned)(d0 + 8) < 20u) ? ~0u : 0u;
  }
  auto side = [&](int part) {
    if (part == 0) {
      TIE(sx0); TIE(sx1);
      float sm = 0.f, sq = 0.f;
#pragma unroll
      for (int i = 0; i < 4; ++i) { const unsigned u0 = sx0[i], u1 = sx1[i];
        sm = __builtin_amdgcn_fdot2_f32_bf16(__builtin_bit_cast(b2, u0), __builtin_bit_cast(b2, 0x3f803f80u), sm, false); sq = __builtin_amdgcn_fdot2_f32_bf16(__builtin_bit_cast(b2, u0), __builtin_bit_cast(b2, u0), sq, false);
        sm = __builtin_amdgcn_fdot2_f32_bf16(__builtin_bit_cast(b2, u1), __builtin_bit_cast(b2, 0x3f803f80u), sm, false); sq = __builtin_amdgcn_fdot2_f32_bf16(__builtin_bit_cast(b2, u1), __builtin_bit_cast(b2, u1), sq, false); }
      const float m = wave_sum(sm) * (1.f / D);
      const float var = fmaxf(wave_sum(sq) * (1.f / D) - m * m, 0.f);
      st_b64_sc1_m((u32x2){__float_as_uint(m), __float_as_uint(__builtin_amdgcn_rsqf(var + 1e-5f))}, rws, WS_STATS + srow * 8, M_ONE);
    } else if (part == 1) {
      st_b128_sc1(__builtin_bit_cast(u32x4, sf0), rws, WS_WQP + (((2 * cg + shc) * 40 + klo + (w >> 2)) * 2 + sht) * 1024 + l * 16);
      st_b128_sc1_m(__builtin_bit_cast(u32x4, sf1), rws, v3 ? WS_WQP + (((2 * cg + shc) * 40 + klo + 2) * 2 + sht) * 1024 + l * 16 : OOB, v3 ? M_ALL : M_ONE);
      const int kst = b / 5, cb = b % 5, ct = 8 * cb + w;
      st_b128_sc1(__builtin_bit_cast(u32x4, sf2), rws, WS_W2P + (((ct / 5) * 68 + kst) * 5 + ct % 5) * 1024 + l * 16);
      const float c0 = swap_add(dot_ones(sf0, 0.f)), c1 = swap_add(dot_ones(sf1, 0.f));
      if (h == 0) { const unsigned qa = L_W2S + ((shc * 2 + sht) * 32 + l31) * 16 + (w >> 2) * 4; *(LAS float*)(size_t)qa = c0; *(LAS float*)(size_t)(qa + 8) = v3 ? c1 : 0.f; }
    } else {
      *(LAS u32x4*)(size_t)(L_WPF + w * 1024 + l * 16) = (u32x4){(se[0] | (se[1] << 16)) & smA, (se[2] | (se[3] << 16)) & smA, (se[4] | (se[5] << 16)) & smB, (se[6] | (se[7] << 16)) & smB};
    }
  };
  STAMPF(8);
  const unsigned qoff = (p4 == 1) ? 8 : (p4 == 2) ? 4 : (p4 == 3) ? 12 : 0;
  const unsigned wl = L_W + ((gi & 1) ? W_UOFF : 0) + (8 * h + q4) * 192 + 2 * qoff + 96 * ch;
  const unsigned wl2 = wl + 2 * W_STAGE;
  const int xrow = 32 * tg + l31;
  unsigned xa[4];
#pragma unroll
  for (int j = 0; j < 4; ++j) { xa[j] = (L_X + xrow * 128 + ((h ^ ((xrow >> 1) & 7)) << 4)) ^ (j * 32); asm volatile("" : "+v"(xa[j])); }
  s8 XF[40];
  f32x16 acc[3]; float cs[3];
#pragma unroll
  for (int i = 0; i < 3; ++i) { cs[i] = 0.f;
#pragma unroll
    for (int r = 0; r < 16; ++r) acc[i][r] = 0.f; }
  s8 wf[2][3];
  auto rdx = [&](int s, int j) { XF[4 * s + j] = lds_b128(xa[j] + (s % 3) * 16384); };
  auto rdw = [&](int n, int i) { const int s = n >> 2, j = n & 3; wf[n & 1][i] = tr_frag(((s & 2) ? wl2 : wl) + (s & 1) * W_STAGE + j * 3072 + i * 32, 768); };
  const unsigned vq2 = WS_WQP + ((2 * cg + ch) * 40 + tg) * 2048 + l * 16;
  auto slot2 = [&](int q) -> unsigned { return q == 0 || q == 7 ? 16384u : q == 1 || q == 8 ? L_W + 2 * W_STAGE : q == 2 || q == 9 ? 32768u : q == 3 ? L_W + 3 * W_STAGE : q == 4 ? 0u : q == 5 ? L_W : L_W + W_STAGE; };
  auto issue2 = [&](int q) { const unsigned so = sig(q) * 8192; dma16_sc1(slot2(q) + w * 2048, vq2, rws, so); dma16_sc1(slot2(q) + w * 2048 + 1024, vq2, rws, so + 1024); };
  const bool hv = b < 168 && w < 4; const int hu = 256 + (b >> 1), hkst = hu / 5, hcb = hu % 5;
  u32x2 pf, pf2, st; u32x4 pl0, pl1, cg4[4]; unsigned cg1[4]; f32x4 qs0, qs1;
  rdx(0, 0); rdw(0, 0); rdw(0, 1); rdw(0, 2); rdx(0, 1); rdx(0, 2); rdx(0, 3);
  static_for<0, 40>([&](auto N_) {
    constexpr int n = decltype(N_)::v;
    constexpr int s = n >> 2, j = n & 3;
    if constexpr (n == 3) STAMPF(9);
    if constexpr (n == 11) STAMPF(10);
    if constexpr (n == 19) STAMPF(11);
    if constexpr (n == 27) STAMPF(12);
    if constexpr (n == 35) STAMPF(13);
    if constexpr (j == 3 && s < 9) STAMPL(2 + 2 * s);
    if constexpr (n == 3) WAITV_BAR(10);
    if constexpr (n == 7) { WAITV_BAR(9); qs0 = *(LAS f32x4*)(size_t)(L_W2S + ((ch * 2) * 32 + l31) * 16); qs1 = *(LAS f32x4*)(size_t)(L_W2S + ((ch * 2 + 1) * 32 + l31) * 16); }
    if constexpr (n == 11) WAITV_BAR(7);
    if constexpr (n == 15) { WAITV_BAR(5); st_b64_sc1_m(tag, rws, w == 0 ? WS_FP0 + (cg * 16 + rb) * 8 : OOB, M_ONE); }
    if constexpr (n == 19) WAITV_BAR(7);
    if constexpr (n == 23) WAITV_BAR(5);
    if constexpr (n == 27) { WAITV_BAR(6); pf = POLL16(WS_FP0 + cg * 128 + l * 8); pf2 = POLL16(WS_FP0 + rb * 8 + (l & 15) * 128); }
    if constexpr (n == 31) {
      WAITV(5); TIE(pf); TIE(pf2);
      if (w == 0) {
        if (__builtin_expect(!flags_ok(pf, tag, l < 16), 0)) wait_flags(rws, WS_FP0 + cg * 128, 8, 16, tag, l);
        if (__builtin_expect(!flags_ok(pf2, tag, l < 16), 0)) wait_flags(rws, WS_FP0 + rb * 8, 128, 16, tag, l);
      }
      asm volatile("s_barrier" ::: "memory");
      st_b64_sc1_m(tag, rws, w == 0 ? WS_FP1 + b * 8 : OOB, M_ONE);
      st = ld_b64_sc1(rws, WS_STATS + (128 * rb + xrow) * 8, 0);
      {
        const unsigned vo = w < 4 ? WS_CSP + (cg * 16 + 4 * w) * 1280 + l * 16 : OOB, vo1 = w < 4 ? WS_CSP + (cg * 16 + 4 * w) * 1280 + 1024 + l * 4 : OOB;
#pragma unroll
        for (int r = 0; r < 4; ++r) { cg4[r] = ld_b128_sc1_m(rws, vo, r * 1280, w < 4 ? M_ALL : M_ONE); cg1[r] = ld_b32_sc1_m(rws, vo1, r * 1280, w < 4 ? M_ALL : M_ONE); } }
      issue2(0); issue2(1);
    }
    if constexpr (n == 35) { WAITV_BAR(14); issue2(2); issue2(3); }
    if constexpr (j == 3 && s < 9) STAMPL(3 + 2 * s);
    SB();
#pragma unroll
    for (int i = 0; i < 3; ++i) {
      acc[i] = MFMA32(wf[n & 1][i], XF[n], acc[i]);
      SB();
      if (i == 0 && j == 3 && s < 9) rdx(s + 1, 0);
      if (n + 1 < 40) rdw(n + 1, i);
      if (j == 0 && s > 0) rdx(s, i + 1);
      if (n < 3 && i == 2) side(n);
      if (n < 8 && i == 2) { if ((unsigned)(n - kloc) < (unsigned)kcnt && tg == 0) { cs[0] = dot_ones(wf[n & 1][0], cs[0]); cs[1] = dot_ones(wf[n & 1][1], cs[1]); cs[2] = dot_ones(wf[n & 1][2], cs[2]); } }
      if (n == 8 && i == 0) {
        const unsigned dst = tg == 0 ? WS_CSP + (cg * 16 + rb) * 1280 + (ch * 32 + l31) * 16 : OOB; const u64 mk = tg == 0 ? 0xffffffffull : M_ONE;
        st_b128_sc1_m((u32x4){__float_as_uint(swap_add(cs[0])), __float_as_uint(swap_add(cs[1])), __float_as_uint(swap_add(cs[2])), __float_as_uint((qs0[0] + qs0[1]) + (qs0[2] + qs0[3]))}, rws, dst, mk);
        st_b32_sc1_m(__float_as_uint((qs1[0] + qs1[1]) + (qs1[2] + qs1[3])), rws, tg == 0 ? WS_CSP + (cg * 16 + rb) * 1280 + 1024 + (ch * 32 + l31) * 4 : OOB, mk);
      }
      if (j == 3 && s + 3 < 10) {
        if (i == 0) { dmax(s + 3, 0); dmax(s + 3, 1); }
        if (i == 1) { dmaw(s + 3, 0); dmaw(s + 3, 1); }
        if (i == 2) dmaw(s + 3, 2);
      }
      if constexpr (n == 15) { if (i == 2) {
        const rsrc_t rr = mk_rsrc(wo, D * D * 2);
        dma16_m(hv ? L_W2S + w * 1024 : L_DUMMY, hv ? (unsigned)((l >> 3) * 1280 + (l & 7) * 16) : OOB, rr, hv ? (32 * (hkst - 48) + 8 * w) * 1280 + hcb * 256 + (b & 1) * 128 : 0, hv ? M_ALL : M_ONE);
      } }
      if constexpr (n == 23) { if (i == 2) {
        const int ct = 8 * hcb + 4 * (b & 1) + (w & 3);
        const s8 f = tr_frag(L_W2S + (8 * gi + q4) * 128 + (w & 3) * 32 + p4 * 8, 512);
        st_b128_sc1_m(__builtin_bit_cast(u32x4, f), rws, hv ? WS_W2P + (((ct / 5) * 68 + hkst) * 5 + ct % 5) * 1024 + l * 16 : OOB, hv ? M_ALL : M_ONE);
      } }
      SB();
    }
  });
  STAMP(2);
  STAMPL(20);
  WAITV(4);
#pragma unroll
  for (int r = 0; r < 4; ++r) { TIE(cg4[r]); TIE(cg1[r]); }
  if (w < 4) { const unsigned ad = L_CS + ((h * 5) * 32 + l31) * 16 + w * 4;
#pragma unroll
    for (int e = 0; e < 4; ++e) *(LAS float*)(size_t)(ad + e * 512) = (__uint_as_float(cg4[0][e]) + __uint_as_float(cg4[1][e])) + (__uint_as_float(cg4[2][e]) + __uint_as_float(cg4[3][e]));
    *(LAS float*)(size_t)(ad + 4 * 512) = (__uint_as_float(cg1[0]) + __uint_as_float(cg1[1])) + (__uint_as_float(cg1[2]) + __uint_as_float(cg1[3])); }
  WAITVL_BAR(0);
  TIE(st);
  const float mean = __uint_as_float(st[0]), rstd = __uint_as_float(st[1]);
  s8 bm;
  { const float nm = -mean; const unsigned hi = pk2(nm, 0.f) & 0xffffu; const float lo = nm - __uint_as_float(hi << 16); unsigned mpk = hi | (pk2(lo, 0.f) << 16); if (h) mpk = 0; bm = __builtin_bit_cast(s8, (u32x4){mpk, mpk, 0u, 0u}); }
  auto cs_frag = [&](int tile) -> s8 {
    const f32x4 pp = *(LAS f32x4*)(size_t)(L_CS + ((ch * 5 + tile) * 32 + l31) * 16);
    const float c = (pp[0] + pp[1]) + (pp[2] + pp[3]);
    unsigned hi = pk2(c, c); const float lo = c - __uint_as_float(hi << 16); unsigned lo2 = pk2(lo, lo);
    if (h) { hi = 0; lo2 = 0; }
    return __builtin_bit_cast(s8, (u32x4){hi, lo2, 0u, 0u});
  };
  const unsigned tok = 128 * rb + xrow;
  const unsigned actbase = WS_ACT + (tok >> 4) * 68 * 1024 + (l & 15) * 16;
  {
    const float gs = -1.4426950408889634f * rstd, irs = __builtin_amdgcn_rcpf(rstd);
#pragma unroll
    for (int i = 0; i < 3; ++i) acc[i] = MFMA32(cs_frag(i), bm, acc[i]);
#pragma unroll
    for (int i = 0; i < 3; ++i) {
      issue2(4 + i);
      const f32x2 gs2 = {gs, gs}, irs2 = {irs, irs};
      u32x4 ho;
#pragma unroll
      for (int r = 0; r < 4; ++r) {
        const f32x2 g2 = {acc[i][2 * r], acc[i][2 * r + 1]}, u2 = {acc[i][8 + 2 * r], acc[i][9 + 2 * r]};
        const f32x2 dn = __builtin_elementwise_fma(exp2_2(g2 * gs2), irs2, irs2);
        const f32x2 rc = {__builtin_amdgcn_rcpf(dn[0]), __builtin_amdgcn_rcpf(dn[1])};
        ho[r] = pk2v(u2 * rc);
      }
      const unsigned G = 12 * cg + 6 * ch + 2 * i + h;
      st_b128_sc1(ho, rws, actbase + (G >> 2) * 1024 + (G & 3) * 256);
      SB();
    }
  }
  STAMP(3);
  STAMPL(21);
  const int kq = w & 3, rh = w >> 2;
  const unsigned va = WS_ACT + (4 * r2 + 2 * rh) * 68 * 1024 + kq * 1024 + l * 16;
  u32x4 af[10][2];
  auto lda = [&](int t) { af[t % 10][0] = ld_b128_sc1(rws, va, t * 4096); af[t % 10][1] = ld_b128_sc1(rws, va + 68 * 1024, t * 4096); };
  f32x16 T[2];
#pragma unroll
  for (int i = 0; i < 2; ++i)
#pragma unroll
    for (int r = 0; r < 16; ++r) T[i][r] = 0.f;
  s8 qf[3][2];
  const unsigned ql = ch * 8192 + l * 16, ql2 = ql + L_W;
  auto rdq = [&](int m, int t) { const int q = m >> 2, j = m & 3; const unsigned sl = slot2(q); qf[m % 3][t] = lds_b128((sl >= L_W ? ql2 + (sl - L_W) : ql + sl) + j * 2048 + t * 1024); };
  rdq(0, 0); rdq(0, 1); rdq(1, 0); rdq(1, 1);
  static_for<0, 40>([&](auto M_) {
    constexpr int m = decltype(M_)::v;
    if constexpr (m == 6) STAMPL(22);
    if constexpr (m == 14) STAMPL(23);
    if constexpr (m == 15) STAMPL(24);
    if constexpr (m == 26) STAMPL(25);
    if constexpr (m == 34) STAMPL(26);
    if constexpr (m == 14) { WAITV_BAR(0); st_b64_sc1_m(tag, rws, w == 0 ? WS_FH + (rb * 16 + cg) * 8 : OOB, M_ONE); issue2(7); issue2(8); issue2(9); }
    if constexpr (m == 26) {
      WAITV_BAR(0);
      pl0 = ld_b128_sc1_m(rws, WS_FP1 + l * 32, 0, w == 0 ? M_ALL : M_ONE); pl1 = ld_b128_sc1_m(rws, WS_FP1 + l * 32 + 16, 0, w == 0 ? M_ALL : M_ONE);
      pf = POLL16(WS_FH + (r2 >> 1) * 128 + l * 8);
    }
    if constexpr (m == 30) {
      WAITV(0); TIE(pl0); TIE(pl1); TIE(pf);
      if (w == 0) {
        if (__builtin_expect(!flags256_ok(pl0, pl1, tag), 0)) wait_flags256(rws, WS_FP1, tag, l);
        if (__builtin_expect(!flags_ok(pf, tag, l < 16), 0)) wait_flags(rws, WS_FH + (r2 >> 1) * 128, 8, 16, tag, l);
      }
      asm volatile("s_barrier" ::: "memory");
    }
    if constexpr (m >= 30 && (m & 1) == 0) lda((m - 30) >> 1);
    SB();
#pragma unroll
    for (int t = 0; t < 2; ++t) {
      T[t] = MFMA32(qf[m % 3][t], XF[m], T[t]);
      SB();
      if (m + 2 < 40) rdq(m + 2, t);
      SB();
    }
  });
  STAMP(4);
  STAMPL(27);
  LBAR();
  STAMPL(28);
  const unsigned vb = WS_W2P + nb * 68 * 5 * 1024 + l * 16;
  const unsigned bf0 = w < 4 ? 3 * w : 12 + 2 * (w - 4);
  auto issueB = [&](int t) {
    const unsigned base = L_BRING + (t % 6) * B_STAGE;
    dma16_sc1(base + bf0 * 1024, vb, rws, t * B_STAGE + bf0 * 1024);
    dma16_sc1(base + (bf0 + 1) * 1024, vb, rws, t * B_STAGE + (bf0 + 1) * 1024);
    asm volatile("s_mov_b32 m0, %0\n\ts_mov_b64 exec, %4\n\tbuffer_load_dwordx4 %1, %2, %3 offen sc1 lds\n\ts_mov_b64 exec, -1" ::"s"(w < 4 ? base + (bf0 + 2) * 1024 : L_DUMMY), "v"(w < 4 ? vb : OOB), "s"(rws), "s"(t * B_STAGE + (bf0 + 2) * 1024), "s"(w < 4 ? M_ALL : M_ONE) : "memory");
  };
  f32x16 P;
  const float psc = 1.4426950408889634f * rstd;
  {
    issueB(0);
    T[0] = MFMA32(cs_frag(3), bm, T[0]);
    T[1] = MFMA32(cs_frag(4), bm, T[1]);
    const unsigned wpl = L_WPF + ch * 4096 + l * 16;
    const s8 A0 = lds_b128(wpl), A1 = lds_b128(wpl + 1024), A2 = lds_b128(wpl + 2048), A3 = lds_b128(wpl + 3072);
    s8 E0, E1;
    { const int k0 = l31 - 8 * h; const unsigned val = (k0 & 1) ? 0x3f800000u : 0x3f80u; u32x4 u0, u1;
#pragma unroll
      for (int d2 = 0; d2 < 4; ++d2) { u0[d2] = (k0 >> 1) == d2 ? val : 0u; u1[d2] = ((k0 - 16) >> 1) == d2 ? val : 0u; }
      E0 = __builtin_bit_cast(s8, u0); E1 = __builtin_bit_cast(s8, u1); }
    SB(); issueB(1); SB();
    const s8 t01 = pack_step(T[0], 1), t10u = pack_step(T[1], 0), t00 = pack_step(T[0], 0);
    f32x16 z16;
#pragma unroll
    for (int r = 0; r < 16; ++r) z16[r] = 0.f;
    f32x16 Pk = MFMA32(A2, t01, z16); Pk = MFMA32(A3, t10u, Pk);
#pragma unroll
    for (int r = 0; r < 8; ++r) { const f32x2 v = (f32x2){T[1][2 * r], T[1][2 * r + 1]} * (f32x2){rstd, rstd}; T[1][2 * r] = v[0]; T[1][2 * r + 1] = v[1]; }
    const s8 t10 = pack_step(T[1], 0);
    s8 t11 = pack_step(T[1], 1);
    { u32x4 u = __builtin_bit_cast(u32x4, t11); if (h) u[2] = (u[2] & 0xffff0000u) | 0x3f80u; t11 = __builtin_bit_cast(s8, u); }
    f32x16 V = MFMA32(t10, E0, z16); V = MFMA32(t11, E1, V);
    SB(); lda(5); SB();
    unsigned phk[10];
    { f32x2 ep[5], en[5]; phi_map1(Pk, psc, ep, en); SB(); issueB(2); SB(); phi_map2(ep, en, phk); }
    SB(); lda(6); SB();
    const s8 v0 = pack_step(V, 0), v1 = pack_step(V, 1);
    const s8 F0 = __builtin_bit_cast(s8, (u32x4){phk[0], phk[1], phk[2], phk[3]}), F1 = __builtin_bit_cast(s8, (u32x4){phk[4], phk[5], phk[6], phk[7]}), F2 = __builtin_bit_cast(s8, (u32x4){phk[8], phk[9], 0u, 0u});
    f32x16 Z0 = MFMA32(F0, E0, z16); Z0 = MFMA32(F1, E1, Z0);
    f32x16 Z1 = MFMA32(F2, E0, z16);
    f32x16 KV0 = MFMA32(pack_step(Z0, 0), v0, z16); KV0 = MFMA32(pack_step(Z0, 1), v1, KV0);
    f32x16 KV1 = MFMA32(pack_step(Z1, 0), v0, z16);
    const int rho = 16 * (l31 >> 4) + 8 * ((l31 & 7) >> 2) + 4 * ((l31 >> 3) & 1) + (l31 & 3);
    const int d = rho == 28 ? 20 : rho - 8;
    KV1 = MFMA32(pack_step(Z1, 1), v1, KV1);
    P = MFMA32(A0, t00, z16); P = MFMA32(A1, t01, P);
    if (d >= 0 && d <= 20) {
      const unsigned kb = L_KVR + w * 3360 + d * 160;
#pragma unroll
      for (int g = 0; g < 4; ++g) *(LAS f32x4*)(size_t)(kb + (8 * g + 4 * h) * 4) = (f32x4){KV0[4 * g], KV0[4 * g + 1], KV0[4 * g + 2], KV0[4 * g + 3]};
      if (h == 0) { *(LAS f32x4*)(size_t)(kb + 128) = (f32x4){KV1[0], KV1[1], KV1[2], KV1[3]}; *(LAS f32x4*)(size_t)(kb + 144) = (f32x4){KV1[4], KV1[5], KV1[6], KV1[7]}; }
    }
  }
  STAMPL(29);
  LBAR();
  {
    const int c = tid & 255, hc = tid >> 8;
    u32x4 o = {0u, 0u, 0u, 0u};
    if (c < 105) {
      float a[8];
#pragma unroll
      for (int e = 0; e < 8; ++e) a[e] = 0.f;
#pragma unroll
      for (int g = 0; g < 4; ++g) {
        const unsigned ad = L_KVR + (hc * 4 + g) * 3360 + c * 32;
        const f32x4 u0 = *(LAS f32x4*)(size_t)ad, u1 = *(LAS f32x4*)(size_t)(ad + 16);
#pragma unroll
        for (int e = 0; e < 4; ++e) { a[e] += u0[e]; a[4 + e] += u1[e]; }
      }
      o = (u32x4){pk2(a[0], a[1]), pk2(a[2], a[3]), pk2(a[4], a[5]), pk2(a[6], a[7])};
    }
    st_b128_sc1_m(o, rws, c < 105 ? WS_KVP + ((cg * 2 + hc) * 16 + rb) * KVP_STRIDE + c * 16 : OOB, tg == 0 ? M_ALL : (tg == 1 ? 0x1ffffffffffull : M_ONE));
  }
  STAMPL(30);
  const rsrc_t rout = mk_rsrc(out, S * D * 2);
  unsigned phq[10];
  lda(7); issueB(3); SB();
  { f32x2 ep[5], en[5]; phi_map1(P, psc, ep, en); SB(); phi_map2(ep, en, phq); }
#pragma unroll
  for (int r = 0; r < 10; ++r) TIE(phq[r]);
  STAMPL(31);
  WAITV_BAR(5);
  st_b64_sc1_m(tag, rws, w == 0 ? WS_FKV + (cg * 16 + rb) * 8 : OOB, M_ONE);
  lda(8); issueB(4);
  STAMP(5);
  STAMPL(32);
  f32x4 a2[2][5];
#pragma unroll
  for (int a = 0; a < 2; ++a)
#pragma unroll
    for (int c = 0; c < 5; ++c) a2[a][c] = (f32x4){0.f, 0.f, 0.f, 0.f};
  u32x4 kp[8]; u32x2 xr[3]; unsigned go[3]; s8 bfr[2][5];
  const int kc = (64 * tg + l) >> 1; const bool kact = kc < 105;
  static_for<0, 17>([&](auto T_) {
    constexpr int t = decltype(T_)::v;
    if constexpr (t == 4) STAMPF(16);
    if constexpr (t == 7) STAMPF(17);
    if constexpr (t == 11) STAMPF(18);
    if constexpr (t == 12) STAMPF(19);
    constexpr int T_PKV = 2, T_KVL = 3, T_SUM = 5, T_O = 6, T_FO = 9, T_PO = 11, T_AO = 12;
    constexpr int allow_tab[17] = {19, 19, 16, 5, 22, 6, 17, 11, 11, 9, 10, 10, 3, 6, 4, 5, 3};
    STAMPL(33 + t);
    asm volatile("s_waitcnt vmcnt(%0) lgkmcnt(0)" ::"i"(allow_tab[t]) : "memory");
    if constexpr (t == T_KVL) { TIE(pf); if (w == 0) { if (__builtin_expect(!flags_ok(pf, tag, l < 16), 0)) wait_flags(rws, WS_FKV + cg * 128, 8, 16, tag, l); } }
    if constexpr (t == T_AO) { TIE(pf); if (w == 0) { if (__builtin_expect(!flags_ok(pf, tag, l < 16), 0)) wait_flags(rws, WS_FO + (r2 >> 1) * 128, 8, 16, tag, l); } }
    asm volatile("s_barrier" ::: "memory");
    if constexpr (t == T_PKV) pf = POLL16(WS_FKV + cg * 128 + l * 8);
    if constexpr (t == T_KVL) {
#pragma unroll
      for (int r = 0; r < 8; ++r) kp[r] = ld_b128_sc1_m(rws, kact ? WS_KVP + ((cg * 2 + ch) * 16 + 8 * (l & 1)) * KVP_STRIDE + kc * 16 : OOB, r * KVP_STRIDE, tg < 3 ? M_ALL : 0x3ffffull);
    }
    if constexpr (t == T_SUM) {
      float a[8];
#pragma unroll
      for (int r = 0; r < 8; ++r) { TIE(kp[r]);
#pragma unroll
        for (int e = 0; e < 4; ++e) { a[2 * e] = r == 0 ? bf_lo(kp[r][e]) : sadd(a[2 * e], bf_lo(kp[r][e])); a[2 * e + 1] = r == 0 ? bf_hi(kp[r][e]) : sadd(a[2 * e + 1], bf_hi(kp[r][e])); } }
      asm volatile("s_nop 1" : "+v"(a[0]), "+v"(a[1]), "+v"(a[2]), "+v"(a[3]), "+v"(a[4]), "+v"(a[5]), "+v"(a[6]), "+v"(a[7]));
#pragma unroll
      for (int e = 0; e < 8; ++e) a[e] = dpp_add(a[e], 0);
      if (kact && (l & 1) == 0) { const int d = kc / 5, cc = kc % 5; *(LAS u32x4*)(size_t)(L_KVS + ch * 2016 + d * 96 + cc * 16) = (u32x4){pk2(a[0], a[1]), pk2(a[2], a[3]), pk2(a[4], a[5]), pk2(a[6], a[7])}; }
    }
    if constexpr (t == T_O) {
      const int reg = (l31 & 3) + 4 * (l31 >> 3), hh = (l31 >> 2) & 1;
      int d;
      if (reg >= 12) d = 20; else if (ch == 0) d = reg < 8 ? 8 * hh + reg : (hh == 0 ? 8 + reg : 20); else d = reg < 8 ? (hh == 0 ? 4 + reg : 12 + reg) : (hh == 0 ? reg - 8 : 20);
      const unsigned ka = L_KVS + ch * 2016 + d * 96;
      const s8 A0 = lds_b128(ka + h * 16), A1 = lds_b128(ka + 32 + h * 16);
      const u32x2 a2l = *(LAS u32x2*)(size_t)(ka + 64 + h * 8);
      const s8 A2 = __builtin_bit_cast(s8, (u32x4){a2l[0], a2l[1], 0u, 0u});
      f32x16 O;
#pragma unroll
      for (int r = 0; r < 16; ++r) O[r] = 0.f;
      O = MFMA32(A0, __builtin_bit_cast(s8, (u32x4){phq[0], phq[1], phq[2], phq[3]}), O);
      O = MFMA32(A1, __builtin_bit_cast(s8, (u32x4){phq[4], phq[5], phq[6], phq[7]}), O);
      O = MFMA32(A2, __builtin_bit_cast(s8, (u32x4){phq[8], phq[9], 0u, 0u}), O);
      float rc = __builtin_amdgcn_rcpf(O[12] + 1e-6f);
      asm volatile("s_nop 1" : "+v"(rc));
      const unsigned G0 = 192 + 5 * cg + (ch == 0 ? h : 3 + h);
      st_b128_sc1((u32x4){pk2(smul(O[0], rc), smul(O[1], rc)), pk2(smul(O[2], rc), smul(O[3], rc)), pk2(smul(O[4], rc), smul(O[5], rc)), pk2(smul(O[6], rc), smul(O[7], rc))}, rws, actbase + (G0 >> 2) * 1024 + (G0 & 3) * 256);
      const unsigned G2 = 192 + 5 * cg + 2;
      st_b64_sc1_m((u32x2){pk2(smul(O[8], rc), smul(O[9], rc)), pk2(smul(O[10], rc), smul(O[11], rc))}, rws, actbase + (G2 >> 2) * 1024 + (G2 & 3) * 256 + ch * 8, 0xffffffffull);
    }
    if constexpr (t == T_FO) st_b64_sc1_m(tag, rws, w == 0 ? WS_FO + (rb * 16 + cg) * 8 : OOB, M_ONE);
    if constexpr (t == T_PO) pf = POLL16(WS_FO + (r2 >> 1) * 128 + l * 8);
    if constexpr (t == T_AO) { lda(12); lda(13); lda(14); lda(15); lda(16); }
    if constexpr (t == 0) { lda(9); issueB(5); }
    if constexpr (t == 1) lda(10);
    if constexpr (t == 2) lda(11);
    if constexpr (t >= 1 && t + 5 < 17) issueB(t + 5);
    if constexpr (t == 14) {
#pragma unroll
      for (int z = 0; z < 3; ++z) { const int ix = kq + 4 * z; const bool v = ix < 10; const int a = ix >= 5 ? 1 : 0, c = ix - 5 * a;
        go[z] = v ? (unsigned)((64 * r2 + 32 * rh + 16 * a + (l & 15)) * 1280 + (80 * nb + 16 * c + 4 * (l >> 4)) * 2) : OOB; xr[z] = ld_b64_m(rx, go[z], v ? M_ALL : M_ONE); }
    }
    if constexpr (t == 12) WAITV(8);
    TIE(af[t % 10][0]); TIE(af[t % 10][1]);
    if constexpr (t == 0) {
#pragma unroll
      for (int c = 0; c < 5; ++c) bfr[0][c] = lds_b128(L_BRING + kq * 5120 + l * 16 + c * 1024);
    }
    SB();
#pragma unroll
    for (int c = 0; c < 5; ++c) {
      a2[0][c] = MFMA16(bfr[t & 1][c], af[t % 10][0], a2[0][c]);
      a2[1][c] = MFMA16(bfr[t & 1][c], af[t % 10][1], a2[1][c]);
      if constexpr (t + 1 < 17) bfr[(t + 1) & 1][c] = lds_b128(L_BRING + ((t + 1) % 6) * B_STAGE + kq * 5120 + l * 16 + c * 1024);
      SB();
    }
  });
  STAMP(6);
  STAMPL(50);
  LBAR();
  static_for<0, 10>([&](auto I_) { constexpr int ix = decltype(I_)::v;
    if ((ix & 3) != kq) *(LAS f32x4*)(size_t)(L_RED + ((rh * 10 + ix) * 3 + ((kq - ix - 1) & 3)) * 1024 + l * 16) = a2[ix / 5][ix % 5]; });
  asm volatile("s_waitcnt vmcnt(0) lgkmcnt(0)\n\ts_barrier" ::: "memory");
  auto fin = [&](auto I_, int z) { constexpr int ix = decltype(I_)::v;
    const unsigned ad = L_RED + (rh * 10 + ix) * 3 * 1024 + l * 16;
    const f32x4 v = (a2[ix / 5][ix % 5] + *(LAS f32x4*)(size_t)ad) + (*(LAS f32x4*)(size_t)(ad + 1024) + *(LAS f32x4*)(size_t)(ad + 2048));
    st_b64((u32x2){pk2(v[0] + bf_lo(xr[z][0]), v[1] + bf_hi(xr[z][0])), pk2(v[2] + bf_lo(xr[z][1]), v[3] + bf_hi(xr[z][1]))}, rout, go[z]); };
#pragma unroll
  for (int z = 0; z < 3; ++z) TIE(xr[z]);
  if (kq == 0) { fin(IC<0>{}, 0); fin(IC<4>{}, 1); fin(IC<8>{}, 2); }
  else if (kq == 1) { fin(IC<1>{}, 0); fin(IC<5>{}, 1); fin(IC<9>{}, 2); }
  else if (kq == 2) { fin(IC<2>{}, 0); fin(IC<6>{}, 1); }
  else { fin(IC<3>{}, 0); fin(IC<7>{}, 1); }
  asm volatile(".Lfk_end:");
  if (b == 0 && tid == 0) st_b64_sc1(tag, rws, WS_EPOCH);
  STAMPL(51);
  STAMP(7);
}

extern "C" void kernel_launch(void* const* d_in, const int* in_sizes, int n_in, void* d_out, int out_size, void* d_ws, size_t ws_size, hipStream_t stream) {
  (void)in_sizes; (void)n_in; (void)out_size; (void)ws_size;
  hipFuncSetAttribute((const void*)fused_k, hipFuncAttributeMaxDynamicSharedMemorySize, LDS_BYTES);
  fused_k<<<256, 512, LDS_BYTES, stream>>>((const u16*)d_in[0], (const u16*)d_in[1], (const u16*)d_in[2], (const u16*)d_in[3], (const u16*)d_in[5], (const u16*)d_in[6], (const u16*)d_in[7], (u16*)d_out, (char*)d_ws);
}
```

```cpp
#include <hip/hip_runtime.h>
#include <hip/hip_bf16.h>
typedef unsigned short u16;
typedef unsigned long long u64;
typedef short s4 __attribute__((ext_vector_type(4)));
typedef short s8 __attribute__((ext_vector_type(8)));
typedef __bf16 b8 __attribute__((ext_vector_type(8)));
typedef __bf16 b2 __attribute__((ext_vector_type(2)));
typedef float f32x16 __attribute__((ext_vector_type(16)));
typedef float f32x4 __attribute__((ext_vector_type(4)));
typedef float f32x2 __attribute__((ext_vector_type(2)));
typedef unsigned u32x4 __attribute__((ext_vector_type(4)));
typedef unsigned u32x2 __attribute__((ext_vector_type(2)));
#define LAS __attribute__((address_space(3)))
#define DI __device__ __forceinline__
typedef __amdgpu_buffer_rsrc_t rsrc_t;

constexpr int S = 2048, D = 640, F = 1536;
constexpr unsigned WS_EPOCH = 0;
constexpr unsigned WS_FP0 = 1024, WS_FP1 = 3072, WS_FH = 5120, WS_FO = 7168, WS_FKV = 9216;
constexpr unsigned WS_STATS = 16384;
constexpr unsigned WS_KVP = 32768;
constexpr unsigned KVP_STRIDE = 1792;
constexpr unsigned WS_WQP = 1u << 20;
constexpr unsigned WS_W2P = 4u << 20;
constexpr unsigned WS_CSP = 7u << 20;
constexpr unsigned WS_ACT = 8u << 20;
constexpr unsigned WS_USED = 17u << 20;
constexpr unsigned OOB = 0x80000000u;
constexpr unsigned L_X = 0;
constexpr unsigned L_W = 49152;
constexpr unsigned W_STAGE = 24832, W_UOFF = 12320;
constexpr unsigned L_MISC = L_W + 4 * W_STAGE;
constexpr unsigned L_W2S = L_MISC;
constexpr unsigned L_CS = L_W2S;
constexpr unsigned L_WPF = L_W2S + 7168;
constexpr unsigned L_QRAW = L_W + 3 * W_STAGE;
constexpr unsigned L_WPR = L_QRAW + 12288;
constexpr unsigned L_W2A = L_QRAW + 14336;
constexpr unsigned L_BRING = 0;
constexpr unsigned B_STAGE = 20480;
constexpr unsigned L_KVR = 122880;
constexpr unsigned L_KVS = 149760;
constexpr unsigned L_DUMMY1 = L_QRAW + 23552;
constexpr unsigned L_DUMMY = L_W2S + 6144;
constexpr unsigned L_RED = 0;
constexpr unsigned LDS_BYTES = 160 * 1024;

DI rsrc_t mk_rsrc(const void* p, unsigned bytes) { return __builtin_amdgcn_make_buffer_rsrc((void*)p, 0, bytes, 0x00020000); }
DI void dma16(unsigned lds_addr, unsigned voff, rsrc_t r, unsigned soff) {
  asm volatile("s_mov_b32 m0, %0\n\ts_nop 0\n\tbuffer_load_dwordx4 %1, %2, %3 offen lds" ::"s"(lds_addr), "v"(voff), "s"(r), "s"(soff) : "memory");
}
DI void dma16_sc1(unsigned lds_addr, unsigned voff, rsrc_t r, unsigned soff) {
  asm volatile("s_mov_b32 m0, %0\n\ts_nop 0\n\tbuffer_load_dwordx4 %1, %2, %3 offen sc1 lds" ::"s"(lds_addr), "v"(voff), "s"(r), "s"(soff) : "memory");
}
DI u32x2 ld_b64_sc1(rsrc_t r, unsigned voff, unsigned soff) { u32x2 v; asm volatile("buffer_load_dwordx2 %0, %1, %2, %3 offen sc1" : "=v"(v) : "v"(voff), "s"(r), "s"(soff) : "memory"); return v; }
DI u32x4 ld_b128_sc1(rsrc_t r, unsigned voff, unsigned soff) { u32x4 v; asm volatile("buffer_load_dwordx4 %0, %1, %2, %3 offen sc1" : "=v"(v) : "v"(voff), "s"(r), "s"(soff) : "memory"); return v; }
DI u32x2 ld_b64_m(rsrc_t r, unsigned voff, u64 mask) { u32x2 v; asm volatile("s_mov_b64 exec, %3\n\tbuffer_load_dwordx2 %0, %1, %2, 0 offen\n\ts_mov_b64 exec, -1" : "=v"(v) : "v"(voff), "s"(r), "s"(mask) : "memory"); return v; }
DI void st_b64(u32x2 v, rsrc_t r, unsigned voff) { asm volatile("buffer_store_dwordx2 %0, %1, %2, 0 offen" ::"v"(v), "v"(voff), "s"(r) : "memory"); }
DI u32x4 ld_b128(rsrc_t r, unsigned voff, unsigned soff) { u32x4 v; asm volatile("buffer_load_dwordx4 %0, %1, %2, %3 offen" : "=v"(v) : "v"(voff), "s"(r), "s"(soff) : "memory"); return v; }
DI void st_b128_sc1(u32x4 v, rsrc_t r, unsigned voff) {
 asm volatile("buffer_store_dwordx4 %0, %1, %2, 0 offen sc1\n\ts_nop 1" ::"v"(v), "v"(voff), "s"(r) : "memory"); }
DI void st_b64_sc1(u32x2 v, rsrc_t r, unsigned voff) { asm volatile("buffer_store_dwordx2 %0, %1, %2, 0 offen sc1" ::"v"(v), "v"(voff), "s"(r) : "memory"); }
DI void st_b64_sc1_m(u32x2 v, rsrc_t r, unsigned voff, u64 mask) {
 asm volatile("s_mov_b64 exec, %3\n\tbuffer_store_dwordx2 %0, %1, %2, 0 offen sc1\n\ts_mov_b64 exec, -1" ::"v"(v), "v"(voff), "s"(r), "s"(mask) : "memory"); }
DI void st_b32_sc1_m(unsigned v, rsrc_t r, unsigned voff, u64 mask) { asm volatile("s_mov_b64 exec, %3\n\tbuffer_store_dword %0, %1, %2, 0 offen sc1\n\ts_mov_b64 exec, -1" ::"v"(v), "v"(voff), "s"(r), "s"(mask) : "memory"); }
DI u32x2 ld_b64_sc1_m(rsrc_t r, unsigned voff, u64 mask) { u32x2 v; asm volatile("s_mov_b64 exec, %3\n\tbuffer_load_dwordx2 %0, %1, %2, 0 offen sc1\n\ts_mov_b64 exec, -1" : "=v"(v) : "v"(voff), "s"(r), "s"(mask) : "memory"); return v; }
DI unsigned ld_b32_sc1_m(rsrc_t r, unsigned voff, unsigned soff, u64 mask) { unsigned v; asm volatile("s_mov_b64 exec, %4\n\tbuffer_load_dword %0, %1, %2, %3 offen sc1\n\ts_mov_b64 exec, -1" : "=v"(v) : "v"(voff), "s"(r), "s"(soff), "s"(mask) : "memory"); return v; }
DI u32x4 ld_b128_sc1_m(rsrc_t r, unsigned voff, unsigned soff, u64 mask) { u32x4 v; asm volatile("s_mov_b64 exec, %4\n\tbuffer_load_dwordx4 %0, %1, %2, %3 offen sc1\n\ts_mov_b64 exec, -1" : "=v"(v) : "v"(voff), "s"(r), "s"(soff), "s"(mask) : "memory"); return v; }
DI void st_b128_sc1_m(u32x4 v, rsrc_t r, unsigned voff, u64 mask) {
 asm volatile("s_mov_b64 exec, %3\n\tbuffer_store_dwordx4 %0, %1, %2, 0 offen sc1\n\ts_mov_b64 exec, -1\n\ts_nop 0" ::"v"(v), "v"(voff), "s"(r), "s"(mask) : "memory"); }
DI void dma16_m(unsigned lds_addr, unsigned voff, rsrc_t r, unsigned soff, u64 mask) {
  asm volatile("s_mov_b32 m0, %0\n\ts_mov_b64 exec, %4\n\tbuffer_load_dwordx4 %1, %2, %3 offen lds\n\ts_mov_b64 exec, -1" ::"s"(lds_addr), "v"(voff), "s"(r), "s"(soff), "s"(mask) : "memory");
}
#define POLL16(off) ld_b64_sc1_m(rws, w == 0 ? (off) : OOB, w == 0 ? 0xffffull : M_ONE)
#define TIE(x) asm volatile("" : "+v"(x))
#define WAITV(n) asm volatile("s_waitcnt vmcnt(" #n ")" ::: "memory")
#define WAITV_BAR(n) asm volatile("s_waitcnt vmcnt(" #n ")\n\ts_barrier" ::: "memory")
#define WAITVL_BAR(n) asm volatile("s_waitcnt vmcnt(" #n ") lgkmcnt(0)\n\ts_barrier" ::: "memory")
#define LBAR() asm volatile("s_waitcnt lgkmcnt(0)\n\ts_barrier" ::: "memory")
#define SB() __builtin_amdgcn_sched_barrier(0)
#define MFMA32(a, b, c) __builtin_amdgcn_mfma_f32_32x32x16_bf16(__builtin_bit_cast(b8, (a)), __builtin_bit_cast(b8, (b)), (c), 0, 0, 0)
#define MFMA16(a, b, c) __builtin_amdgcn_mfma_f32_16x16x32_bf16(__builtin_bit_cast(b8, (a)), __builtin_bit_cast(b8, (b)), (c), 0, 0, 0)
DI s8 lds_b128(unsigned addr) { return *(LAS s8*)(size_t)addr; }
DI s8 tr_frag(unsigned addr, unsigned off2) {
  s4 a = __builtin_amdgcn_ds_read_tr16_b64_v4i16((LAS s4*)(size_t)addr);
  s4 b = __builtin_amdgcn_ds_read_tr16_b64_v4i16((LAS s4*)(size_t)(addr + off2));
  return __builtin_shufflevector(a, b, 0, 1, 2, 3, 4, 5, 6, 7);
}
DI float dot_ones(s8 f, float acc) {
  u32x4 u = __builtin_bit_cast(u32x4, f);
#pragma unroll
  for (int i = 0; i < 4; ++i) { const unsigned t = u[i]; acc = __builtin_amdgcn_fdot2_f32_bf16(__builtin_bit_cast(b2, t), __builtin_bit_cast(b2, 0x3f803f80u), acc, false); }
  asm volatile("" : "+v"(acc));
  return acc;
}
DI unsigned pk2(float lo, float hi) { b2 c; c[0] = (__bf16)lo; c[1] = (__bf16)hi; return __builtin_bit_cast(unsigned, c); }
DI float bf_lo(unsigned u) { return __uint_as_float(u << 16); }
DI float bf_hi(unsigned u) { return __uint_as_float(u & 0xffff0000u); }
DI float swap_add(float v) { unsigned u = __float_as_uint(v); auto r = __builtin_amdgcn_permlane32_swap(u, u, false, false); return __uint_as_float(r[0]) + __uint_as_float(r[1]); }
DI float sadd(float a, float b) { float r; asm("v_add_f32 %0, %1, %2" : "=v"(r) : "v"(a), "v"(b)); return r; }
DI float smul(float a, float b) { float r; asm("v_mul_f32 %0, %1, %2" : "=v"(r) : "v"(a), "v"(b)); return r; }
DI float dpp_add(float v, const int ctrl_sel) {
  const int u = __float_as_int(v); int r;
  if (ctrl_sel == 0) r = __builtin_amdgcn_update_dpp(0, u, 0xB1, 0xf, 0xf, true);
  else if (ctrl_sel == 1) r = __builtin_amdgcn_update_dpp(0, u, 0x4E, 0xf, 0xf, true);
  else if (ctrl_sel == 2) r = __builtin_amdgcn_update_dpp(0, u, 0x141, 0xf, 0xf, true);
  else r = __builtin_amdgcn_update_dpp(0, u, 0x140, 0xf, 0xf, true);
  return v + __int_as_float(r);
}
DI float wave_sum(float v) {
  v = dpp_add(v, 0); v = dpp_add(v, 1); v = dpp_add(v, 2); v = dpp_add(v, 3);
  { unsigned u = __float_as_uint(v); auto r = __builtin_amdgcn_permlane16_swap(u, u, false, false); v = __uint_as_float(r[0]) + __uint_as_float(r[1]); }
  return swap_add(v);
}
DI s8 pack_step(const f32x16& x, int s) {
  return __builtin_bit_cast(s8, (u32x4){pk2(x[8 * s], x[8 * s + 1]), pk2(x[8 * s + 2], x[8 * s + 3]), pk2(x[8 * s + 4], x[8 * s + 5]), pk2(x[8 * s + 6], x[8 * s + 7])});
}
template <int I> struct IC { static constexpr int v = I; };
template <int B, int E, class Fn> DI void static_for(Fn&& f) { if constexpr (B < E) { f(IC<B>{}); static_for<B + 1, E>(f); } }
DI bool flags_ok(u32x2 v, u32x2 tag, bool act) { return __all(!act || (v[0] == tag[0] && v[1] == tag[1])); }
DI void wait_flags(rsrc_t rws, unsigned base, unsigned stride, int n, u32x2 tag, int l, int skip = -1) {

  const bool act = l < n && l != skip; const unsigned off = act ? base + l * stride : OOB;
#pragma nounroll
  for (int it = 0; it < (1 << 17); ++it) {
    u32x2 v; asm volatile("buffer_load_dwordx2 %0, %1, %2, 0 offen sc1\n\ts_waitcnt vmcnt(0)" : "=&v"(v) : "v"(off), "s"(rws) : "memory");
    if (flags_ok(v, tag, act)) return;
    __builtin_amdgcn_s_sleep(1);
  }
}
DI void wait_flags256(rsrc_t rws, unsigned base, u32x2 tag, int l) {
#pragma nounroll
  for (int it = 0; it < (1 << 17); ++it) {
    u32x4 a, b;
    asm volatile("buffer_load_dwordx4 %0, %2, %3, 0 offen sc1\n\tbuffer_load_dwordx4 %1, %2, %3, 0 offen offset:16 sc1\n\ts_waitcnt vmcnt(0)" : "=&v"(a), "=&v"(b) : "v"(base + l * 32), "s"(rws) : "memory");
    const bool ok = a[0] == tag[0] && a[1] == tag[1] && a[2] == tag[0] && a[3] == tag[1] && b[0] == tag[0] && b[1] == tag[1] && b[2] == tag[0] && b[3] == tag[1];
    if (__all(ok)) return;
    __builtin_amdgcn_s_sleep(1);
  }
}
DI bool flags256_ok(u32x4 a, u32x4 b, u32x2 tag) {
  return __all(a[0] == tag[0] && a[1] == tag[1] && a[2] == tag[0] && a[3] == tag[1] && b[0] == tag[0] && b[1] == tag[1] && b[2] == tag[0] && b[3] == tag[1]);
}
DI float max3(float a, float b, float c) { float r; asm("v_max3_f32 %0, %1, %2, %3" : "=v"(r) : "v"(a), "v"(b), "v"(c)); return r; }
DI float min3(float a, float b, float c) { float r; asm("v_min3_f32 %0, %1, %2, %3" : "=v"(r) : "v"(a), "v"(b), "v"(c)); return r; }
DI f32x2 exp2_2(f32x2 a) { f32x2 r; r[0] = __builtin_amdgcn_exp2f(a[0]); r[1] = __builtin_amdgcn_exp2f(a[1]); return r; }
DI unsigned pk2v(f32x2 a) { return pk2(a[0], a[1]); }
DI void phi_map1(const f32x16& pin, float sc, f32x2 (&ep)[5], f32x2 (&en)[5]) {
  float p[10];
#pragma unroll
  for (int r = 0; r < 10; ++r) p[r] = pin[r];
  asm volatile("s_nop 7\n\ts_nop 3" : "+v"(p[0]), "+v"(p[1]), "+v"(p[2]), "+v"(p[3]), "+v"(p[4]), "+v"(p[5]), "+v"(p[6]), "+v"(p[7]), "+v"(p[8]), "+v"(p[9]));
  float mx = max3(p[0], p[1], p[2]), mn = min3(p[0], p[1], p[2]);
#pragma unroll
  for (int r = 3; r < 9; r += 2) { mx = max3(mx, p[r], p[r + 1]); mn = min3(mn, p[r], p[r + 1]); }
  mx = max3(mx, p[9], p[9]); mn = min3(mn, p[9], p[9]);
  { unsigned u = __float_as_uint(mx); auto r = __builtin_amdgcn_permlane32_swap(u, u, false, false); mx = max3(__uint_as_float(r[0]), __uint_as_float(r[1]), __uint_as_float(r[1])); }
  { unsigned u = __float_as_uint(mn); auto r = __builtin_amdgcn_permlane32_swap(u, u, false, false); mn = min3(__uint_as_float(r[0]), __uint_as_float(r[1]), __uint_as_float(r[1])); }
  const f32x2 s2 = {sc, sc}, bp = {-mx * sc, -mx * sc}, bn = {mn * sc, mn * sc};
#pragma unroll
  for (int r = 0; r < 5; ++r) { const f32x2 v = {p[2 * r], p[2 * r + 1]}; ep[r] = exp2_2(__builtin_elementwise_fma(v, s2, bp)); en[r] = exp2_2(__builtin_elementwise_fma(v, -s2, bn)); }
}
DI void phi_map2(const f32x2 (&ep)[5], const f32x2 (&en)[5], unsigned (&o)[10]) {
  const f32x2 sp2 = (ep[0] + ep[1]) + (ep[2] + ep[3]) + ep[4], sn2 = (en[0] + en[1]) + (en[2] + en[3]) + en[4];
  const float rp = __builtin_amdgcn_rcpf(swap_add(sp2[0] + sp2[1])), rn = __builtin_amdgcn_rcpf(swap_add(sn2[0] + sn2[1]));
  const f32x2 rp2 = {rp, rp}, rn2 = {rn, rn};
#pragma unroll
  for (int r = 0; r < 5; ++r) { o[r] = pk2v(ep[r] * rp2); o[5 + r] = pk2v(en[r] * rn2); }
}


__global__ __launch_bounds__(512, 2) void fused_k(const u16* __restrict__ x, const u16* __restrict__ wqkv, const u16* __restrict__ wphi, const u16* __restrict__ wo,
                                                   const u16* __restrict__ wg, const u16* __restrict__ wu, const u16* __restrict__ wd, u16* __restrict__ out, char* __restrict__ ws) {
  extern __shared__ __attribute__((aligned(256))) char smem[];
  const int tid = threadIdx.x; const int l = tid & 63; const int w = __builtin_amdgcn_readfirstlane(tid >> 6);
  const int tg = w & 3, ch = w >> 2; const int l31 = l & 31, h = l >> 5;
  const int b = blockIdx.x, xcd = b & 7, idx = b >> 3;
  const int rb = (xcd & 1) * 8 + (idx & 7), cg = (xcd >> 1) * 4 + (idx >> 3);
  const int r2 = (xcd & 3) * 8 + (idx & 7), nb = (xcd >> 2) * 4 + (idx >> 3);
  const int klo = (5 * rb) >> 1, kcnt = ((5 * (rb + 1)) >> 1) - klo, rot = klo >> 2, kloc = klo - 4 * rot;
  const rsrc_t rx = mk_rsrc(x, S * D * 2);
  const rsrc_t rw = mk_rsrc(w < 4 ? wg : wu, D * F * 2);
  const rsrc_t rws = mk_rsrc(ws, WS_USED);
  const u64 M_ALL = ~0ull, M_ONE = 1ull;
  auto sig = [&](int s) -> unsigned { const int t = s + rot; return t >= 10 ? t - 10 : t; };
  unsigned vx[2], vw[3];
#pragma unroll
  for (int i = 0; i < 2; ++i) { const int r = 8 * (2 * w + i) + (l >> 3); const int c = (l & 7) ^ ((r >> 1) & 7); vx[i] = (128 * rb + r) * 1280 + c * 16; }
#pragma unroll
  for (int i = 0; i < 3; ++i) { const int p = 64 * (3 * (w & 3) + i) + l; const int k = p / 12, c = p % 12; vw[i] = k * 3072 + cg * 192 + c * 16; }
  const unsigned lx0 = L_X + 2 * w * 1024;
  const unsigned lw0 = L_W + (w < 4 ? 0 : W_UOFF) + 3 * (w & 3) * 1024;
  auto dmax = [&](int s, int i) { dma16(lx0 + (s % 3) * 16384 + i * 1024, vx[i], rx, sig(s) * 128); };
  auto dmaw = [&](int s, int i) { dma16(lw0 + (s & 3) * W_STAGE + i * 1024, vw[i], rw, sig(s) * (64 * 3072)); };
  auto issue1 = [&](int s) { dmax(s, 0); dmax(s, 1); dmaw(s, 0); dmaw(s, 1); dmaw(s, 2); };
  issue1(0);
  u32x2 ep = ld_b64_sc1_m(rws, WS_EPOCH, M_ONE);
  const int srow = 128 * rb + 8 * cg + w;
  const bool v3 = kcnt == 3 && w < 4;
  u32x4 sx0, sx1;
  {
    const rsrc_t rq = mk_rsrc(wqkv, D * 1920 * 2);
    const int p = 64 * (w & 3) + l; const int k = p / 15, c = p % 15;
    const unsigned vo = p < 240 ? (unsigned)(k * 3840 + (c / 5) * 1280 + cg * 80 + (c % 5) * 16) : OOB;
    dma16(L_QRAW + w * 1024, vo, rq, (16 * (klo + (w >> 2))) * 3840);
    dma16_m(v3 ? L_QRAW + 8192 + w * 1024 : L_DUMMY1, v3 ? vo : OOB, rq, v3 ? (16 * (klo + 2)) * 3840 : 0, v3 ? M_ALL : M_ONE);
    const rsrc_t rp = mk_rsrc(wphi, 32 * 400 * 2);
    dma16_m(w < 2 ? L_WPR + w * 1024 : L_DUMMY1, (w < 2 && w * 64 + l < 100) ? (unsigned)(cg * 1600 + (w * 64 + l) * 16) : OOB, rp, 0, w == 0 ? M_ALL : (w == 1 ? 0xfffffffffull : M_ONE));
    sx0 = ld_b128(rx, srow * 1280 + l * 16, 0);
    sx1 = ld_b128(rx, l < 16 ? (unsigned)(srow * 1280 + 1024 + l * 16) : OOB, 0);
    const int kst = b / 5, cb = b % 5; const bool isd = kst < 48;
    const rsrc_t rr = mk_rsrc(isd ? wd : wo, isd ? F * D * 2 : D * D * 2);
    dma16(L_W2A + w * 1024, (unsigned)((l >> 4) * 1280 + (l & 15) * 16), rr, ((isd ? 32 * kst : 32 * (kst - 48)) + 4 * w) * 1280 + cb * 256);
  }
  const int gi = l >> 4, q4 = (l >> 2) & 3, p4 = l & 3;
  issue1(1); issue1(2);
  {
    u64 pc; unsigned len;
    asm volatile("s_getpc_b64 %0\n.Lfk_here:\n\ts_mov_b32 %1, .Lfk_end-.Lfk_here" : "=s"(pc), "=s"(len));
    const rsrc_t rc = mk_rsrc((const void*)pc, len);
    const unsigned off = (idx * 8 + w) * 256;
    asm volatile("s_mov_b32 m0, %0\n\ts_mov_b64 exec, 0xffff\n\tbuffer_load_dwordx4 %1, %2, 0 offen lds\n\ts_mov_b64 exec, -1" ::"s"(L_DUMMY1), "v"(off + l * 16), "s"(rc) : "memory");
  }
  WAITV_BAR(11);
  TIE(ep);
  u32x2 tag;
  { const unsigned lo = __builtin_amdgcn_readfirstlane(ep[0]), hi = __builtin_amdgcn_readfirstlane(ep[1]); tag[0] = lo + 1u; tag[1] = hi + (lo == 0xffffffffu ? 1u : 0u); }
  const int shc = (w >> 1) & 1, sht = w & 1;
  s8 sf0, sf1, sf2; unsigned se[8]; unsigned smA, smB;
  {
    const int Q = 4 * (gi & 1) + p4;
    int seg, qd;
    if (sht == 0) { seg = Q < 5 ? 0 : 1; qd = Q < 5 ? Q : Q - 5; } else { seg = Q < 2 ? 1 : 2; qd = Q < 2 ? 3 + Q : (Q < 7 ? Q - 2 : 4); }
    const unsigned lo = L_QRAW + (8 * h + q4) * 240 + seg * 80 + shc * 40 + qd * 8;
    sf0 = tr_frag(lo + (w >> 2) * 4096, 960); sf1 = tr_frag(lo + 8192, 960);
    sf2 = tr_frag(L_W2A + (8 * gi + q4) * 256 + w * 32 + p4 * 8, 1024);
    const int reg = (l31 & 3) + 4 * (l31 >> 3), hh = (l31 >> 2) & 1;
    const int d0 = (tg == 0 ? 0 : tg == 1 ? 16 : tg == 2 ? -4 : 12) + 4 * h;
    const unsigned wa = L_WPR + ch * 800 + (10 * hh + reg) * 2 + d0 * 40;
#pragma unroll
    for (int j = 0; j < 8; ++j) se[j] = *(LAS u16*)(size_t)(wa + (8 * (j >> 2) + (j & 3)) * 40);
    smA = (reg < 10 && (unsigned)d0 < 20u) ? ~0u : 0u; smB = (reg < 10 && (unsigned)(d0 + 8) < 20u) ? ~0u : 0u;
  }
  auto side = [&](int part) {
    if (part == 0) {
      TIE(sx0); TIE(sx1);
      float sm = 0.f, sq = 0.f;
#pragma unroll
      for (int i = 0; i < 4; ++i) { const unsigned u0 = sx0[i], u1 = sx1[i];
        sm = __builtin_amdgcn_fdot2_f32_bf16(__builtin_bit_cast(b2, u0), __builtin_bit_cast(b2, 0x3f803f80u), sm, false); sq = __builtin_amdgcn_fdot2_f32_bf16(__builtin_bit_cast(b2, u0), __builtin_bit_cast(b2, u0), sq, false);
        sm = __builtin_amdgcn_fdot2_f32_bf16(__builtin_bit_cast(b2, u1), __builtin_bit_cast(b2, 0x3f803f80u), sm, false); sq = __builtin_amdgcn_fdot2_f32_bf16(__builtin_bit_cast(b2, u1), __builtin_bit_cast(b2, u1), sq, false); }
      const float m = wave_sum(sm) * (1.f / D);
      const float var = fmaxf(wave_sum(sq) * (1.f / D) - m * m, 0.f);
      st_b64_sc1_m((u32x2){__float_as_uint(m), __float_as_uint(__builtin_amdgcn_rsqf(var + 1e-5f))}, rws, WS_STATS + srow * 8, M_ONE);
    } else if (part == 1) {
      st_b128_sc1(__builtin_bit_cast(u32x4, sf0), rws, WS_WQP + (((2 * cg + shc) * 40 + klo + (w >> 2)) * 2 + sht) * 1024 + l * 16);
      st_b128_sc1_m(__builtin_bit_cast(u32x4, sf1), rws, v3 ? WS_WQP + (((2 * cg + shc) * 40 + klo + 2) * 2 + sht) * 1024 + l * 16 : OOB, v3 ? M_ALL : M_ONE);
      const int kst = b / 5, cb = b % 5, ct = 8 * cb + w;
      st_b128_sc1(__builtin_bit_cast(u32x4, sf2), rws, WS_W2P + (((ct / 5) * 68 + kst) * 5 + ct % 5) * 1024 + l * 16);
      const float c0 = swap_add(dot_ones(sf0, 0.f)), c1 = swap_add(dot_ones(sf1, 0.f));
      if (h == 0) { const unsigned qa = L_W2S + ((shc * 2 + sht) * 32 + l31) * 16 + (w >> 2) * 4; *(LAS float*)(size_t)qa = c0; *(LAS float*)(size_t)(qa + 8) = v3 ? c1 : 0.f; }
    } else {
      *(LAS u32x4*)(size_t)(L_WPF + w * 1024 + l * 16) = (u32x4){(se[0] | (se[1] << 16)) & smA, (se[2] | (se[3] << 16)) & smA, (se[4] | (se[5] << 16)) & smB, (se[6] | (se[7] << 16)) & smB};
    }
  };
  const unsigned qoff = (p4 == 1) ? 8 : (p4 == 2) ? 4 : (p4 == 3) ? 12 : 0;
  const unsigned wl = L_W + ((gi & 1) ? W_UOFF : 0) + (8 * h + q4) * 192 + 2 * qoff + 96 * ch;
  const unsigned wl2 = wl + 2 * W_STAGE;
  const int xrow = 32 * tg + l31;
  unsigned xa[4];
#pragma unroll
  for (int j = 0; j < 4; ++j) { xa[j] = (L_X + xrow * 128 + ((h ^ ((xrow >> 1) & 7)) << 4)) ^ (j * 32); asm volatile("" : "+v"(xa[j])); }
  s8 XF[40];
  f32x16 acc[3]; float cs[3];
#pragma unroll
  for (int i = 0; i < 3; ++i) { cs[i] = 0.f;
#pragma unroll
    for (int r = 0; r < 16; ++r) acc[i][r] = 0.f; }
  s8 wf[2][3];
  auto rdx = [&](int s, int j) { XF[4 * s + j] = lds_b128(xa[j] + (s % 3) * 16384); };
  auto rdw = [&](int n, int i) { const int s = n >> 2, j = n & 3; wf[n & 1][i] = tr_frag(((s & 2) ? wl2 : wl) + (s & 1) * W_STAGE + j * 3072 + i * 32, 768); };
  const unsigned vq2 = WS_WQP + ((2 * cg + ch) * 40 + tg) * 2048 + l * 16;
  auto slot2 = [&](int q) -> unsigned { return q == 0 || q == 7 ? 16384u : q == 1 || q == 8 ? L_W + 2 * W_STAGE : q == 2 || q == 9 ? 32768u : q == 3 ? L_W + 3 * W_STAGE : q == 4 ? 0u : q == 5 ? L_W : L_W + W_STAGE; };
  auto issue2 = [&](int q) { const unsigned so = sig(q) * 8192; dma16_sc1(slot2(q) + w * 2048, vq2, rws, so); dma16_sc1(slot2(q) + w * 2048 + 1024, vq2, rws, so + 1024); };
  const bool hv = b < 168 && w < 4; const int hu = 256 + (b >> 1), hkst = hu / 5, hcb = hu % 5;
  u32x2 pf, pf2, st; u32x4 pl0, pl1, cg4[4]; unsigned cg1[4]; f32x4 qs0, qs1;
  rdx(0, 0); rdw(0, 0); rdw(0, 1); rdw(0, 2); rdx(0, 1); rdx(0, 2); rdx(0, 3);
  static_for<0, 40>([&](auto N_) {
    constexpr int n = decltype(N_)::v;
    constexpr int s = n >> 2, j = n & 3;
    if constexpr (n == 3) WAITV_BAR(10);
    if constexpr (n == 7) { WAITV_BAR(9); qs0 = *(LAS f32x4*)(size_t)(L_W2S + ((ch * 2) * 32 + l31) * 16); qs1 = *(LAS f32x4*)(size_t)(L_W2S + ((ch * 2 + 1) * 32 + l31) * 16); }
    if constexpr (n == 11) WAITV_BAR(7);
    if constexpr (n == 15) { WAITV_BAR(5); st_b64_sc1_m(tag, rws, w == 0 ? WS_FP0 + (cg * 16 + rb) * 8 : OOB, M_ONE); }
    if constexpr (n == 19) WAITV_BAR(7);
    if constexpr (n == 23) WAITV_BAR(5);
    if constexpr (n == 27) { WAITV_BAR(6); pf = POLL16(WS_FP0 + cg * 128 + l * 8); pf2 = POLL16(WS_FP0 + rb * 8 + (l & 15) * 128); }
    if constexpr (n == 31) {
      WAITV(5); TIE(pf); TIE(pf2);
      if (w == 0) {
        if (__builtin_expect(!flags_ok(pf, tag, l < 16), 0)) wait_flags(rws, WS_FP0 + cg * 128, 8, 16, tag, l);
        if (__builtin_expect(!flags_ok(pf2, tag, l < 16), 0)) wait_flags(rws, WS_FP0 + rb * 8, 128, 16, tag, l);
      }
      asm volatile("s_barrier" ::: "memory");
      st_b64_sc1_m(tag, rws, w == 0 ? WS_FP1 + b * 8 : OOB, M_ONE);
      st = ld_b64_sc1(rws, WS_STATS + (128 * rb + xrow) * 8, 0);
      {
        const unsigned vo = w < 4 ? WS_CSP + (cg * 16 + 4 * w) * 1280 + l * 16 : OOB, vo1 = w < 4 ? WS_CSP + (cg * 16 + 4 * w) * 1280 + 1024 + l * 4 : OOB;
#pragma unroll
        for (int r = 0; r < 4; ++r) { cg4[r] = ld_b128_sc1_m(rws, vo, r * 1280, w < 4 ? M_ALL : M_ONE); cg1[r] = ld_b32_sc1_m(rws, vo1, r * 1280, w < 4 ? M_ALL : M_ONE); } }
      issue2(0); issue2(1);
    }
    if constexpr (n == 35) { WAITV_BAR(14); issue2(2); issue2(3); }
    SB();
#pragma unroll
    for (int i = 0; i < 3; ++i) {
      acc[i] = MFMA32(wf[n & 1][i], XF[n], acc[i]);
      SB();
      if (i == 0 && j == 3 && s < 9) rdx(s + 1, 0);
      if (n + 1 < 40) rdw(n + 1, i);
      if (j == 0 && s > 0) rdx(s, i + 1);
      if (n < 3 && i == 2) side(n);
      if (n < 8 && i == 2) { if ((unsigned)(n - kloc) < (unsigned)kcnt && tg == 0) { cs[0] = dot_ones(wf[n & 1][0], cs[0]); cs[1] = dot_ones(wf[n & 1][1], cs[1]); cs[2] = dot_ones(wf[n & 1][2], cs[2]); } }
      if (n == 8 && i == 0) {
        const unsigned dst = tg == 0 ? WS_CSP + (cg * 16 + rb) * 1280 + (ch * 32 + l31) * 16 : OOB; const u64 mk = tg == 0 ? 0xffffffffull : M_ONE;
        st_b128_sc1_m((u32x4){__float_as_uint(swap_add(cs[0])), __float_as_uint(swap_add(cs[1])), __float_as_uint(swap_add(cs[2])), __float_as_uint((qs0[0] + qs0[1]) + (qs0[2] + qs0[3]))}, rws, dst, mk);
        st_b32_sc1_m(__float_as_uint((qs1[0] + qs1[1]) + (qs1[2] + qs1[3])), rws, tg == 0 ? WS_CSP + (cg * 16 + rb) * 1280 + 1024 + (ch * 32 + l31) * 4 : OOB, mk);
      }
      if (j == 3 && s + 3 < 10) {
        if (i == 0) { dmax(s + 3, 0); dmax(s + 3, 1); }
        if (i == 1) { dmaw(s + 3, 0); dmaw(s + 3, 1); }
        if (i == 2) dmaw(s + 3, 2);
      }
      if constexpr (n == 15) { if (i == 2) {
        const rsrc_t rr = mk_rsrc(wo, D * D * 2);
        dma16_m(hv ? L_W2S + w * 1024 : L_DUMMY, hv ? (unsigned)((l >> 3) * 1280 + (l & 7) * 16) : OOB, rr, hv ? (32 * (hkst - 48) + 8 * w) * 1280 + hcb * 256 + (b & 1) * 128 : 0, hv ? M_ALL : M_ONE);
      } }
      if constexpr (n == 23) { if (i == 2) {
        const int ct = 8 * hcb + 4 * (b & 1) + (w & 3);
        const s8 f = tr_frag(L_W2S + (8 * gi + q4) * 128 + (w & 3) * 32 + p4 * 8, 512);
        st_b128_sc1_m(__builtin_bit_cast(u32x4, f), rws, hv ? WS_W2P + (((ct / 5) * 68 + hkst) * 5 + ct % 5) * 1024 + l * 16 : OOB, hv ? M_ALL : M_ONE);
      } }
      SB();
    }
  });
  WAITV(4);
#pragma unroll
  for (int r = 0; r < 4; ++r) { TIE(cg4[r]); TIE(cg1[r]); }
  if (w < 4) { const unsigned ad = L_CS + ((h * 5) * 32 + l31) * 16 + w * 4;
#pragma unroll
    for (int e = 0; e < 4; ++e) *(LAS float*)(size_t)(ad + e * 512) = (__uint_as_float(cg4[0][e]) + __uint_as_float(cg4[1][e])) + (__uint_as_float(cg4[2][e]) + __uint_as_float(cg4[3][e]));
    *(LAS float*)(size_t)(ad + 4 * 512) = (__uint_as_float(cg1[0]) + __uint_as_float(cg1[1])) + (__uint_as_float(cg1[2]) + __uint_as_float(cg1[3])); }
  WAITVL_BAR(0);
  TIE(st);
  const float mean = __uint_as_float(st[0]), rstd = __uint_as_float(st[1]);
  s8 bm;
  { const float nm = -mean; const unsigned hi = pk2(nm, 0.f) & 0xffffu; const float lo = nm - __uint_as_float(hi << 16); unsigned mpk = hi | (pk2(lo, 0.f) << 16); if (h) mpk = 0; bm = __builtin_bit_cast(s8, (u32x4){mpk, mpk, 0u, 0u}); }
  auto cs_frag = [&](int tile) -> s8 {
    const f32x4 pp = *(LAS f32x4*)(size_t)(L_CS + ((ch * 5 + tile) * 32 + l31) * 16);
    const float c = (pp[0] + pp[1]) + (pp[2] + pp[3]);
    unsigned hi = pk2(c, c); const float lo = c - __uint_as_float(hi << 16); unsigned lo2 = pk2(lo, lo);
    if (h) { hi = 0; lo2 = 0; }
    return __builtin_bit_cast(s8, (u32x4){hi, lo2, 0u, 0u});
  };
  const unsigned tok = 128 * rb + xrow;
  const unsigned actbase = WS_ACT + (tok >> 4) * 68 * 1024 + (l & 15) * 16;
  {
    const float gs = -1.4426950408889634f * rstd, irs = __builtin_amdgcn_rcpf(rstd);
#pragma unroll
    for (int i = 0; i < 3; ++i) acc[i] = MFMA32(cs_frag(i), bm, acc[i]);
#pragma unroll
    for (int i = 0; i < 3; ++i) {
      issue2(4 + i);
      const f32x2 gs2 = {gs, gs}, irs2 = {irs, irs};
      u32x4 ho;
#pragma unroll
      for (int r = 0; r < 4; ++r) {
        const f32x2 g2 = {acc[i][2 * r], acc[i][2 * r + 1]}, u2 = {acc[i][8 + 2 * r], acc[i][9 + 2 * r]};
        const f32x2 dn = __builtin_elementwise_fma(exp2_2(g2 * gs2), irs2, irs2);
        const f32x2 rc = {__builtin_amdgcn_rcpf(dn[0]), __builtin_amdgcn_rcpf(dn[1])};
        ho[r] = pk2v(u2 * rc);
      }
      const unsigned G = 12 * cg + 6 * ch + 2 * i + h;
      st_b128_sc1(ho, rws, actbase + (G >> 2) * 1024 + (G & 3) * 256);
      SB();
    }
  }
  const int kq = w & 3, rh = w >> 2;
  const unsigned va = WS_ACT + (4 * r2 + 2 * rh) * 68 * 1024 + kq * 1024 + l * 16;
  u32x4 af[10][2];
  auto lda = [&](int t) { af[t % 10][0] = ld_b128_sc1(rws, va, t * 4096); af[t % 10][1] = ld_b128_sc1(rws, va + 68 * 1024, t * 4096); };
  f32x16 T[2];
#pragma unroll
  for (int i = 0; i < 2; ++i)
#pragma unroll
    for (int r = 0; r < 16; ++r) T[i][r] = 0.f;
  s8 qf[3][2];
  const unsigned ql = ch * 8192 + l * 16, ql2 = ql + L_W;
  auto rdq = [&](int m, int t) { const int q = m >> 2, j = m & 3; const unsigned sl = slot2(q); qf[m % 3][t] = lds_b128((sl >= L_W ? ql2 + (sl - L_W) : ql + sl) + j * 2048 + t * 1024); };
  rdq(0, 0); rdq(0, 1); rdq(1, 0); rdq(1, 1);
  static_for<0, 40>([&](auto M_) {
    constexpr int m = decltype(M_)::v;
    if constexpr (m == 14) { WAITV_BAR(0); st_b64_sc1_m(tag, rws, w == 0 ? WS_FH + (rb * 16 + cg) * 8 : OOB, M_ONE); issue2(7); issue2(8); issue2(9); }
    if constexpr (m == 26) {
      WAITV_BAR(0);
      pl0 = ld_b128_sc1_m(rws, WS_FP1 + l * 32, 0, w == 0 ? M_ALL : M_ONE); pl1 = ld_b128_sc1_m(rws, WS_FP1 + l * 32 + 16, 0, w == 0 ? M_ALL : M_ONE);
      pf = POLL16(WS_FH + (r2 >> 1) * 128 + l * 8);
    }
    if constexpr (m == 30) {
      WAITV(0); TIE(pl0); TIE(pl1); TIE(pf);
      if (w == 0) {
        if (__builtin_expect(!flags256_ok(pl0, pl1, tag), 0)) wait_flags256(rws, WS_FP1, tag, l);
        if (__builtin_expect(!flags_ok(pf, tag, l < 16), 0)) wait_flags(rws, WS_FH + (r2 >> 1) * 128, 8, 16, tag, l);
      }
      asm volatile("s_barrier" ::: "memory");
    }
    if constexpr (m >= 30 && (m & 1) == 0) lda((m - 30) >> 1);
    SB();
#pragma unroll
    for (int t = 0; t < 2; ++t) {
      T[t] = MFMA32(qf[m % 3][t], XF[m], T[t]);
      SB();
      if (m + 2 < 40) rdq(m + 2, t);
      SB();
    }
  });
  LBAR();
  const unsigned vb = WS_W2P + nb * 68 * 5 * 1024 + l * 16;
  const unsigned bf0 = w < 4 ? 3 * w : 12 + 2 * (w - 4);
  auto issueB1 = [&](int t, int k) {
    const unsigned base = L_BRING + (t % 6) * B_STAGE;
    if (k < 2) dma16_sc1(base + (bf0 + k) * 1024, vb, rws, t * B_STAGE + (bf0 + k) * 1024);
    else asm volatile("s_mov_b32 m0, %0\n\ts_mov_b64 exec, %4\n\tbuffer_load_dwordx4 %1, %2, %3 offen sc1 lds\n\ts_mov_b64 exec, -1" ::"s"(w < 4 ? base + (bf0 + 2) * 1024 : L_DUMMY), "v"(w < 4 ? vb : OOB), "s"(rws), "s"(t * B_STAGE + (bf0 + 2) * 1024), "s"(w < 4 ? M_ALL : M_ONE) : "memory");
  };
  auto issueB = [&](int t) { issueB1(t, 0); issueB1(t, 1); issueB1(t, 2); };
  f32x16 P;
  const float psc = 1.4426950408889634f * rstd;
  {
    issueB(0);
    T[0] = MFMA32(cs_frag(3), bm, T[0]);
    T[1] = MFMA32(cs_frag(4), bm, T[1]);
    const unsigned wpl = L_WPF + ch * 4096 + l * 16;
    const s8 A0 = lds_b128(wpl), A1 = lds_b128(wpl + 1024), A2 = lds_b128(wpl + 2048), A3 = lds_b128(wpl + 3072);
    s8 E0, E1;
    { const int k0 = l31 - 8 * h; const unsigned val = (k0 & 1) ? 0x3f800000u : 0x3f80u; u32x4 u0, u1;
#pragma unroll
      for (int d2 = 0; d2 < 4; ++d2) { u0[d2] = (k0 >> 1) == d2 ? val : 0u; u1[d2] = ((k0 - 16) >> 1) == d2 ? val : 0u; }
      E0 = __builtin_bit_cast(s8, u0); E1 = __builtin_bit_cast(s8, u1); }
    SB(); issueB(1); SB();
    const s8 t01 = pack_step(T[0], 1), t10u = pack_step(T[1], 0), t00 = pack_step(T[0], 0);
    f32x16 z16;
#pragma unroll
    for (int r = 0; r < 16; ++r) z16[r] = 0.f;
    f32x16 Pk = MFMA32(A2, t01, z16); Pk = MFMA32(A3, t10u, Pk);
#pragma unroll
    for (int r = 0; r < 8; ++r) { const f32x2 v = (f32x2){T[1][2 * r], T[1][2 * r + 1]} * (f32x2){rstd, rstd}; T[1][2 * r] = v[0]; T[1][2 * r + 1] = v[1]; }
    const s8 t10 = pack_step(T[1], 0);
    s8 t11 = pack_step(T[1], 1);
    { u32x4 u = __builtin_bit_cast(u32x4, t11); if (h) u[2] = (u[2] & 0xffff0000u) | 0x3f80u; t11 = __builtin_bit_cast(s8, u); }
    f32x16 V = MFMA32(t10, E0, z16); V = MFMA32(t11, E1, V);
    SB(); lda(5); SB();
    unsigned phk[10];
    { f32x2 ep[5], en[5]; phi_map1(Pk, psc, ep, en); SB(); issueB(2); SB(); phi_map2(ep, en, phk); }
    SB(); lda(6); SB();
    const s8 v0 = pack_step(V, 0), v1 = pack_step(V, 1);
    const s8 F0 = __builtin_bit_cast(s8, (u32x4){phk[0], phk[1], phk[2], phk[3]}), F1 = __builtin_bit_cast(s8, (u32x4){phk[4], phk[5], phk[6], phk[7]}), F2 = __builtin_bit_cast(s8, (u32x4){phk[8], phk[9], 0u, 0u});
    f32x16 Z0 = MFMA32(F0, E0, z16); Z0 = MFMA32(F1, E1, Z0);
    f32x16 Z1 = MFMA32(F2, E0, z16);
    f32x16 KV0 = MFMA32(pack_step(Z0, 0), v0, z16); KV0 = MFMA32(pack_step(Z0, 1), v1, KV0);
    f32x16 KV1 = MFMA32(pack_step(Z1, 0), v0, z16);
    const int rho = 16 * (l31 >> 4) + 8 * ((l31 & 7) >> 2) + 4 * ((l31 >> 3) & 1) + (l31 & 3);
    const int d = rho == 28 ? 20 : rho - 8;
    KV1 = MFMA32(pack_step(Z1, 1), v1, KV1);
    P = MFMA32(A0, t00, z16); P = MFMA32(A1, t01, P);
    if (d >= 0 && d <= 20) {
      const unsigned kb = L_KVR + w * 3360 + d * 160;
#pragma unroll
      for (int g = 0; g < 4; ++g) *(LAS f32x4*)(size_t)(kb + (8 * g + 4 * h) * 4) = (f32x4){KV0[4 * g], KV0[4 * g + 1], KV0[4 * g + 2], KV0[4 * g + 3]};
      if (h == 0) { *(LAS f32x4*)(size_t)(kb + 128) = (f32x4){KV1[0], KV1[1], KV1[2], KV1[3]}; *(LAS f32x4*)(size_t)(kb + 144) = (f32x4){KV1[4], KV1[5], KV1[6], KV1[7]}; }
    }
  }
  LBAR();
  {
    const int c = tid & 255, hc = tid >> 8;
    u32x4 o = {0u, 0u, 0u, 0u};
    if (c < 105) {
      float a[8];
#pragma unroll
      for (int e = 0; e < 8; ++e) a[e] = 0.f;
#pragma unroll
      for (int g = 0; g < 4; ++g) {
        const unsigned ad = L_KVR + (hc * 4 + g) * 3360 + c * 32;
        const f32x4 u0 = *(LAS f32x4*)(size_t)ad, u1 = *(LAS f32x4*)(size_t)(ad + 16);
#pragma unroll
        for (int e = 0; e < 4; ++e) { a[e] += u0[e]; a[4 + e] += u1[e]; }
      }
      o = (u32x4){pk2(a[0], a[1]), pk2(a[2], a[3]), pk2(a[4], a[5]), pk2(a[6], a[7])};
    }
    st_b128_sc1_m(o, rws, c < 105 ? WS_KVP + ((cg * 2 + hc) * 16 + rb) * KVP_STRIDE + c * 16 : OOB, tg == 0 ? M_ALL : (tg == 1 ? 0x1ffffffffffull : M_ONE));
  }
  const rsrc_t rout = mk_rsrc(out, S * D * 2);
  unsigned phq[10];
  lda(7); issueB(3); SB();
  { f32x2 ep[5], en[5]; phi_map1(P, psc, ep, en); SB(); phi_map2(ep, en, phq); }
#pragma unroll
  for (int r = 0; r < 10; ++r) TIE(phq[r]);
  WAITV_BAR(5);
  st_b64_sc1_m(tag, rws, w == 0 ? WS_FKV + (cg * 16 + rb) * 8 : OOB, M_ONE);
  lda(8); issueB(4);
  f32x4 a2[2][5];
#pragma unroll
  for (int a = 0; a < 2; ++a)
#pragma unroll
    for (int c = 0; c < 5; ++c) a2[a][c] = (f32x4){0.f, 0.f, 0.f, 0.f};
  u32x4 kp[8]; u32x2 xr[3]; unsigned go[3]; s8 bfr[2][5];
  const int kc = (64 * tg + l) >> 1; const bool kact = kc < 105;
  static_for<0, 17>([&](auto T_) {
    constexpr int t = decltype(T_)::v;
        constexpr int T_PKV = 2, T_KVL = 3, T_SUM = 5, T_O = 6, T_FO = 9, T_PO = 11, T_AO = 12;
    constexpr int allow_tab[17] = {19, 19, 16, 5, 22, 6, 17, 11, 11, 9, 10, 10, 3, 6, 4, 5, 3};
    asm volatile("s_waitcnt vmcnt(%0) lgkmcnt(0)" ::"i"(allow_tab[t]) : "memory");
    if constexpr (t == T_KVL) { TIE(pf); if (w == 0) { if (__builtin_expect(!flags_ok(pf, tag, l < 16 && l != rb), 0)) wait_flags(rws, WS_FKV + cg * 128, 8, 16, tag, l, rb); } }
    if constexpr (t == T_AO) { TIE(pf); if (w == 0) { { const int own = (r2 >> 1) == rb ? cg : -1; if (__builtin_expect(!flags_ok(pf, tag, l < 16 && l != own), 0)) wait_flags(rws, WS_FO + (r2 >> 1) * 128, 8, 16, tag, l, own); } } }
    asm volatile("s_barrier" ::: "memory");
    if constexpr (t == T_PKV) pf = POLL16(WS_FKV + cg * 128 + l * 8);
    if constexpr (t == T_KVL) {
#pragma unroll
      for (int r = 0; r < 8; ++r) kp[r] = ld_b128_sc1_m(rws, kact ? WS_KVP + ((cg * 2 + ch) * 16 + 8 * (l & 1)) * KVP_STRIDE + kc * 16 : OOB, r * KVP_STRIDE, tg < 3 ? M_ALL : 0x3ffffull);
    }
    if constexpr (t == T_SUM) {
      float a[8];
#pragma unroll
      for (int r = 0; r < 8; ++r) { TIE(kp[r]);
#pragma unroll
        for (int e = 0; e < 4; ++e) { a[2 * e] = r == 0 ? bf_lo(kp[r][e]) : sadd(a[2 * e], bf_lo(kp[r][e])); a[2 * e + 1] = r == 0 ? bf_hi(kp[r][e]) : sadd(a[2 * e + 1], bf_hi(kp[r][e])); } }
      asm volatile("s_nop 1" : "+v"(a[0]), "+v"(a[1]), "+v"(a[2]), "+v"(a[3]), "+v"(a[4]), "+v"(a[5]), "+v"(a[6]), "+v"(a[7]));
#pragma unroll
      for (int e = 0; e < 8; ++e) a[e] = dpp_add(a[e], 0);
      if (kact && (l & 1) == 0) { const int d = kc / 5, cc = kc % 5; *(LAS u32x4*)(size_t)(L_KVS + ch * 2016 + d * 96 + cc * 16) = (u32x4){pk2(a[0], a[1]), pk2(a[2], a[3]), pk2(a[4], a[5]), pk2(a[6], a[7])}; }
    }
    if constexpr (t == T_O) {
      const int reg = (l31 & 3) + 4 * (l31 >> 3), hh = (l31 >> 2) & 1;
      int d;
      if (reg >= 12) d = 20; else if (ch == 0) d = reg < 8 ? 8 * hh + reg : (hh == 0 ? 8 + reg : 20); else d = reg < 8 ? (hh == 0 ? 4 + reg : 12 + reg) : (hh == 0 ? reg - 8 : 20);
      const unsigned ka = L_KVS + ch * 2016 + d * 96;
      const s8 A0 = lds_b128(ka + h * 16), A1 = lds_b128(ka + 32 + h * 16);
      const u32x2 a2l = *(LAS u32x2*)(size_t)(ka + 64 + h * 8);
      const s8 A2 = __builtin_bit_cast(s8, (u32x4){a2l[0], a2l[1], 0u, 0u});
      f32x16 O;
#pragma unroll
      for (int r = 0; r < 16; ++r) O[r] = 0.f;
      O = MFMA32(A0, __builtin_bit_cast(s8, (u32x4){phq[0], phq[1], phq[2], phq[3]}), O);
      O = MFMA32(A1, __builtin_bit_cast(s8, (u32x4){phq[4], phq[5], phq[6], phq[7]}), O);
      O = MFMA32(A2, __builtin_bit_cast(s8, (u32x4){phq[8], phq[9], 0u, 0u}), O);
      float rc = __builtin_amdgcn_rcpf(O[12] + 1e-6f);
      asm volatile("s_nop 1" : "+v"(rc));
      const unsigned G0 = 192 + 5 * cg + (ch == 0 ? h : 3 + h);
      st_b128_sc1((u32x4){pk2(smul(O[0], rc), smul(O[1], rc)), pk2(smul(O[2], rc), smul(O[3], rc)), pk2(smul(O[4], rc), smul(O[5], rc)), pk2(smul(O[6], rc), smul(O[7], rc))}, rws, actbase + (G0 >> 2) * 1024 + (G0 & 3) * 256);
      const unsigned G2 = 192 + 5 * cg + 2;
      st_b64_sc1_m((u32x2){pk2(smul(O[8], rc), smul(O[9], rc)), pk2(smul(O[10], rc), smul(O[11], rc))}, rws, actbase + (G2 >> 2) * 1024 + (G2 & 3) * 256 + ch * 8, 0xffffffffull);
    }
    if constexpr (t == T_FO) st_b64_sc1_m(tag, rws, w == 0 ? WS_FO + (rb * 16 + cg) * 8 : OOB, M_ONE);
    if constexpr (t == T_PO) pf = POLL16(WS_FO + (r2 >> 1) * 128 + l * 8);
    if constexpr (t == T_AO) { lda(12); lda(13); lda(14); lda(15); lda(16); }
    if constexpr (t == 0) { lda(9); issueB(5); }
    if constexpr (t == 1) lda(10);
    if constexpr (t == 2) lda(11);
    if constexpr (t == 14) {
#pragma unroll
      for (int z = 0; z < 3; ++z) { const int ix = kq + 4 * z; const bool v = ix < 10; const int a = ix >= 5 ? 1 : 0, c = ix - 5 * a;
        go[z] = v ? (unsigned)((64 * r2 + 32 * rh + 16 * a + (l & 15)) * 1280 + (80 * nb + 16 * c + 4 * (l >> 4)) * 2) : OOB; xr[z] = ld_b64_m(rx, go[z], v ? M_ALL : M_ONE); }
    }
    if constexpr (t == 12 && T_AO == 12) WAITV(8);
    TIE(af[t % 10][0]); TIE(af[t % 10][1]);
    if constexpr (t == 0) {
#pragma unroll
      for (int c = 0; c < 5; ++c) bfr[0][c] = lds_b128(L_BRING + kq * 5120 + l * 16 + c * 1024);
    }
    SB();
#pragma unroll
    for (int c = 0; c < 5; ++c) {
      a2[0][c] = MFMA16(bfr[t & 1][c], af[t % 10][0], a2[0][c]);
      a2[1][c] = MFMA16(bfr[t & 1][c], af[t % 10][1], a2[1][c]);
      if constexpr (t + 1 < 17) bfr[(t + 1) & 1][c] = lds_b128(L_BRING + ((t + 1) % 6) * B_STAGE + kq * 5120 + l * 16 + c * 1024);
      if constexpr (t >= 1 && t + 5 < 17) { if (c == 0) issueB1(t + 5, 0); if (c == 2) issueB1(t + 5, 1); if (c == 4) issueB1(t + 5, 2); }
      SB();
    }
  });
  LBAR();
  static_for<0, 10>([&](auto I_) { constexpr int ix = decltype(I_)::v;
    if ((ix & 3) != kq) *(LAS f32x4*)(size_t)(L_RED + ((rh * 10 + ix) * 3 + ((kq - ix - 1) & 3)) * 1024 + l * 16) = a2[ix / 5][ix % 5]; });
  asm volatile("s_waitcnt vmcnt(0) lgkmcnt(0)\n\ts_barrier" ::: "memory");
  auto fin = [&](auto I_, int z) { constexpr int ix = decltype(I_)::v;
    const unsigned ad = L_RED + (rh * 10 + ix) * 3 * 1024 + l * 16;
    const f32x4 v = (a2[ix / 5][ix % 5] + *(LAS f32x4*)(size_t)ad) + (*(LAS f32x4*)(size_t)(ad + 1024) + *(LAS f32x4*)(size_t)(ad + 2048));
    st_b64((u32x2){pk2(v[0] + bf_lo(xr[z][0]), v[1] + bf_hi(xr[z][0])), pk2(v[2] + bf_lo(xr[z][1]), v[3] + bf_hi(xr[z][1]))}, rout, go[z]); };
#pragma unroll
  for (int z = 0; z < 3; ++z) TIE(xr[z]);
  if (kq == 0) { fin(IC<0>{}, 0); fin(IC<4>{}, 1); fin(IC<8>{}, 2); }
  else if (kq == 1) { fin(IC<1>{}, 0); fin(IC<5>{}, 1); fin(IC<9>{}, 2); }
  else if (kq == 2) { fin(IC<2>{}, 0); fin(IC<6>{}, 1); }
  else { fin(IC<3>{}, 0); fin(IC<7>{}, 1); }
  asm volatile(".Lfk_end:");
  if (b == 0 && tid == 0) st_b64_sc1(tag, rws, WS_EPOCH);
}

extern "C" void kernel_launch(void* const* d_in, const int* in_sizes, int n_in, void* d_out, int out_size, void* d_ws, size_t ws_size, hipStream_t stream) {
  (void)in_sizes; (void)n_in; (void)out_size; (void)ws_size;
  (void)hipFuncSetAttribute((const void*)fused_k, hipFuncAttributeMaxDynamicSharedMemorySize, LDS_BYTES);
  fused_k<<<256, 512, LDS_BYTES, stream>>>((const u16*)d_in[0], (const u16*)d_in[1], (const u16*)d_in[2], (const u16*)d_in[3], (const u16*)d_in[5], (const u16*)d_in[6], (const u16*)d_in[7], (u16*)d_out, (char*)d_ws);
}
```

```cpp
#include <hip/hip_runtime.h>
#include <hip/hip_bf16.h>
typedef unsigned short u16;
typedef unsigned long long u64;
typedef short s4 __attribute__((ext_vector_type(4)));
typedef short s8 __attribute__((ext_vector_type(8)));
typedef __bf16 b8 __attribute__((ext_vector_type(8)));
typedef __bf16 b2 __attribute__((ext_vector_type(2)));
typedef float f32x16 __attribute__((ext_vector_type(16)));
typedef float f32x4 __attribute__((ext_vector_type(4)));
typedef float f32x2 __attribute__((ext_vector_type(2)));
typedef unsigned u32x4 __attribute__((ext_vector_type(4)));
typedef unsigned u32x2 __attribute__((ext_vector_type(2)));
#define LAS __attribute__((address_space(3)))
#define DI __device__ __forceinline__
typedef __amdgpu_buffer_rsrc_t rsrc_t;

constexpr int S = 2048, D = 640, F = 1536;
constexpr unsigned WS_EPOCH = 0;
constexpr unsigned WS_FP0 = 1024, WS_FP1 = 3072, WS_FH = 5120, WS_FO = 7168, WS_FKV = 9216, WS_FCS = 11264;
constexpr unsigned WS_STATS = 16384;
constexpr unsigned WS_KVP = 32768;
constexpr unsigned KVP_STRIDE = 1792;
constexpr unsigned WS_WQP = 1u << 20;
constexpr unsigned WS_W2P = 4u << 20;
constexpr unsigned WS_CSP = 7u << 20;
constexpr unsigned WS_ACT = 8u << 20;
constexpr unsigned WS_USED = 17u << 20;
constexpr unsigned OOB = 0x80000000u;
constexpr unsigned L_X = 0;
constexpr unsigned L_W = 49152;
constexpr unsigned W_STAGE = 24832, W_UOFF = 12320;
constexpr unsigned L_MISC = L_W + 4 * W_STAGE;
constexpr unsigned L_W2S = L_MISC;
constexpr unsigned L_CS = L_W2S;
constexpr unsigned L_WPF = L_W2S + 7168;
constexpr unsigned L_QRAW = L_W + 3 * W_STAGE;
constexpr unsigned L_WPR = L_QRAW + 12288;
constexpr unsigned L_W2A = L_QRAW + 14336;
constexpr unsigned L_BRING = 0;
constexpr unsigned B_STAGE = 20480;
constexpr unsigned L_KVR = 122880;
constexpr unsigned L_KVS = 149760;
constexpr unsigned L_DUMMY1 = L_QRAW + 23552;
constexpr unsigned L_DUMMY = L_W2S + 6144;
constexpr unsigned L_RED = 0;
constexpr unsigned LDS_BYTES = 160 * 1024;

DI rsrc_t mk_rsrc(const void* p, unsigned bytes) { return __builtin_amdgcn_make_buffer_rsrc((void*)p, 0, bytes, 0x00020000); }
DI void dma16(unsigned lds_addr, unsigned voff, rsrc_t r, unsigned soff) {
  asm volatile("s_mov_b32 m0, %0\n\ts_nop 0\n\tbuffer_load_dwordx4 %1, %2, %3 offen lds" ::"s"(lds_addr), "v"(voff), "s"(r), "s"(soff) : "memory");
}
DI void dma16_sc1(unsigned lds_addr, unsigned voff, rsrc_t r, unsigned soff) {
  asm volatile("s_mov_b32 m0, %0\n\ts_nop 0\n\tbuffer_load_dwordx4 %1, %2, %3 offen sc1 lds" ::"s"(lds_addr), "v"(voff), "s"(r), "s"(soff) : "memory");
}
DI unsigned ld_u16(rsrc_t r, unsigned voff, unsigned soff) { unsigned v; asm volatile("buffer_load_ushort %0, %1, %2, %3 offen" : "=v"(v) : "v"(voff), "s"(r), "s"(soff) : "memory"); return v; }
DI unsigned ld_b32(rsrc_t r, unsigned voff, unsigned soff) { unsigned v; asm volatile("buffer_load_dword %0, %1, %2, %3 offen" : "=v"(v) : "v"(voff), "s"(r), "s"(soff) : "memory"); return v; }
DI u32x2 ld_b64_sc1(rsrc_t r, unsigned voff, unsigned soff) { u32x2 v; asm volatile("buffer_load_dwordx2 %0, %1, %2, %3 offen sc1" : "=v"(v) : "v"(voff), "s"(r), "s"(soff) : "memory"); return v; }
DI u32x4 ld_b128_sc1(rsrc_t r, unsigned voff, unsigned soff) { u32x4 v; asm volatile("buffer_load_dwordx4 %0, %1, %2, %3 offen sc1" : "=v"(v) : "v"(voff), "s"(r), "s"(soff) : "memory"); return v; }
DI u32x2 ld_b64_m(rsrc_t r, unsigned voff, u64 mask) { u32x2 v; asm volatile("s_mov_b64 exec, %3\n\tbuffer_load_dwordx2 %0, %1, %2, 0 offen\n\ts_mov_b64 exec, -1" : "=v"(v) : "v"(voff), "s"(r), "s"(mask) : "memory"); return v; }
DI void st_b64(u32x2 v, rsrc_t r, unsigned voff) { asm volatile("buffer_store_dwordx2 %0, %1, %2, 0 offen" ::"v"(v), "v"(voff), "s"(r) : "memory"); }
DI u32x4 ld_b128(rsrc_t r, unsigned voff, unsigned soff) { u32x4 v; asm volatile("buffer_load_dwordx4 %0, %1, %2, %3 offen" : "=v"(v) : "v"(voff), "s"(r), "s"(soff) : "memory"); return v; }
DI void st_b128_sc1(u32x4 v, rsrc_t r, unsigned voff) {
 asm volatile("buffer_store_dwordx4 %0, %1, %2, 0 offen sc1\n\ts_nop 1" ::"v"(v), "v"(voff), "s"(r) : "memory"); }
DI void st_b64_sc1(u32x2 v, rsrc_t r, unsigned voff) { asm volatile("buffer_store_dwordx2 %0, %1, %2, 0 offen sc1" ::"v"(v), "v"(voff), "s"(r) : "memory"); }
DI void st_b128(u32x4 v, rsrc_t r, unsigned voff) { asm volatile("buffer_store_dwordx4 %0, %1, %2, 0 offen\n\ts_nop 1" ::"v"(v), "v"(voff), "s"(r) : "memory"); }
DI void st_b64_sc1_m(u32x2 v, rsrc_t r, unsigned voff, u64 mask) {
 asm volatile("s_mov_b64 exec, %3\n\tbuffer_store_dwordx2 %0, %1, %2, 0 offen sc1\n\ts_mov_b64 exec, -1" ::"v"(v), "v"(voff), "s"(r), "s"(mask) : "memory"); }
DI void st_b32_sc1_m(unsigned v, rsrc_t r, unsigned voff, u64 mask) { asm volatile("s_mov_b64 exec, %3\n\tbuffer_store_dword %0, %1, %2, 0 offen sc1\n\ts_mov_b64 exec, -1" ::"v"(v), "v"(voff), "s"(r), "s"(mask) : "memory"); }
DI u32x2 ld_b64_sc1_m(rsrc_t r, unsigned voff, u64 mask) { u32x2 v; asm volatile("s_mov_b64 exec, %3\n\tbuffer_load_dwordx2 %0, %1, %2, 0 offen sc1\n\ts_mov_b64 exec, -1" : "=v"(v) : "v"(voff), "s"(r), "s"(mask) : "memory"); return v; }
DI unsigned ld_b32_sc1_m(rsrc_t r, unsigned voff, unsigned soff, u64 mask) { unsigned v; asm volatile("s_mov_b64 exec, %4\n\tbuffer_load_dword %0, %1, %2, %3 offen sc1\n\ts_mov_b64 exec, -1" : "=v"(v) : "v"(voff), "s"(r), "s"(soff), "s"(mask) : "memory"); return v; }
DI u32x4 ld_b128_sc1_m(rsrc_t r, unsigned voff, unsigned soff, u64 mask) { u32x4 v; asm volatile("s_mov_b64 exec, %4\n\tbuffer_load_dwordx4 %0, %1, %2, %3 offen sc1\n\ts_mov_b64 exec, -1" : "=v"(v) : "v"(voff), "s"(r), "s"(soff), "s"(mask) : "memory"); return v; }
DI void st_b128_sc1_m(u32x4 v, rsrc_t r, unsigned voff, u64 mask) {
 asm volatile("s_mov_b64 exec, %3\n\tbuffer_store_dwordx4 %0, %1, %2, 0 offen sc1\n\ts_mov_b64 exec, -1\n\ts_nop 0" ::"v"(v), "v"(voff), "s"(r), "s"(mask) : "memory"); }
DI void dma16_m(unsigned lds_addr, unsigned voff, rsrc_t r, unsigned soff, u64 mask) {
  asm volatile("s_mov_b32 m0, %0\n\ts_mov_b64 exec, %4\n\tbuffer_load_dwordx4 %1, %2, %3 offen lds\n\ts_mov_b64 exec, -1" ::"s"(lds_addr), "v"(voff), "s"(r), "s"(soff), "s"(mask) : "memory");
}
#define POLL16(off) ld_b64_sc1_m(rws, w == 0 ? (off) : OOB, w == 0 ? 0xffffull : M_ONE)
#define TIE(x) asm volatile("" : "+v"(x))
#define WAITV(n) asm volatile("s_waitcnt vmcnt(" #n ")" ::: "memory")
#define WAITV_BAR(n) asm volatile("s_waitcnt vmcnt(" #n ")\n\ts_barrier" ::: "memory")
#define WAITVL_BAR(n) asm volatile("s_waitcnt vmcnt(" #n ") lgkmcnt(0)\n\ts_barrier" ::: "memory")
#define LBAR() asm volatile("s_waitcnt lgkmcnt(0)\n\ts_barrier" ::: "memory")
#define SB() __builtin_amdgcn_sched_barrier(0)
#define MFMA32(a, b, c) __builtin_amdgcn_mfma_f32_32x32x16_bf16(__builtin_bit_cast(b8, (a)), __builtin_bit_cast(b8, (b)), (c), 0, 0, 0)
#define MFMA16(a, b, c) __builtin_amdgcn_mfma_f32_16x16x32_bf16(__builtin_bit_cast(b8, (a)), __builtin_bit_cast(b8, (b)), (c), 0, 0, 0)
DI s8 lds_b128(unsigned addr) { return *(LAS s8*)(size_t)addr; }
DI s8 tr_frag(unsigned addr, unsigned off2) {
  s4 a = __builtin_amdgcn_ds_read_tr16_b64_v4i16((LAS s4*)(size_t)addr);
  s4 b = __builtin_amdgcn_ds_read_tr16_b64_v4i16((LAS s4*)(size_t)(addr + off2));
  return __builtin_shufflevector(a, b, 0, 1, 2, 3, 4, 5, 6, 7);
}
DI float dot_ones(s8 f, float acc) {
  u32x4 u = __builtin_bit_cast(u32x4, f);
#pragma unroll
  for (int i = 0; i < 4; ++i) { const unsigned t = u[i]; acc = __builtin_amdgcn_fdot2_f32_bf16(__builtin_bit_cast(b2, t), __builtin_bit_cast(b2, 0x3f803f80u), acc, false); }
  asm volatile("" : "+v"(acc));
  return acc;
}
DI unsigned pk2(float lo, float hi) { b2 c; c[0] = (__bf16)lo; c[1] = (__bf16)hi; return __builtin_bit_cast(unsigned, c); }
DI float bf_lo(unsigned u) { return __uint_as_float(u << 16); }
DI float bf_hi(unsigned u) { return __uint_as_float(u & 0xffff0000u); }
DI float swap_add(float v) { unsigned u = __float_as_uint(v); auto r = __builtin_amdgcn_permlane32_swap(u, u, false, false); return __uint_as_float(r[0]) + __uint_as_float(r[1]); }
DI float swap_max(float v) { unsigned u = __float_as_uint(v); auto r = __builtin_amdgcn_permlane32_swap(u, u, false, false); return fmaxf(__uint_as_float(r[0]), __uint_as_float(r[1])); }
DI float swap_min(float v) { unsigned u = __float_as_uint(v); auto r = __builtin_amdgcn_permlane32_swap(u, u, false, false); return fminf(__uint_as_float(r[0]), __uint_as_float(r[1])); }
DI float sadd(float a, float b) { float r; asm("v_add_f32 %0, %1, %2" : "=v"(r) : "v"(a), "v"(b)); return r; }
DI float smul(float a, float b) { float r; asm("v_mul_f32 %0, %1, %2" : "=v"(r) : "v"(a), "v"(b)); return r; }
DI float sfma(float a, float b, float c) { float r; asm("v_fma_f32 %0, %1, %2, %3" : "=v"(r) : "v"(a), "v"(b), "v"(c)); return r; }
DI float dpp_add(float v, const int ctrl_sel) {
  const int u = __float_as_int(v); int r;
  if (ctrl_sel == 0) r = __builtin_amdgcn_update_dpp(0, u, 0xB1, 0xf, 0xf, true);
  else if (ctrl_sel == 1) r = __builtin_amdgcn_update_dpp(0, u, 0x4E, 0xf, 0xf, true);
  else if (ctrl_sel == 2) r = __builtin_amdgcn_update_dpp(0, u, 0x141, 0xf, 0xf, true);
  else r = __builtin_amdgcn_update_dpp(0, u, 0x140, 0xf, 0xf, true);
  return v + __int_as_float(r);
}
DI float wave_sum(float v) {
  v = dpp_add(v, 0); v = dpp_add(v, 1); v = dpp_add(v, 2); v = dpp_add(v, 3);
  { unsigned u = __float_as_uint(v); auto r = __builtin_amdgcn_permlane16_swap(u, u, false, false); v = __uint_as_float(r[0]) + __uint_as_float(r[1]); }
  return swap_add(v);
}
DI float sum8(s8 f, float acc) {
  const u32x4 u = __builtin_bit_cast(u32x4, f);
#pragma unroll
  for (int i = 0; i < 4; ++i) { const unsigned t = u[i]; acc += __uint_as_float(t << 16); acc += __uint_as_float(t & 0xffff0000u); }
  asm volatile("" : "+v"(acc));
  return acc;
}
DI s8 pack_step(const f32x16& x, int s) {
  return __builtin_bit_cast(s8, (u32x4){pk2(x[8 * s], x[8 * s + 1]), pk2(x[8 * s + 2], x[8 * s + 3]), pk2(x[8 * s + 4], x[8 * s + 5]), pk2(x[8 * s + 6], x[8 * s + 7])});
}
template <int I> struct IC { static constexpr int v = I; };
template <int B, int E, class Fn> DI void static_for(Fn&& f) { if constexpr (B < E) { f(IC<B>{}); static_for<B + 1, E>(f); } }
DI bool flags_ok(u32x2 v, u32x2 tag, bool act) { return __all(!act || (v[0] == tag[0] && v[1] == tag[1])); }
DI void wait_flags(rsrc_t rws, unsigned base, unsigned stride, int n, u32x2 tag, int l, int skip = -1) {

  const bool act = l < n && l != skip; const unsigned off = act ? base + l * stride : OOB;
#pragma nounroll
  for (int it = 0; it < (1 << 17); ++it) {
    u32x2 v; asm volatile("buffer_load_dwordx2 %0, %1, %2, 0 offen sc1\n\ts_waitcnt vmcnt(0)" : "=&v"(v) : "v"(off), "s"(rws) : "memory");
    if (flags_ok(v, tag, act)) return;
    __builtin_amdgcn_s_sleep(1);
  }
}
DI void wait_flags256(rsrc_t rws, unsigned base, u32x2 tag, int l) {
#pragma nounroll
  for (int it = 0; it < (1 << 17); ++it) {
    u32x4 a, b;
    asm volatile("buffer_load_dwordx4 %0, %2, %3, 0 offen sc1\n\tbuffer_load_dwordx4 %1, %2, %3, 0 offen offset:16 sc1\n\ts_waitcnt vmcnt(0)" : "=&v"(a), "=&v"(b) : "v"(base + l * 32), "s"(rws) : "memory");
    const bool ok = a[0] == tag[0] && a[1] == tag[1] && a[2] == tag[0] && a[3] == tag[1] && b[0] == tag[0] && b[1] == tag[1] && b[2] == tag[0] && b[3] == tag[1];
    if (__all(ok)) return;
    __builtin_amdgcn_s_sleep(1);
  }
}
DI bool flags256_ok(u32x4 a, u32x4 b, u32x2 tag) {
  return __all(a[0] == tag[0] && a[1] == tag[1] && a[2] == tag[0] && a[3] == tag[1] && b[0] == tag[0] && b[1] == tag[1] && b[2] == tag[0] && b[3] == tag[1]);
}
DI float max3(float a, float b, float c) { float r; asm("v_max3_f32 %0, %1, %2, %3" : "=v"(r) : "v"(a), "v"(b), "v"(c)); return r; }
DI float min3(float a, float b, float c) { float r; asm("v_min3_f32 %0, %1, %2, %3" : "=v"(r) : "v"(a), "v"(b), "v"(c)); return r; }
DI f32x2 exp2_2(f32x2 a) { f32x2 r; r[0] = __builtin_amdgcn_exp2f(a[0]); r[1] = __builtin_amdgcn_exp2f(a[1]); return r; }
DI unsigned pk2v(f32x2 a) { return pk2(a[0], a[1]); }
DI void phi_map1(const f32x16& pin, float sc, f32x2 (&ep)[5], f32x2 (&en)[5]) {
  float p[10];
#pragma unroll
  for (int r = 0; r < 10; ++r) p[r] = pin[r];
  asm volatile("s_nop 7\n\ts_nop 3" : "+v"(p[0]), "+v"(p[1]), "+v"(p[2]), "+v"(p[3]), "+v"(p[4]), "+v"(p[5]), "+v"(p[6]), "+v"(p[7]), "+v"(p[8]), "+v"(p[9]));
  float mx = max3(p[0], p[1], p[2]), mn = min3(p[0], p[1], p[2]);
#pragma unroll
  for (int r = 3; r < 9; r += 2) { mx = max3(mx, p[r], p[r + 1]); mn = min3(mn, p[r], p[r + 1]); }
  mx = max3(mx, p[9], p[9]); mn = min3(mn, p[9], p[9]);
  { unsigned u = __float_as_uint(mx); auto r = __builtin_amdgcn_permlane32_swap(u, u, false, false); mx = max3(__uint_as_float(r[0]), __uint_as_float(r[1]), __uint_as_float(r[1])); }
  { unsigned u = __float_as_uint(mn); auto r = __builtin_amdgcn_permlane32_swap(u, u, false, false); mn = min3(__uint_as_float(r[0]), __uint_as_float(r[1]), __uint_as_float(r[1])); }
  const f32x2 s2 = {sc, sc}, bp = {-mx * sc, -mx * sc}, bn = {mn * sc, mn * sc};
#pragma unroll
  for (int r = 0; r < 5; ++r) { const f32x2 v = {p[2 * r], p[2 * r + 1]}; ep[r] = exp2_2(__builtin_elementwise_fma(v, s2, bp)); en[r] = exp2_2(__builtin_elementwise_fma(v, -s2, bn)); }
}
DI void phi_map2(const f32x2 (&ep)[5], const f32x2 (&en)[5], unsigned (&o)[10]) {
  const f32x2 sp2 = (ep[0] + ep[1]) + (ep[2] + ep[3]) + ep[4], sn2 = (en[0] + en[1]) + (en[2] + en[3]) + en[4];
  const float rp = __builtin_amdgcn_rcpf(swap_add(sp2[0] + sp2[1])), rn = __builtin_amdgcn_rcpf(swap_add(sn2[0] + sn2[1]));
  const f32x2 rp2 = {rp, rp}, rn2 = {rn, rn};
#pragma unroll
  for (int r = 0; r < 5; ++r) { o[r] = pk2v(ep[r] * rp2); o[5 + r] = pk2v(en[r] * rn2); }
}

#define STAMP(i)
#define STAMPL(k)
#define STAMPC(i)
#define STAMPF(i)

__global__ __launch_bounds__(512, 2) void fused_k(const u16* __restrict__ x, const u16* __restrict__ wqkv, const u16* __restrict__ wphi, const u16* __restrict__ wo,
                                                   const u16* __restrict__ wg, const u16* __restrict__ wu, const u16* __restrict__ wd, u16* __restrict__ out, char* __restrict__ ws) {
  extern __shared__ __attribute__((aligned(256))) char smem[];
  const int tid = threadIdx.x; const int l = tid & 63; const int w = __builtin_amdgcn_readfirstlane(tid >> 6);
  const int tg = w & 3, ch = w >> 2; const int l31 = l & 31, h = l >> 5;
  const int b = blockIdx.x, xcd = b & 7, idx = b >> 3;
  const int rb = (xcd & 1) * 8 + (idx & 7), cg = (xcd >> 1) * 4 + (idx >> 3);
  const int r2 = (xcd & 3) * 8 + (idx & 7), nb = (xcd >> 2) * 4 + (idx >> 3);
  const int klo = (5 * rb) >> 1, kcnt = ((5 * (rb + 1)) >> 1) - klo, rot = klo >> 2, kloc = klo - 4 * rot;
  STAMP(0);
  STAMPL(0);
  const rsrc_t rx = mk_rsrc(x, S * D * 2);
  const rsrc_t rw = mk_rsrc(w < 4 ? wg : wu, D * F * 2);
  const rsrc_t rws = mk_rsrc(ws, WS_USED);
  const u64 M_ALL = ~0ull, M_ONE = 1ull;
  auto sig = [&](int s) -> unsigned { const int t = s + rot; return t >= 10 ? t - 10 : t; };
  unsigned vx[2], vw[3];
#pragma unroll
  for (int i = 0; i < 2; ++i) { const int r = 8 * (2 * w + i) + (l >> 3); const int c = (l & 7) ^ ((r >> 1) & 7); vx[i] = (128 * rb + r) * 1280 + c * 16; }
#pragma unroll
  for (int i = 0; i < 3; ++i) { const int p = 64 * (3 * (w & 3) + i) + l; const int k = p / 12, c = p % 12; vw[i] = k * 3072 + cg * 192 + c * 16; }
  const unsigned lx0 = L_X + 2 * w * 1024;
  const unsigned lw0 = L_W + (w < 4 ? 0 : W_UOFF) + 3 * (w & 3) * 1024;
  auto dmax = [&](int s, int i) { dma16(lx0 + (s % 3) * 16384 + i * 1024, vx[i], rx, sig(s) * 128); };
  auto dmaw = [&](int s, int i) { dma16(lw0 + (s & 3) * W_STAGE + i * 1024, vw[i], rw, sig(s) * (64 * 3072)); };
  auto issue1 = [&](int s) { dmax(s, 0); dmax(s, 1); dmaw(s, 0); dmaw(s, 1); dmaw(s, 2); };
  issue1(0);
  u32x2 ep = ld_b64_sc1_m(rws, WS_EPOCH, M_ONE);
  const int srow = 128 * rb + 8 * cg + w;
  const bool v3 = kcnt == 3 && w < 4;
  u32x4 sx0, sx1;
  {
    const rsrc_t rq = mk_rsrc(wqkv, D * 1920 * 2);
    const int p = 64 * (w & 3) + l; const int k = p / 15, c = p % 15;
    const unsigned vo = p < 240 ? (unsigned)(k * 3840 + (c / 5) * 1280 + cg * 80 + (c % 5) * 16) : OOB;
    dma16(L_QRAW + w * 1024, vo, rq, (16 * (klo + (w >> 2))) * 3840);
    dma16_m(v3 ? L_QRAW + 8192 + w * 1024 : L_DUMMY1, v3 ? vo : OOB, rq, v3 ? (16 * (klo + 2)) * 3840 : 0, v3 ? M_ALL : M_ONE);
    const rsrc_t rp = mk_rsrc(wphi, 32 * 400 * 2);
    dma16_m(w < 2 ? L_WPR + w * 1024 : L_DUMMY1, (w < 2 && w * 64 + l < 100) ? (unsigned)(cg * 1600 + (w * 64 + l) * 16) : OOB, rp, 0, w == 0 ? M_ALL : (w == 1 ? 0xfffffffffull : M_ONE));
    sx0 = ld_b128(rx, srow * 1280 + l * 16, 0);
    sx1 = ld_b128(rx, l < 16 ? (unsigned)(srow * 1280 + 1024 + l * 16) : OOB, 0);
    const int kst = b / 5, cb = b % 5; const bool isd = kst < 48;
    const rsrc_t rr = mk_rsrc(isd ? wd : wo, isd ? F * D * 2 : D * D * 2);
    dma16(L_W2A + w * 1024, (unsigned)((l >> 4) * 1280 + (l & 15) * 16), rr, ((isd ? 32 * kst : 32 * (kst - 48)) + 4 * w) * 1280 + cb * 256);
  }
  const int gi = l >> 4, q4 = (l >> 2) & 3, p4 = l & 3;
  issue1(1); issue1(2);
  {
    u64 pc; unsigned len;
    asm volatile("s_getpc_b64 %0\n.Lfk_here:\n\ts_mov_b32 %1, .Lfk_end-.Lfk_here" : "=s"(pc), "=s"(len));
    const rsrc_t rc = mk_rsrc((const void*)pc, len);
    const unsigned off = (idx * 8 + w) * 256;
    asm volatile("s_mov_b32 m0, %0\n\ts_mov_b64 exec, 0xffff\n\tbuffer_load_dwordx4 %1, %2, 0 offen lds\n\ts_mov_b64 exec, -1" ::"s"(L_DUMMY1), "v"(off + l * 16), "s"(rc) : "memory");
  }
  WAITV_BAR(11);
  STAMP(1);
  STAMPL(1);
  TIE(ep);
  u32x2 tag;
  { const unsigned lo = __builtin_amdgcn_readfirstlane(ep[0]), hi = __builtin_amdgcn_readfirstlane(ep[1]); tag[0] = lo + 1u; tag[1] = hi + (lo == 0xffffffffu ? 1u : 0u); }
  const int shc = (w >> 1) & 1, sht = w & 1;
  s8 sf0, sf1, sf2; unsigned se[8]; unsigned smA, smB;
  {
    const int Q = 4 * (gi & 1) + p4;
    int seg, qd;
    if (sht == 0) { seg = Q < 5 ? 0 : 1; qd = Q < 5 ? Q : Q - 5; } else { seg = Q < 2 ? 1 : 2; qd = Q < 2 ? 3 + Q : (Q < 7 ? Q - 2 : 4); }
    const unsigned lo = L_QRAW + (8 * h + q4) * 240 + seg * 80 + shc * 40 + qd * 8;
    sf0 = tr_frag(lo + (w >> 2) * 4096, 960); sf1 = tr_frag(lo + 8192, 960);
    sf2 = tr_frag(L_W2A + (8 * gi + q4) * 256 + w * 32 + p4 * 8, 1024);
    const int reg = (l31 & 3) + 4 * (l31 >> 3), hh = (l31 >> 2) & 1;
    const int d0 = (tg == 0 ? 0 : tg == 1 ? 16 : tg == 2 ? -4 : 12) + 4 * h;
    const unsigned wa = L_WPR + ch * 800 + (10 * hh + reg) * 2 + d0 * 40;
#pragma unroll
    for (int j = 0; j < 8; ++j) se[j] = *(LAS u16*)(size_t)(wa + (8 * (j >> 2) + (j & 3)) * 40);
    smA = (reg < 10 && (unsigned)d0 < 20u) ? ~0u : 0u; smB = (reg < 10 && (unsigned)(d0 + 8) < 20u) ? ~0u : 0u;
  }
  auto side = [&](int part) {
    if (part == 0) {
      TIE(sx0); TIE(sx1);
      float sm = 0.f, sq = 0.f;
#pragma unroll
      for (int i = 0; i < 4; ++i) { const unsigned u0 = sx0[i], u1 = sx1[i];
        sm = __builtin_amdgcn_fdot2_f32_bf16(__builtin_bit_cast(b2, u0), __builtin_bit_cast(b2, 0x3f803f80u), sm, false); sq = __builtin_amdgcn_fdot2_f32_bf16(__builtin_bit_cast(b2, u0), __builtin_bit_cast(b2, u0), sq, false);
        sm = __builtin_amdgcn_fdot2_f32_bf16(__builtin_bit_cast(b2, u1), __builtin_bit_cast(b2, 0x3f803f80u), sm, false); sq = __builtin_amdgcn_fdot2_f32_bf16(__builtin_bit_cast(b2, u1), __builtin_bit_cast(b2, u1), sq, false); }
      const float m = wave_sum(sm) * (1.f / D);
      const float var = fmaxf(wave_sum(sq) * (1.f / D) - m * m, 0.f);
      st_b64_sc1_m((u32x2){__float_as_uint(m), __float_as_uint(__builtin_amdgcn_rsqf(var + 1e-5f))}, rws, WS_STATS + srow * 8, M_ONE);
    } else if (part == 1) {
      st_b128_sc1(__builtin_bit_cast(u32x4, sf0), rws, WS_WQP + (((2 * cg + shc) * 40 + klo + (w >> 2)) * 2 + sht) * 1024 + l * 16);
      st_b128_sc1_m(__builtin_bit_cast(u32x4, sf1), rws, v3 ? WS_WQP + (((2 * cg + shc) * 40 + klo + 2) * 2 + sht) * 1024 + l * 16 : OOB, v3 ? M_ALL : M_ONE);
      const int kst = b / 5, cb = b % 5, ct = 8 * cb + w;
      st_b128_sc1(__builtin_bit_cast(u32x4, sf2), rws, WS_W2P + (((ct / 5) * 68 + kst) * 5 + ct % 5) * 1024 + l * 16);
      const float c0 = swap_add(dot_ones(sf0, 0.f)), c1 = swap_add(dot_ones(sf1, 0.f));
      if (h == 0) { const unsigned qa = L_W2S + ((shc * 2 + sht) * 32 + l31) * 16 + (w >> 2) * 4; *(LAS float*)(size_t)qa = c0; *(LAS float*)(size_t)(qa + 8) = v3 ? c1 : 0.f; }
    } else {
      *(LAS u32x4*)(size_t)(L_WPF + w * 1024 + l * 16) = (u32x4){(se[0] | (se[1] << 16)) & smA, (se[2] | (se[3] << 16)) & smA, (se[4] | (se[5] << 16)) & smB, (se[6] | (se[7] << 16)) & smB};
    }
  };
  STAMPF(8);
  const unsigned qoff = (p4 == 1) ? 8 : (p4 == 2) ? 4 : (p4 == 3) ? 12 : 0;
  const unsigned wl = L_W + ((gi & 1) ? W_UOFF : 0) + (8 * h + q4) * 192 + 2 * qoff + 96 * ch;
  const unsigned wl2 = wl + 2 * W_STAGE;
  const int xrow = 32 * tg + l31;
  unsigned xa[4];
#pragma unroll
  for (int j = 0; j < 4; ++j) { xa[j] = (L_X + xrow * 128 + ((h ^ ((xrow >> 1) & 7)) << 4)) ^ (j * 32); asm volatile("" : "+v"(xa[j])); }
  s8 XF[40];
  f32x16 acc[3]; float cs[3];
#pragma unroll
  for (int i = 0; i < 3; ++i) { cs[i] = 0.f;
#pragma unroll
    for (int r = 0; r < 16; ++r) acc[i][r] = 0.f; }
  s8 wf[2][3];
  auto rdx = [&](int s, int j) { XF[4 * s + j] = lds_b128(xa[j] + (s % 3) * 16384); };
  auto rdw = [&](int n, int i) { const int s = n >> 2, j = n & 3; wf[n & 1][i] = tr_frag(((s & 2) ? wl2 : wl) + (s & 1) * W_STAGE + j * 3072 + i * 32, 768); };
  const unsigned vq2 = WS_WQP + ((2 * cg + ch) * 40 + tg) * 2048 + l * 16;
  auto slot2 = [&](int q) -> unsigned { return q == 0 || q == 7 ? 16384u : q == 1 || q == 8 ? L_W + 2 * W_STAGE : q == 2 || q == 9 ? 32768u : q == 3 ? L_W + 3 * W_STAGE : q == 4 ? 0u : q == 5 ? L_W : L_W + W_STAGE; };
  auto issue2 = [&](int q) { const unsigned so = sig(q) * 8192; dma16_sc1(slot2(q) + w * 2048, vq2, rws, so); dma16_sc1(slot2(q) + w * 2048 + 1024, vq2, rws, so + 1024); };
  const bool hv = b < 168 && w < 4; const int hu = 256 + (b >> 1), hkst = hu / 5, hcb = hu % 5;
  u32x2 pf, pf2, st; u32x4 pl0, pl1, cg4[4]; unsigned cg1[4]; f32x4 qs0, qs1;
  rdx(0, 0); rdw(0, 0); rdw(0, 1); rdw(0, 2); rdx(0, 1); rdx(0, 2); rdx(0, 3);
  static_for<0, 40>([&](auto N_) {
    constexpr int n = decltype(N_)::v;
    constexpr int s = n >> 2, j = n & 3;
    if constexpr (n == 3) STAMPF(9);
    if constexpr (n == 11) STAMPF(10);
    if constexpr (n == 19) STAMPF(11);
    if constexpr (n == 27) STAMPF(12);
    if constexpr (n == 35) STAMPF(13);
    if constexpr (j == 3 && s < 9) STAMPL(2 + 2 * s);
    if constexpr (n == 3) WAITV_BAR(10);
    if constexpr (n == 7) { WAITV_BAR(9); qs0 = *(LAS f32x4*)(size_t)(L_W2S + ((ch * 2) * 32 + l31) * 16); qs1 = *(LAS f32x4*)(size_t)(L_W2S + ((ch * 2 + 1) * 32 + l31) * 16); }
    if constexpr (n == 11) WAITV_BAR(7);
    if constexpr (n == 15) { WAITV_BAR(5); st_b64_sc1_m(tag, rws, w == 0 ? WS_FP0 + (cg * 16 + rb) * 8 : OOB, M_ONE); }
    if constexpr (n == 19) WAITV_BAR(7);
    if constexpr (n == 23) WAITV_BAR(5);
    if constexpr (n == 27) { WAITV_BAR(6); pf = POLL16(WS_FP0 + cg * 128 + l * 8); pf2 = POLL16(WS_FP0 + rb * 8 + (l & 15) * 128); }
    if constexpr (n == 31) {
      WAITV(5); TIE(pf); TIE(pf2);
      if (w == 0) {
        if (__builtin_expect(!flags_ok(pf, tag, l < 16), 0)) wait_flags(rws, WS_FP0 + cg * 128, 8, 16, tag, l);
        if (__builtin_expect(!flags_ok(pf2, tag, l < 16), 0)) wait_flags(rws, WS_FP0 + rb * 8, 128, 16, tag, l);
      }
      asm volatile("s_barrier" ::: "memory");
      st_b64_sc1_m(tag, rws, w == 0 ? WS_FP1 + b * 8 : OOB, M_ONE);
      st = ld_b64_sc1(rws, WS_STATS + (128 * rb + xrow) * 8, 0);
      {
        const unsigned vo = w < 4 ? WS_CSP + (cg * 16 + 4 * w) * 1280 + l * 16 : OOB, vo1 = w < 4 ? WS_CSP + (cg * 16 + 4 * w) * 1280 + 1024 + l * 4 : OOB;
#pragma unroll
        for (int r = 0; r < 4; ++r) { cg4[r] = ld_b128_sc1_m(rws, vo, r * 1280, w < 4 ? M_ALL : M_ONE); cg1[r] = ld_b32_sc1_m(rws, vo1, r * 1280, w < 4 ? M_ALL : M_ONE); } }
      issue2(0); issue2(1);
    }
    if constexpr (n == 35) { WAITV_BAR(14); issue2(2); issue2(3); }
    if constexpr (j == 3 && s < 9) STAMPL(3 + 2 * s);
    SB();
#pragma unroll
    for (int i = 0; i < 3; ++i) {
      acc[i] = MFMA32(wf[n & 1][i], XF[n], acc[i]);
      SB();
      if (i == 0 && j == 3 && s < 9) rdx(s + 1, 0);
      if (n + 1 < 40) rdw(n + 1, i);
      if (j == 0 && s > 0) rdx(s, i + 1);
      if (n < 3 && i == 2) side(n);
      if (n < 8 && i == 2) { if ((unsigned)(n - kloc) < (unsigned)kcnt && tg == 0) { cs[0] = dot_ones(wf[n & 1][0], cs[0]); cs[1] = dot_ones(wf[n & 1][1], cs[1]); cs[2] = dot_ones(wf[n & 1][2], cs[2]); } }
      if (n == 8 && i == 0) {
        const unsigned dst = tg == 0 ? WS_CSP + (cg * 16 + rb) * 1280 + (ch * 32 + l31) * 16 : OOB; const u64 mk = tg == 0 ? 0xffffffffull : M_ONE;
        st_b128_sc1_m((u32x4){__float_as_uint(swap_add(cs[0])), __float_as_uint(swap_add(cs[1])), __float_as_uint(swap_add(cs[2])), __float_as_uint((qs0[0] + qs0[1]) + (qs0[2] + qs0[3]))}, rws, dst, mk);
        st_b32_sc1_m(__float_as_uint((qs1[0] + qs1[1]) + (qs1[2] + qs1[3])), rws, tg == 0 ? WS_CSP + (cg * 16 + rb) * 1280 + 1024 + (ch * 32 + l31) * 4 : OOB, mk);
      }
      if (j == 3 && s + 3 < 10) { if (i == 0) dmax(s + 3, 0); if (i == 2) dmax(s + 3, 1); }
      if (j < 3 && s >= 1 && s + 2 < 10) { if (i == 1) dmaw(s + 2, j); }
      if constexpr (n == 15) { if (i == 2) {
        const rsrc_t rr = mk_rsrc(wo, D * D * 2);
        dma16_m(hv ? L_W2S + w * 1024 : L_DUMMY, hv ? (unsigned)((l >> 3) * 1280 + (l & 7) * 16) : OOB, rr, hv ? (32 * (hkst - 48) + 8 * w) * 1280 + hcb * 256 + (b & 1) * 128 : 0, hv ? M_ALL : M_ONE);
      } }
      if constexpr (n == 23) { if (i == 2) {
        const int ct = 8 * hcb + 4 * (b & 1) + (w & 3);
        const s8 f = tr_frag(L_W2S + (8 * gi + q4) * 128 + (w & 3) * 32 + p4 * 8, 512);
        st_b128_sc1_m(__builtin_bit_cast(u32x4, f), rws, hv ? WS_W2P + (((ct / 5) * 68 + hkst) * 5 + ct % 5) * 1024 + l * 16 : OOB, hv ? M_ALL : M_ONE);
      } }
      SB();
    }
  });
  STAMP(2);
  STAMPL(20);
  WAITV(4);
#pragma unroll
  for (int r = 0; r < 4; ++r) { TIE(cg4[r]); TIE(cg1[r]); }
  if (w < 4) { const unsigned ad = L_CS + ((h * 5) * 32 + l31) * 16 + w * 4;
#pragma unroll
    for (int e = 0; e < 4; ++e) *(LAS float*)(size_t)(ad + e * 512) = (__uint_as_float(cg4[0][e]) + __uint_as_float(cg4[1][e])) + (__uint_as_float(cg4[2][e]) + __uint_as_float(cg4[3][e]));
    *(LAS float*)(size_t)(ad + 4 * 512) = (__uint_as_float(cg1[0]) + __uint_as_float(cg1[1])) + (__uint_as_float(cg1[2]) + __uint_as_float(cg1[3])); }
  WAITVL_BAR(0);
  TIE(st);
  const float mean = __uint_as_float(st[0]), rstd = __uint_as_float(st[1]);
  s8 bm;
  { const float nm = -mean; const unsigned hi = pk2(nm, 0.f) & 0xffffu; const float lo = nm - __uint_as_float(hi << 16); unsigned mpk = hi | (pk2(lo, 0.f) << 16); if (h) mpk = 0; bm = __builtin_bit_cast(s8, (u32x4){mpk, mpk, 0u, 0u}); }
  auto cs_frag = [&](int tile) -> s8 {
    const f32x4 pp = *(LAS f32x4*)(size_t)(L_CS + ((ch * 5 + tile) * 32 + l31) * 16);
    const float c = (pp[0] + pp[1]) + (pp[2] + pp[3]);
    unsigned hi = pk2(c, c); const float lo = c - __uint_as_float(hi << 16); unsigned lo2 = pk2(lo, lo);
    if (h) { hi = 0; lo2 = 0; }
    return __builtin_bit_cast(s8, (u32x4){hi, lo2, 0u, 0u});
  };
  const unsigned tok = 128 * rb + xrow;
  const unsigned actbase = WS_ACT + (tok >> 4) * 68 * 1024 + (l & 15) * 16;
  {
    const float gs = -1.4426950408889634f * rstd, irs = __builtin_amdgcn_rcpf(rstd);
#pragma unroll
    for (int i = 0; i < 3; ++i) acc[i] = MFMA32(cs_frag(i), bm, acc[i]);
#pragma unroll
    for (int i = 0; i < 3; ++i) {
      issue2(4 + i);
      const f32x2 gs2 = {gs, gs}, irs2 = {irs, irs};
      u32x4 ho;
#pragma unroll
      for (int r = 0; r < 4; ++r) {
        const f32x2 g2 = {acc[i][2 * r], acc[i][2 * r + 1]}, u2 = {acc[i][8 + 2 * r], acc[i][9 + 2 * r]};
        const f32x2 dn = __builtin_elementwise_fma(exp2_2(g2 * gs2), irs2, irs2);
        const f32x2 rc = {__builtin_amdgcn_rcpf(dn[0]), __builtin_amdgcn_rcpf(dn[1])};
        ho[r] = pk2v(u2 * rc);
      }
      const unsigned G = 12 * cg + 6 * ch + 2 * i + h;
      st_b128_sc1(ho, rws, actbase + (G >> 2) * 1024 + (G & 3) * 256);
      SB();
    }
  }
  STAMP(3);
  STAMPL(21);
  const int kq = w & 3, rh = w >> 2;
  const unsigned va = WS_ACT + (4 * r2 + 2 * rh) * 68 * 1024 + kq * 1024 + l * 16;
  u32x4 af[10][2];
  auto lda = [&](int t) { af[t % 10][0] = ld_b128_sc1(rws, va, t * 4096); af[t % 10][1] = ld_b128_sc1(rws, va + 68 * 1024, t * 4096); };
  f32x16 T[2];
#pragma unroll
  for (int i = 0; i < 2; ++i)
#pragma unroll
    for (int r = 0; r < 16; ++r) T[i][r] = 0.f;
  s8 qf[3][2];
  const unsigned ql = ch * 8192 + l * 16, ql2 = ql + L_W;
  auto rdq = [&](int m, int t) { const int q = m >> 2, j = m & 3; const unsigned sl = slot2(q); qf[m % 3][t] = lds_b128((sl >= L_W ? ql2 + (sl - L_W) : ql + sl) + j * 2048 + t * 1024); };
  rdq(0, 0); rdq(0, 1); rdq(1, 0); rdq(1, 1);
  static_for<0, 40>([&](auto M_) {
    constexpr int m = decltype(M_)::v;
    if constexpr (m == 6) STAMPL(22);
    if constexpr (m == 14) STAMPL(23);
    if constexpr (m == 15) STAMPL(24);
    if constexpr (m == 26) STAMPL(25);
    if constexpr (m == 34) STAMPL(26);
    if constexpr (m == 14) { WAITV_BAR(0); st_b64_sc1_m(tag, rws, w == 0 ? WS_FH + (rb * 16 + cg) * 8 : OOB, M_ONE); issue2(7); issue2(8); issue2(9); }
    if constexpr (m == 26) {
      WAITV_BAR(0);
      pl0 = ld_b128_sc1_m(rws, WS_FP1 + l * 32, 0, w == 0 ? M_ALL : M_ONE); pl1 = ld_b128_sc1_m(rws, WS_FP1 + l * 32 + 16, 0, w == 0 ? M_ALL : M_ONE);
      pf = POLL16(WS_FH + (r2 >> 1) * 128 + l * 8);
    }
    if constexpr (m == 30) {
      WAITV(0); TIE(pl0); TIE(pl1); TIE(pf);
      if (w == 0) {
        if (__builtin_expect(!flags256_ok(pl0, pl1, tag), 0)) wait_flags256(rws, WS_FP1, tag, l);
        if (__builtin_expect(!flags_ok(pf, tag, l < 16), 0)) wait_flags(rws, WS_FH + (r2 >> 1) * 128, 8, 16, tag, l);
      }
      asm volatile("s_barrier" ::: "memory");
    }
    if constexpr (m >= 30 && (m & 1) == 0) lda((m - 30) >> 1);
    SB();
#pragma unroll
    for (int t = 0; t < 2; ++t) {
      T[t] = MFMA32(qf[m % 3][t], XF[m], T[t]);
      SB();
      if (m + 2 < 40) rdq(m + 2, t);
      SB();
    }
  });
  STAMP(4);
  STAMPL(27);
  LBAR();
  STAMPL(28);
  const unsigned vb = WS_W2P + nb * 68 * 5 * 1024 + l * 16;
  const unsigned bf0 = w < 4 ? 3 * w : 12 + 2 * (w - 4);
  auto issueB1 = [&](int t, int k) {
    const unsigned base = L_BRING + (t % 6) * B_STAGE;
    if (k < 2) dma16_sc1(base + (bf0 + k) * 1024, vb, rws, t * B_STAGE + (bf0 + k) * 1024);
    else asm volatile("s_mov_b32 m0, %0\n\ts_mov_b64 exec, %4\n\tbuffer_load_dwordx4 %1, %2, %3 offen sc1 lds\n\ts_mov_b64 exec, -1" ::"s"(w < 4 ? base + (bf0 + 2) * 1024 : L_DUMMY), "v"(w < 4 ? vb : OOB), "s"(rws), "s"(t * B_STAGE + (bf0 + 2) * 1024), "s"(w < 4 ? M_ALL : M_ONE) : "memory");
  };
  auto issueB = [&](int t) { issueB1(t, 0); issueB1(t, 1); issueB1(t, 2); };
  f32x16 P;
  const float psc = 1.4426950408889634f * rstd;
  {
    issueB(0);
    T[0] = MFMA32(cs_frag(3), bm, T[0]);
    T[1] = MFMA32(cs_frag(4), bm, T[1]);
    const unsigned wpl = L_WPF + ch * 4096 + l * 16;
    const s8 A0 = lds_b128(wpl), A1 = lds_b128(wpl + 1024), A2 = lds_b128(wpl + 2048), A3 = lds_b128(wpl + 3072);
    s8 E0, E1;
    { const int k0 = l31 - 8 * h; const unsigned val = (k0 & 1) ? 0x3f800000u : 0x3f80u; u32x4 u0, u1;
#pragma unroll
      for (int d2 = 0; d2 < 4; ++d2) { u0[d2] = (k0 >> 1) == d2 ? val : 0u; u1[d2] = ((k0 - 16) >> 1) == d2 ? val : 0u; }
      E0 = __builtin_bit_cast(s8, u0); E1 = __builtin_bit_cast(s8, u1); }
    SB(); issueB(1); SB();
    const s8 t01 = pack_step(T[0], 1), t10u = pack_step(T[1], 0), t00 = pack_step(T[0], 0);
    f32x16 z16;
#pragma unroll
    for (int r = 0; r < 16; ++r) z16[r] = 0.f;
    f32x16 Pk = MFMA32(A2, t01, z16); Pk = MFMA32(A3, t10u, Pk);
#pragma unroll
    for (int r = 0; r < 8; ++r) { const f32x2 v = (f32x2){T[1][2 * r], T[1][2 * r + 1]} * (f32x2){rstd, rstd}; T[1][2 * r] = v[0]; T[1][2 * r + 1] = v[1]; }
    const s8 t10 = pack_step(T[1], 0);
    s8 t11 = pack_step(T[1], 1);
    { u32x4 u = __builtin_bit_cast(u32x4, t11); if (h) u[2] = (u[2] & 0xffff0000u) | 0x3f80u; t11 = __builtin_bit_cast(s8, u); }
    f32x16 V = MFMA32(t10, E0, z16); V = MFMA32(t11, E1, V);
    SB(); lda(5); SB();
    unsigned phk[10];
    { f32x2 ep[5], en[5]; phi_map1(Pk, psc, ep, en); SB(); issueB(2); SB(); phi_map2(ep, en, phk); }
    SB(); lda(6); SB();
    const s8 v0 = pack_step(V, 0), v1 = pack_step(V, 1);
    const s8 F0 = __builtin_bit_cast(s8, (u32x4){phk[0], phk[1], phk[2], phk[3]}), F1 = __builtin_bit_cast(s8, (u32x4){phk[4], phk[5], phk[6], phk[7]}), F2 = __builtin_bit_cast(s8, (u32x4){phk[8], phk[9], 0u, 0u});
    f32x16 Z0 = MFMA32(F0, E0, z16); Z0 = MFMA32(F1, E1, Z0);
    f32x16 Z1 = MFMA32(F2, E0, z16);
    f32x16 KV0 = MFMA32(pack_step(Z0, 0), v0, z16); KV0 = MFMA32(pack_step(Z0, 1), v1, KV0);
    f32x16 KV1 = MFMA32(pack_step(Z1, 0), v0, z16);
    const int rho = 16 * (l31 >> 4) + 8 * ((l31 & 7) >> 2) + 4 * ((l31 >> 3) & 1) + (l31 & 3);
    const int d = rho == 28 ? 20 : rho - 8;
    KV1 = MFMA32(pack_step(Z1, 1), v1, KV1);
    P = MFMA32(A0, t00, z16); P = MFMA32(A1, t01, P);
    if (d >= 0 && d <= 20) {
      const unsigned kb = L_KVR + w * 3360 + d * 160;
#pragma unroll
      for (int g = 0; g < 4; ++g) *(LAS f32x4*)(size_t)(kb + (8 * g + 4 * h) * 4) = (f32x4){KV0[4 * g], KV0[4 * g + 1], KV0[4 * g + 2], KV0[4 * g + 3]};
      if (h == 0) { *(LAS f32x4*)(size_t)(kb + 128) = (f32x4){KV1[0], KV1[1], KV1[2], KV1[3]}; *(LAS f32x4*)(size_t)(kb + 144) = (f32x4){KV1[4], KV1[5], KV1[6], KV1[7]}; }
    }
  }
  STAMPL(29);
  LBAR();
  {
    const int c = tid & 255, hc = tid >> 8;
    u32x4 o = {0u, 0u, 0u, 0u};
    if (c < 105) {
      float a[8];
#pragma unroll
      for (int e = 0; e < 8; ++e) a[e] = 0.f;
#pragma unroll
      for (int g = 0; g < 4; ++g) {
        const unsigned ad = L_KVR + (hc * 4 + g) * 3360 + c * 32;
        const f32x4 u0 = *(LAS f32x4*)(size_t)ad, u1 = *(LAS f32x4*)(size_t)(ad + 16);
#pragma unroll
        for (int e = 0; e < 4; ++e) { a[e] += u0[e]; a[4 + e] += u1[e]; }
      }
      o = (u32x4){pk2(a[0], a[1]), pk2(a[2], a[3]), pk2(a[4], a[5]), pk2(a[6], a[7])};
    }
    st_b128_sc1_m(o, rws, c < 105 ? WS_KVP + ((cg * 2 + hc) * 16 + rb) * KVP_STRIDE + c * 16 : OOB, tg == 0 ? M_ALL : (tg == 1 ? 0x1ffffffffffull : M_ONE));
  }
  STAMPL(30);
  const rsrc_t rout = mk_rsrc(out, S * D * 2);
  unsigned phq[10];
  lda(7); issueB(3); SB();
  { f32x2 ep[5], en[5]; phi_map1(P, psc, ep, en); SB(); phi_map2(ep, en, phq); }
#pragma unroll
  for (int r = 0; r < 10; ++r) TIE(phq[r]);
  STAMPL(31);
  WAITV_BAR(5);
  st_b64_sc1_m(tag, rws, w == 0 ? WS_FKV + (cg * 16 + rb) * 8 : OOB, M_ONE);
  lda(8); issueB(4);
  STAMP(5);
  STAMPL(32);
  f32x4 a2[2][5];
#pragma unroll
  for (int a = 0; a < 2; ++a)
#pragma unroll
    for (int c = 0; c < 5; ++c) a2[a][c] = (f32x4){0.f, 0.f, 0.f, 0.f};
  u32x4 kp[8]; u32x2 xr[3]; unsigned go[3]; s8 bfr[2][5];
  const int kc = (64 * tg + l) >> 1; const bool kact = kc < 105;
  static_for<0, 17>([&](auto T_) {
    constexpr int t = decltype(T_)::v;
    if constexpr (t == 4) STAMPF(16);
    if constexpr (t == 7) STAMPF(17);
    if constexpr (t == 11) STAMPF(18);
    if constexpr (t == 12) STAMPF(19);
        constexpr int T_PKV = 2, T_KVL = 3, T_SUM = 5, T_O = 6, T_FO = 9, T_PO = 11, T_AO = 12;
    constexpr int allow_tab[17] = {19, 19, 16, 5, 22, 6, 17, 11, 11, 9, 10, 10, 3, 6, 4, 5, 3};
    STAMPL(33 + t);
    asm volatile("s_waitcnt vmcnt(%0) lgkmcnt(0)" ::"i"(allow_tab[t]) : "memory");
    if constexpr (t == T_KVL) { TIE(pf); if (w == 0) { if (__builtin_expect(!flags_ok(pf, tag, l < 16 && l != rb), 0)) wait_flags(rws, WS_FKV + cg * 128, 8, 16, tag, l, rb); } }
    if constexpr (t == T_AO) { TIE(pf); if (w == 0) { { const int own = (r2 >> 1) == rb ? cg : -1; if (__builtin_expect(!flags_ok(pf, tag, l < 16 && l != own), 0)) wait_flags(rws, WS_FO + (r2 >> 1) * 128, 8, 16, tag, l, own); } } }
    asm volatile("s_barrier" ::: "memory");
    if constexpr (t == T_PKV) pf = POLL16(WS_FKV + cg * 128 + l * 8);
    if constexpr (t == T_KVL) {
#pragma unroll
      for (int r = 0; r < 8; ++r) kp[r] = ld_b128_sc1_m(rws, kact ? WS_KVP + ((cg * 2 + ch) * 16 + 8 * (l & 1)) * KVP_STRIDE + kc * 16 : OOB, r * KVP_STRIDE, tg < 3 ? M_ALL : 0x3ffffull);
    }
    if constexpr (t == T_SUM) {
      float a[8];
#pragma unroll
      for (int r = 0; r < 8; ++r) { TIE(kp[r]);
#pragma unroll
        for (int e = 0; e < 4; ++e) { a[2 * e] = r == 0 ? bf_lo(kp[r][e]) : sadd(a[2 * e], bf_lo(kp[r][e])); a[2 * e + 1] = r == 0 ? bf_hi(kp[r][e]) : sadd(a[2 * e + 1], bf_hi(kp[r][e])); } }
      asm volatile("s_nop 1" : "+v"(a[0]), "+v"(a[1]), "+v"(a[2]), "+v"(a[3]), "+v"(a[4]), "+v"(a[5]), "+v"(a[6]), "+v"(a[7]));
#pragma unroll
      for (int e = 0; e < 8; ++e) a[e] = dpp_add(a[e], 0);
      if (kact && (l & 1) == 0) { const int d = kc / 5, cc = kc % 5; *(LAS u32x4*)(size_t)(L_KVS + ch * 2016 + d * 96 + cc * 16) = (u32x4){pk2(a[0], a[1]), pk2(a[2], a[3]), pk2(a[4], a[5]), pk2(a[6], a[7])}; }
    }
    if constexpr (t == T_O) {
      const int reg = (l31 & 3) + 4 * (l31 >> 3), hh = (l31 >> 2) & 1;
      int d;
      if (reg >= 12) d = 20; else if (ch == 0) d = reg < 8 ? 8 * hh + reg : (hh == 0 ? 8 + reg : 20); else d = reg < 8 ? (hh == 0 ? 4 + reg : 12 + reg) : (hh == 0 ? reg - 8 : 20);
      const unsigned ka = L_KVS + ch * 2016 + d * 96;
      const s8 A0 = lds_b128(ka + h * 16), A1 = lds_b128(ka + 32 + h * 16);
      const u32x2 a2l = *(LAS u32x2*)(size_t)(ka + 64 + h * 8);
      const s8 A2 = __builtin_bit_cast(s8, (u32x4){a2l[0], a2l[1], 0u, 0u});
      f32x16 O;
#pragma unroll
      for (int r = 0; r < 16; ++r) O[r] = 0.f;
      O = MFMA32(A0, __builtin_bit_cast(s8, (u32x4){phq[0], phq[1], phq[2], phq[3]}), O);
      O = MFMA32(A1, __builtin_bit_cast(s8, (u32x4){phq[4], phq[5], phq[6], phq[7]}), O);
      O = MFMA32(A2, __builtin_bit_cast(s8, (u32x4){phq[8], phq[9], 0u, 0u}), O);
      float rc = __builtin_amdgcn_rcpf(O[12] + 1e-6f);
      asm volatile("s_nop 1" : "+v"(rc));
      const unsigned G0 = 192 + 5 * cg + (ch == 0 ? h : 3 + h);
      st_b128_sc1((u32x4){pk2(smul(O[0], rc), smul(O[1], rc)), pk2(smul(O[2], rc), smul(O[3], rc)), pk2(smul(O[4], rc), smul(O[5], rc)), pk2(smul(O[6], rc), smul(O[7], rc))}, rws, actbase + (G0 >> 2) * 1024 + (G0 & 3) * 256);
      const unsigned G2 = 192 + 5 * cg + 2;
      st_b64_sc1_m((u32x2){pk2(smul(O[8], rc), smul(O[9], rc)), pk2(smul(O[10], rc), smul(O[11], rc))}, rws, actbase + (G2 >> 2) * 1024 + (G2 & 3) * 256 + ch * 8, 0xffffffffull);
    }
    if constexpr (t == T_FO) st_b64_sc1_m(tag, rws, w == 0 ? WS_FO + (rb * 16 + cg) * 8 : OOB, M_ONE);
    if constexpr (t == T_PO) pf = POLL16(WS_FO + (r2 >> 1) * 128 + l * 8);
    if constexpr (t == T_AO) { lda(12); lda(13); lda(14); lda(15); lda(16); }
    if constexpr (t == 0) { lda(9); issueB(5); }
    if constexpr (t == 1) lda(10);
    if constexpr (t == 2) lda(11);
    if constexpr (t == 14) {
#pragma unroll
      for (int z = 0; z < 3; ++z) { const int ix = kq + 4 * z; const bool v = ix < 10; const int a = ix >= 5 ? 1 : 0, c = ix - 5 * a;
        go[z] = v ? (unsigned)((64 * r2 + 32 * rh + 16 * a + (l & 15)) * 1280 + (80 * nb + 16 * c + 4 * (l >> 4)) * 2) : OOB; xr[z] = ld_b64_m(rx, go[z], v ? M_ALL : M_ONE); }
    }
    if constexpr (t == 12 && T_AO == 12) WAITV(8);
    TIE(af[t % 10][0]); TIE(af[t % 10][1]);
    if constexpr (t == 0) {
#pragma unroll
      for (int c = 0; c < 5; ++c) bfr[0][c] = lds_b128(L_BRING + kq * 5120 + l * 16 + c * 1024);
    }
    SB();
#pragma unroll
    for (int c = 0; c < 5; ++c) {
      a2[0][c] = MFMA16(bfr[t & 1][c], af[t % 10][0], a2[0][c]);
      a2[1][c] = MFMA16(bfr[t & 1][c], af[t % 10][1], a2[1][c]);
      if constexpr (t + 1 < 17) bfr[(t + 1) & 1][c] = lds_b128(L_BRING + ((t + 1) % 6) * B_STAGE + kq * 5120 + l * 16 + c * 1024);
      if constexpr (t >= 1 && t + 5 < 17) { if (c == 0) issueB1(t + 5, 0); if (c == 2) issueB1(t + 5, 1); if (c == 4) issueB1(t + 5, 2); }
      SB();
    }
  });
  STAMP(6);
  STAMPL(50);
  LBAR();
  static_for<0, 10>([&](auto I_) { constexpr int ix = decltype(I_)::v;
    if ((ix & 3) != kq) *(LAS f32x4*)(size_t)(L_RED + ((rh * 10 + ix) * 3 + ((kq - ix - 1) & 3)) * 1024 + l * 16) = a2[ix / 5][ix % 5]; });
  asm volatile("s_waitcnt vmcnt(0) lgkmcnt(0)\n\ts_barrier" ::: "memory");
  auto fin = [&](auto I_, int z) { constexpr int ix = decltype(I_)::v;
    const unsigned ad = L_RED + (rh * 10 + ix) * 3 * 1024 + l * 16;
    const f32x4 v = (a2[ix / 5][ix % 5] + *(LAS f32x4*)(size_t)ad) + (*(LAS f32x4*)(size_t)(ad + 1024) + *(LAS f32x4*)(size_t)(ad + 2048));
    st_b64((u32x2){pk2(v[0] + bf_lo(xr[z][0]), v[1] + bf_hi(xr[z][0])), pk2(v[2] + bf_lo(xr[z][1]), v[3] + bf_hi(xr[z][1]))}, rout, go[z]); };
#pragma unroll
  for (int z = 0; z < 3; ++z) TIE(xr[z]);
  if (kq == 0) { fin(IC<0>{}, 0); fin(IC<4>{}, 1); fin(IC<8>{}, 2); }
  else if (kq == 1) { fin(IC<1>{}, 0); fin(IC<5>{}, 1); fin(IC<9>{}, 2); }
  else if (kq == 2) { fin(IC<2>{}, 0); fin(IC<6>{}, 1); }
  else { fin(IC<3>{}, 0); fin(IC<7>{}, 1); }
  asm volatile(".Lfk_end:");
  if (b == 0 && tid == 0) st_b64_sc1(tag, rws, WS_EPOCH);
  STAMPL(51);
  STAMP(7);
}

extern "C" void kernel_launch(void* const* d_in, const int* in_sizes, int n_in, void* d_out, int out_size, void* d_ws, size_t ws_size, hipStream_t stream) {
  (void)in_sizes; (void)n_in; (void)out_size; (void)ws_size;
  hipFuncSetAttribute((const void*)fused_k, hipFuncAttributeMaxDynamicSharedMemorySize, LDS_BYTES);
  fused_k<<<256, 512, LDS_BYTES, stream>>>((const u16*)d_in[0], (const u16*)d_in[1], (const u16*)d_in[2], (const u16*)d_in[3], (const u16*)d_in[5], (const u16*)d_in[6], (const u16*)d_in[7], (u16*)d_out, (char*)d_ws);
}
```

```cpp
#include <hip/hip_runtime.h>
#include <hip/hip_bf16.h>
typedef unsigned short u16;
typedef unsigned long long u64;
typedef short s4 __attribute__((ext_vector_type(4)));
typedef short s8 __attribute__((ext_vector_type(8)));
typedef __bf16 b8 __attribute__((ext_vector_type(8)));
typedef __bf16 b2 __attribute__((ext_vector_type(2)));
typedef float f32x16 __attribute__((ext_vector_type(16)));
typedef float f32x4 __attribute__((ext_vector_type(4)));
typedef float f32x2 __attribute__((ext_vector_type(2)));
typedef unsigned u32x4 __attribute__((ext_vector_type(4)));
typedef unsigned u32x2 __attribute__((ext_vector_type(2)));
#define LAS __attribute__((address_space(3)))
#define DI __device__ __forceinline__
typedef __amdgpu_buffer_rsrc_t rsrc_t;

constexpr int S = 2048, D = 640, F = 1536;
constexpr unsigned WS_EPOCH = 0;
constexpr unsigned WS_FP0 = 1024, WS_FP1 = 3072, WS_FH = 5120, WS_FO = 7168, WS_FKV = 9216;
constexpr unsigned WS_STATS = 16384;
constexpr unsigned WS_KVP = 32768;
constexpr unsigned KVP_STRIDE = 1792;
constexpr unsigned WS_WQP = 1u << 20;
constexpr unsigned WS_W2P = 4u << 20;
constexpr unsigned WS_CSP = 7u << 20;
constexpr unsigned WS_ACT = 8u << 20;
constexpr unsigned WS_USED = 17u << 20;
constexpr unsigned OOB = 0x80000000u;
constexpr unsigned L_X = 0;
constexpr unsigned L_W = 49152;
constexpr unsigned W_STAGE = 24832, W_UOFF = 12320;
constexpr unsigned L_MISC = L_W + 4 * W_STAGE;
constexpr unsigned L_W2S = L_MISC;
constexpr unsigned L_CS = L_W2S;
constexpr unsigned L_WPF = L_W2S + 7168;
constexpr unsigned L_QRAW = L_W + 3 * W_STAGE;
constexpr unsigned L_WPR = L_QRAW + 12288;
constexpr unsigned L_W2A = L_QRAW + 14336;
constexpr unsigned L_BRING = 0;
constexpr unsigned B_STAGE = 20480;
constexpr unsigned L_KVR = 122880;
constexpr unsigned L_KVS = 149760;
constexpr unsigned L_DUMMY1 = L_QRAW + 23552;
constexpr unsigned L_DUMMY = L_W2S + 6144;
constexpr unsigned L_RED = 0;
constexpr unsigned LDS_BYTES = 160 * 1024;

DI rsrc_t mk_rsrc(const void* p, unsigned bytes) { return __builtin_amdgcn_make_buffer_rsrc((void*)p, 0, bytes, 0x00020000); }
DI void dma16(unsigned lds_addr, unsigned voff, rsrc_t r, unsigned soff) {
  asm volatile("s_mov_b32 m0, %0\n\ts_nop 0\n\tbuffer_load_dwordx4 %1, %2, %3 offen lds" ::"s"(lds_addr), "v"(voff), "s"(r), "s"(soff) : "memory");
}
DI void dma16_sc1(unsigned lds_addr, unsigned voff, rsrc_t r, unsigned soff) {
  asm volatile("s_mov_b32 m0, %0\n\ts_nop 0\n\tbuffer_load_dwordx4 %1, %2, %3 offen sc1 lds" ::"s"(lds_addr), "v"(voff), "s"(r), "s"(soff) : "memory");
}
DI u32x2 ld_b64_sc1(rsrc_t r, unsigned voff, unsigned soff) { u32x2 v; asm volatile("buffer_load_dwordx2 %0, %1, %2, %3 offen sc1" : "=v"(v) : "v"(voff), "s"(r), "s"(soff) : "memory"); return v; }
DI u32x4 ld_b128_sc1(rsrc_t r, unsigned voff, unsigned soff) { u32x4 v; asm volatile("buffer_load_dwordx4 %0, %1, %2, %3 offen sc1" : "=v"(v) : "v"(voff), "s"(r), "s"(soff) : "memory"); return v; }
DI u32x2 ld_b64_m(rsrc_t r, unsigned voff, u64 mask) { u32x2 v; asm volatile("s_mov_b64 exec, %3\n\tbuffer_load_dwordx2 %0, %1, %2, 0 offen\n\ts_mov_b64 exec, -1" : "=v"(v) : "v"(voff), "s"(r), "s"(mask) : "memory"); return v; }
DI void st_b64(u32x2 v, rsrc_t r, unsigned voff) { asm volatile("buffer_store_dwordx2 %0, %1, %2, 0 offen" ::"v"(v), "v"(voff), "s"(r) : "memory"); }
DI u32x4 ld_b128(rsrc_t r, unsigned voff, unsigned soff) { u32x4 v; asm volatile("buffer_load_dwordx4 %0, %1, %2, %3 offen" : "=v"(v) : "v"(voff), "s"(r), "s"(soff) : "memory"); return v; }
DI void st_b128_sc1(u32x4 v, rsrc_t r, unsigned voff) {
 asm volatile("buffer_store_dwordx4 %0, %1, %2, 0 offen sc1\n\ts_nop 1" ::"v"(v), "v"(voff), "s"(r) : "memory"); }
DI void st_b64_sc1(u32x2 v, rsrc_t r, unsigned voff) { asm volatile("buffer_store_dwordx2 %0, %1, %2, 0 offen sc1" ::"v"(v), "v"(voff), "s"(r) : "memory"); }
DI void st_b64_sc1_m(u32x2 v, rsrc_t r, unsigned voff, u64 mask) {
 asm volatile("s_mov_b64 exec, %3\n\tbuffer_store_dwordx2 %0, %1, %2, 0 offen sc1\n\ts_mov_b64 exec, -1" ::"v"(v), "v"(voff), "s"(r), "s"(mask) : "memory"); }
DI void st_b32_sc1_m(unsigned v, rsrc_t r, unsigned voff, u64 mask) { asm volatile("s_mov_b64 exec, %3\n\tbuffer_store_dword %0, %1, %2, 0 offen sc1\n\ts_mov_b64 exec, -1" ::"v"(v), "v"(voff), "s"(r), "s"(mask) : "memory"); }
DI u32x2 ld_b64_sc1_m(rsrc_t r, unsigned voff, u64 mask) { u32x2 v; asm volatile("s_mov_b64 exec, %3\n\tbuffer_load_dwordx2 %0, %1, %2, 0 offen sc1\n\ts_mov_b64 exec, -1" : "=v"(v) : "v"(voff), "s"(r), "s"(mask) : "memory"); return v; }
DI unsigned ld_b32_sc1_m(rsrc_t r, unsigned voff, unsigned soff, u64 mask) { unsigned v; asm volatile("s_mov_b64 exec, %4\n\tbuffer_load_dword %0, %1, %2, %3 offen sc1\n\ts_mov_b64 exec, -1" : "=v"(v) : "v"(voff), "s"(r), "s"(soff), "s"(mask) : "memory"); return v; }
DI u32x4 ld_b128_sc1_m(rsrc_t r, unsigned voff, unsigned soff, u64 mask) { u32x4 v; asm volatile("s_mov_b64 exec, %4\n\tbuffer_load_dwordx4 %0, %1, %2, %3 offen sc1\n\ts_mov_b64 exec, -1" : "=v"(v) : "v"(voff), "s"(r), "s"(soff), "s"(mask) : "memory"); return v; }
DI void st_b128_sc1_m(u32x4 v, rsrc_t r, unsigned voff, u64 mask) {
 asm volatile("s_mov_b64 exec, %3\n\tbuffer_store_dwordx4 %0, %1, %2, 0 offen sc1\n\ts_mov_b64 exec, -1\n\ts_nop 0" ::"v"(v), "v"(voff), "s"(r), "s"(mask) : "memory"); }
DI void dma16_m(unsigned lds_addr, unsigned voff, rsrc_t r, unsigned soff, u64 mask) {
  asm volatile("s_mov_b32 m0, %0\n\ts_mov_b64 exec, %4\n\tbuffer_load_dwordx4 %1, %2, %3 offen lds\n\ts_mov_b64 exec, -1" ::"s"(lds_addr), "v"(voff), "s"(r), "s"(soff), "s"(mask) : "memory");
}
#define POLL16(off) ld_b64_sc1_m(rws, w == 0 ? (off) : OOB, w == 0 ? 0xffffull : M_ONE)
#define TIE(x) asm volatile("" : "+v"(x))
#define WAITV(n) asm volatile("s_waitcnt vmcnt(" #n ")" ::: "memory")
#define WAITV_BAR(n) asm volatile("s_waitcnt vmcnt(" #n ")\n\ts_barrier" ::: "memory")
#define WAITVL_BAR(n) asm volatile("s_waitcnt vmcnt(" #n ") lgkmcnt(0)\n\ts_barrier" ::: "memory")
#define LBAR() asm volatile("s_waitcnt lgkmcnt(0)\n\ts_barrier" ::: "memory")
#define SB() __builtin_amdgcn_sched_barrier(0)
#define MFMA32(a, b, c) __builtin_amdgcn_mfma_f32_32x32x16_bf16(__builtin_bit_cast(b8, (a)), __builtin_bit_cast(b8, (b)), (c), 0, 0, 0)
#define MFMA16(a, b, c) __builtin_amdgcn_mfma_f32_16x16x32_bf16(__builtin_bit_cast(b8, (a)), __builtin_bit_cast(b8, (b)), (c), 0, 0, 0)
DI s8 lds_b128(unsigned addr) { return *(LAS s8*)(size_t)addr; }
DI s8 tr_frag(unsigned addr, unsigned off2) {
  s4 a = __builtin_amdgcn_ds_read_tr16_b64_v4i16((LAS s4*)(size_t)addr);
  s4 b = __builtin_amdgcn_ds_read_tr16_b64_v4i16((LAS s4*)(size_t)(addr + off2));
  return __builtin_shufflevector(a, b, 0, 1, 2, 3, 4, 5, 6, 7);
}
DI float dot_ones(s8 f, float acc) {
  u32x4 u = __builtin_bit_cast(u32x4, f);
#pragma unroll
  for (int i = 0; i < 4; ++i) { const unsigned t = u[i]; acc = __builtin_amdgcn_fdot2_f32_bf16(__builtin_bit_cast(b2, t), __builtin_bit_cast(b2, 0x3f803f80u), acc, false); }
  asm volatile("" : "+v"(acc));
  return acc;
}
DI unsigned pk2(float lo, float hi) { b2 c; c[0] = (__bf16)lo; c[1] = (__bf16)hi; return __builtin_bit_cast(unsigned, c); }
DI float bf_lo(unsigned u) { return __uint_as_float(u << 16); }
DI float bf_hi(unsigned u) { return __uint_as_float(u & 0xffff0000u); }
DI float swap_add(float v) { unsigned u = __float_as_uint(v); auto r = __builtin_amdgcn_permlane32_swap(u, u, false, false); return __uint_as_float(r[0]) + __uint_as_float(r[1]); }
DI float sadd(float a, float b) { float r; asm("v_add_f32 %0, %1, %2" : "=v"(r) : "v"(a), "v"(b)); return r; }
DI float smul(float a, float b) { float r; asm("v_mul_f32 %0, %1, %2" : "=v"(r) : "v"(a), "v"(b)); return r; }
DI float dpp_add(float v, const int ctrl_sel) {
  const int u = __float_as_int(v); int r;
  if (ctrl_sel == 0) r = __builtin_amdgcn_update_dpp(0, u, 0xB1, 0xf, 0xf, true);
  else if (ctrl_sel == 1) r = __builtin_amdgcn_update_dpp(0, u, 0x4E, 0xf, 0xf, true);
  else if (ctrl_sel == 2) r = __builtin_amdgcn_update_dpp(0, u, 0x141, 0xf, 0xf, true);
  else r = __builtin_amdgcn_update_dpp(0, u, 0x140, 0xf, 0xf, true);
  return v + __int_as_float(r);
}
DI float wave_sum(float v) {
  v = dpp_add(v, 0); v = dpp_add(v, 1); v = dpp_add(v, 2); v = dpp_add(v, 3);
  { unsigned u = __float_as_uint(v); auto r = __builtin_amdgcn_permlane16_swap(u, u, false, false); v = __uint_as_float(r[0]) + __uint_as_float(r[1]); }
  return swap_add(v);
}
DI s8 pack_step(const f32x16& x, int s) {
  return __builtin_bit_cast(s8, (u32x4){pk2(x[8 * s], x[8 * s + 1]), pk2(x[8 * s + 2], x[8 * s + 3]), pk2(x[8 * s + 4], x[8 * s + 5]), pk2(x[8 * s + 6], x[8 * s + 7])});
}
template <int I> struct IC { static constexpr int v = I; };
template <int B, int E, class Fn> DI void static_for(Fn&& f) { if constexpr (B < E) { f(IC<B>{}); static_for<B + 1, E>(f); } }
DI bool flags_ok(u32x2 v, u32x2 tag, bool act) { return __all(!act || (v[0] == tag[0] && v[1] == tag[1])); }
DI void wait_flags(rsrc_t rws, unsigned base, unsigned stride, int n, u32x2 tag, int l, int skip = -1) {

  const bool act = l < n && l != skip; const unsigned off = act ? base + l * stride : OOB;
#pragma nounroll
  for (int it = 0; it < (1 << 17); ++it) {
    u32x2 v; asm volatile("buffer_load_dwordx2 %0, %1, %2, 0 offen sc1\n\ts_waitcnt vmcnt(0)" : "=&v"(v) : "v"(off), "s"(rws) : "memory");
    if (flags_ok(v, tag, act)) return;
    __builtin_amdgcn_s_sleep(1);
  }
}
DI void wait_flags256(rsrc_t rws, unsigned base, u32x2 tag, int l) {
#pragma nounroll
  for (int it = 0; it < (1 << 17); ++it) {
    u32x4 a, b;
    asm volatile("buffer_load_dwordx4 %0, %2, %3, 0 offen sc1\n\tbuffer_load_dwordx4 %1, %2, %3, 0 offen offset:16 sc1\n\ts_waitcnt vmcnt(0)" : "=&v"(a), "=&v"(b) : "v"(base + l * 32), "s"(rws) : "memory");
    const bool ok = a[0] == tag[0] && a[1] == tag[1] && a[2] == tag[0] && a[3] == tag[1] && b[0] == tag[0] && b[1] == tag[1] && b[2] == tag[0] && b[3] == tag[1];
    if (__all(ok)) return;
    __builtin_amdgcn_s_sleep(1);
  }
}
DI bool flags256_ok(u32x4 a, u32x4 b, u32x2 tag) {
  return __all(a[0] == tag[0] && a[1] == tag[1] && a[2] == tag[0] && a[3] == tag[1] && b[0] == tag[0] && b[1] == tag[1] && b[2] == tag[0] && b[3] == tag[1]);
}
DI float max3(float a, float b, float c) { float r; asm("v_max3_f32 %0, %1, %2, %3" : "=v"(r) : "v"(a), "v"(b), "v"(c)); return r; }
DI float min3(float a, float b, float c) { float r; asm("v_min3_f32 %0, %1, %2, %3" : "=v"(r) : "v"(a), "v"(b), "v"(c)); return r; }
DI f32x2 exp2_2(f32x2 a) { f32x2 r; r[0] = __builtin_amdgcn_exp2f(a[0]); r[1] = __builtin_amdgcn_exp2f(a[1]); return r; }
DI unsigned pk2v(f32x2 a) { return pk2(a[0], a[1]); }
DI void phi_map1(const f32x16& pin, float sc, f32x2 (&ep)[5], f32x2 (&en)[5]) {
  float p[10];
#pragma unroll
  for (int r = 0; r < 10; ++r) p[r] = pin[r];
  asm volatile("s_nop 7\n\ts_nop 3" : "+v"(p[0]), "+v"(p[1]), "+v"(p[2]), "+v"(p[3]), "+v"(p[4]), "+v"(p[5]), "+v"(p[6]), "+v"(p[7]), "+v"(p[8]), "+v"(p[9]));
  float mx = max3(p[0], p[1], p[2]), mn = min3(p[0], p[1], p[2]);
#pragma unroll
  for (int r = 3; r < 9; r += 2) { mx = max3(mx, p[r], p[r + 1]); mn = min3(mn, p[r], p[r + 1]); }
  mx = max3(mx, p[9], p[9]); mn = min3(mn, p[9], p[9]);
  { unsigned u = __float_as_uint(mx); auto r = __builtin_amdgcn_permlane32_swap(u, u, false, false); mx = max3(__uint_as_float(r[0]), __uint_as_float(r[1]), __uint_as_float(r[1])); }
  { unsigned u = __float_as_uint(mn); auto r = __builtin_amdgcn_permlane32_swap(u, u, false, false); mn = min3(__uint_as_float(r[0]), __uint_as_float(r[1]), __uint_as_float(r[1])); }
  const f32x2 s2 = {sc, sc}, bp = {-mx * sc, -mx * sc}, bn = {mn * sc, mn * sc};
#pragma unroll
  for (int r = 0; r < 5; ++r) { const f32x2 v = {p[2 * r], p[2 * r + 1]}; ep[r] = exp2_2(__builtin_elementwise_fma(v, s2, bp)); en[r] = exp2_2(__builtin_elementwise_fma(v, -s2, bn)); }
}
DI void phi_map2(const f32x2 (&ep)[5], const f32x2 (&en)[5], unsigned (&o)[10]) {
  const f32x2 sp2 = (ep[0] + ep[1]) + (ep[2] + ep[3]) + ep[4], sn2 = (en[0] + en[1]) + (en[2] + en[3]) + en[4];
  const float rp = __builtin_amdgcn_rcpf(swap_add(sp2[0] + sp2[1])), rn = __builtin_amdgcn_rcpf(swap_add(sn2[0] + sn2[1]));
  const f32x2 rp2 = {rp, rp}, rn2 = {rn, rn};
#pragma unroll
  for (int r = 0; r < 5; ++r) { o[r] = pk2v(ep[r] * rp2); o[5 + r] = pk2v(en[r] * rn2); }
}


__global__ __launch_bounds__(512, 2) void fused_k(const u16* __restrict__ x, const u16* __restrict__ wqkv, const u16* __restrict__ wphi, const u16* __restrict__ wo,
                                                   const u16* __restrict__ wg, const u16* __restrict__ wu, const u16* __restrict__ wd, u16* __restrict__ out, char* __restrict__ ws) {
  extern __shared__ __attribute__((aligned(256))) char smem[];
  const int tid = threadIdx.x; const int l = tid & 63; const int w = __builtin_amdgcn_readfirstlane(tid >> 6);
  const int tg = w & 3, ch = w >> 2; const int l31 = l & 31, h = l >> 5;
  const int b = blockIdx.x, xcd = b & 7, idx = b >> 3;
  const int rb = (xcd & 1) * 8 + (idx & 7), cg = (xcd >> 1) * 4 + (idx >> 3);
  const int r2 = (xcd & 3) * 8 + (idx & 7), nb = (xcd >> 2) * 4 + (idx >> 3);
  const int klo = (5 * rb) >> 1, kcnt = ((5 * (rb + 1)) >> 1) - klo, rot = klo >> 2, kloc = klo - 4 * rot;
  const rsrc_t rx = mk_rsrc(x, S * D * 2);
  const rsrc_t rw = mk_rsrc(w < 4 ? wg : wu, D * F * 2);
  const rsrc_t rws = mk_rsrc(ws, WS_USED);
  const u64 M_ALL = ~0ull, M_ONE = 1ull;
  auto sig = [&](int s) -> unsigned { const int t = s + rot; return t >= 10 ? t - 10 : t; };
  unsigned vx[2], vw[3];
#pragma unroll
  for (int i = 0; i < 2; ++i) { const int r = 8 * (2 * w + i) + (l >> 3); const int c = (l & 7) ^ ((r >> 1) & 7); vx[i] = (128 * rb + r) * 1280 + c * 16; }
#pragma unroll
  for (int i = 0; i < 3; ++i) { const int p = 64 * (3 * (w & 3) + i) + l; const int k = p / 12, c = p % 12; vw[i] = k * 3072 + cg * 192 + c * 16; }
  const unsigned lx0 = L_X + 2 * w * 1024;
  const unsigned lw0 = L_W + (w < 4 ? 0 : W_UOFF) + 3 * (w & 3) * 1024;
  auto dmax = [&](int s, int i) { dma16(lx0 + (s % 3) * 16384 + i * 1024, vx[i], rx, sig(s) * 128); };
  auto dmaw = [&](int s, int i) { dma16(lw0 + (s & 3) * W_STAGE + i * 1024, vw[i], rw, sig(s) * (64 * 3072)); };
  auto issue1 = [&](int s) { dmax(s, 0); dmax(s, 1); dmaw(s, 0); dmaw(s, 1); dmaw(s, 2); };
  issue1(0);
  u32x2 ep = ld_b64_sc1_m(rws, WS_EPOCH, M_ONE);
  const int srow = 128 * rb + 8 * cg + w;
  const bool v3 = kcnt == 3 && w < 4;
  u32x4 sx0, sx1;
  {
    const rsrc_t rq = mk_rsrc(wqkv, D * 1920 * 2);
    const int p = 64 * (w & 3) + l; const int k = p / 15, c = p % 15;
    const unsigned vo = p < 240 ? (unsigned)(k * 3840 + (c / 5) * 1280 + cg * 80 + (c % 5) * 16) : OOB;
    dma16(L_QRAW + w * 1024, vo, rq, (16 * (klo + (w >> 2))) * 3840);
    dma16_m(v3 ? L_QRAW + 8192 + w * 1024 : L_DUMMY1, v3 ? vo : OOB, rq, v3 ? (16 * (klo + 2)) * 3840 : 0, v3 ? M_ALL : M_ONE);
    const rsrc_t rp = mk_rsrc(wphi, 32 * 400 * 2);
    dma16_m(w < 2 ? L_WPR + w * 1024 : L_DUMMY1, (w < 2 && w * 64 + l < 100) ? (unsigned)(cg * 1600 + (w * 64 + l) * 16) : OOB, rp, 0, w == 0 ? M_ALL : (w == 1 ? 0xfffffffffull : M_ONE));
    sx0 = ld_b128(rx, srow * 1280 + l * 16, 0);
    sx1 = ld_b128(rx, l < 16 ? (unsigned)(srow * 1280 + 1024 + l * 16) : OOB, 0);
    const int kst = b / 5, cb = b % 5; const bool isd = kst < 48;
    const rsrc_t rr = mk_rsrc(isd ? wd : wo, isd ? F * D * 2 : D * D * 2);
    dma16(L_W2A + w * 1024, (unsigned)((l >> 4) * 1280 + (l & 15) * 16), rr, ((isd ? 32 * kst : 32 * (kst - 48)) + 4 * w) * 1280 + cb * 256);
  }
  const int gi = l >> 4, q4 = (l >> 2) & 3, p4 = l & 3;
  issue1(1); issue1(2);
  {
    u64 pc; unsigned len;
    asm volatile("s_getpc_b64 %0\n.Lfk_here:\n\ts_mov_b32 %1, .Lfk_end-.Lfk_here" : "=s"(pc), "=s"(len));
    const rsrc_t rc = mk_rsrc((const void*)pc, len);
    const unsigned off = (idx * 8 + w) * 256;
    asm volatile("s_mov_b32 m0, %0\n\ts_mov_b64 exec, 0xffff\n\tbuffer_load_dwordx4 %1, %2, 0 offen lds\n\ts_mov_b64 exec, -1" ::"s"(L_DUMMY1), "v"(off + l * 16), "s"(rc) : "memory");
  }
  WAITV_BAR(11);
  TIE(ep);
  u32x2 tag;
  { const unsigned lo = __builtin_amdgcn_readfirstlane(ep[0]), hi = __builtin_amdgcn_readfirstlane(ep[1]); tag[0] = lo + 1u; tag[1] = hi + (lo == 0xffffffffu ? 1u : 0u); }
  const int shc = (w >> 1) & 1, sht = w & 1;
  s8 sf0, sf1, sf2; unsigned se[8]; unsigned smA, smB;
  {
    const int Q = 4 * (gi & 1) + p4;
    int seg, qd;
    if (sht == 0) { seg = Q < 5 ? 0 : 1; qd = Q < 5 ? Q : Q - 5; } else { seg = Q < 2 ? 1 : 2; qd = Q < 2 ? 3 + Q : (Q < 7 ? Q - 2 : 4); }
    const unsigned lo = L_QRAW + (8 * h + q4) * 240 + seg * 80 + shc * 40 + qd * 8;
    sf0 = tr_frag(lo + (w >> 2) * 4096, 960); sf1 = tr_frag(lo + 8192, 960);
    sf2 = tr_frag(L_W2A + (8 * gi + q4) * 256 + w * 32 + p4 * 8, 1024);
    const int reg = (l31 & 3) + 4 * (l31 >> 3), hh = (l31 >> 2) & 1;
    const int d0 = (tg == 0 ? 0 : tg == 1 ? 16 : tg == 2 ? -4 : 12) + 4 * h;
    const unsigned wa = L_WPR + ch * 800 + (10 * hh + reg) * 2 + d0 * 40;
#pragma unroll
    for (int j = 0; j < 8; ++j) se[j] = *(LAS u16*)(size_t)(wa + (8 * (j >> 2) + (j & 3)) * 40);
    smA = (reg < 10 && (unsigned)d0 < 20u) ? ~0u : 0u; smB = (reg < 10 && (unsigned)(d0 + 8) < 20u) ? ~0u : 0u;
  }
  auto side = [&](int part) {
    if (part == 0) {
      TIE(sx0); TIE(sx1);
      float sm = 0.f, sq = 0.f;
#pragma unroll
      for (int i = 0; i < 4; ++i) { const unsigned u0 = sx0[i], u1 = sx1[i];
        sm = __builtin_amdgcn_fdot2_f32_bf16(__builtin_bit_cast(b2, u0), __builtin_bit_cast(b2, 0x3f803f80u), sm, false); sq = __builtin_amdgcn_fdot2_f32_bf16(__builtin_bit_cast(b2, u0), __builtin_bit_cast(b2, u0), sq, false);
        sm = __builtin_amdgcn_fdot2_f32_bf16(__builtin_bit_cast(b2, u1), __builtin_bit_cast(b2, 0x3f803f80u), sm, false); sq = __builtin_amdgcn_fdot2_f32_bf16(__builtin_bit_cast(b2, u1), __builtin_bit_cast(b2, u1), sq, false); }
      const float m = wave_sum(sm) * (1.f / D);
      const float var = fmaxf(wave_sum(sq) * (1.f / D) - m * m, 0.f);
      st_b64_sc1_m((u32x2){__float_as_uint(m), __float_as_uint(__builtin_amdgcn_rsqf(var + 1e-5f))}, rws, WS_STATS + srow * 8, M_ONE);
    } else if (part == 1) {
      st_b128_sc1(__builtin_bit_cast(u32x4, sf0), rws, WS_WQP + (((2 * cg + shc) * 40 + klo + (w >> 2)) * 2 + sht) * 1024 + l * 16);
      st_b128_sc1_m(__builtin_bit_cast(u32x4, sf1), rws, v3 ? WS_WQP + (((2 * cg + shc) * 40 + klo + 2) * 2 + sht) * 1024 + l * 16 : OOB, v3 ? M_ALL : M_ONE);
      const int kst = b / 5, cb = b % 5, ct = 8 * cb + w;
      st_b128_sc1(__builtin_bit_cast(u32x4, sf2), rws, WS_W2P + (((ct / 5) * 68 + kst) * 5 + ct % 5) * 1024 + l * 16);
      const float c0 = swap_add(dot_ones(sf0, 0.f)), c1 = swap_add(dot_ones(sf1, 0.f));
      if (h == 0) { const unsigned qa = L_W2S + ((shc * 2 + sht) * 32 + l31) * 16 + (w >> 2) * 4; *(LAS float*)(size_t)qa = c0; *(LAS float*)(size_t)(qa + 8) = v3 ? c1 : 0.f; }
    } else {
      *(LAS u32x4*)(size_t)(L_WPF + w * 1024 + l * 16) = (u32x4){(se[0] | (se[1] << 16)) & smA, (se[2] | (se[3] << 16)) & smA, (se[4] | (se[5] << 16)) & smB, (se[6] | (se[7] << 16)) & smB};
    }
  };
  const unsigned qoff = (p4 == 1) ? 8 : (p4 == 2) ? 4 : (p4 == 3) ? 12 : 0;
  const unsigned wl = L_W + ((gi & 1) ? W_UOFF : 0) + (8 * h + q4) * 192 + 2 * qoff + 96 * ch;
  const unsigned wl2 = wl + 2 * W_STAGE;
  const int xrow = 32 * tg + l31;
  unsigned xa[4];
#pragma unroll
  for (int j = 0; j < 4; ++j) { xa[j] = (L_X + xrow * 128 + ((h ^ ((xrow >> 1) & 7)) << 4)) ^ (j * 32); asm volatile("" : "+v"(xa[j])); }
  s8 XF[40];
  f32x16 acc[3]; float cs[3];
#pragma unroll
  for (int i = 0; i < 3; ++i) { cs[i] = 0.f;
#pragma unroll
    for (int r = 0; r < 16; ++r) acc[i][r] = 0.f; }
  s8 wf[2][3];
  auto rdx = [&](int s, int j) { XF[4 * s + j] = lds_b128(xa[j] + (s % 3) * 16384); };
  auto rdw = [&](int n, int i) { const int s = n >> 2, j = n & 3; wf[n & 1][i] = tr_frag(((s & 2) ? wl2 : wl) + (s & 1) * W_STAGE + j * 3072 + i * 32, 768); };
  const unsigned vq2 = WS_WQP + ((2 * cg + ch) * 40 + tg) * 2048 + l * 16;
  auto slot2 = [&](int q) -> unsigned { return q == 0 || q == 7 ? 16384u : q == 1 || q == 8 ? L_W + 2 * W_STAGE : q == 2 || q == 9 ? 32768u : q == 3 ? L_W + 3 * W_STAGE : q == 4 ? 0u : q == 5 ? L_W : L_W + W_STAGE; };
  auto issue2 = [&](int q) { const unsigned so = sig(q) * 8192; dma16_sc1(slot2(q) + w * 2048, vq2, rws, so); dma16_sc1(slot2(q) + w * 2048 + 1024, vq2, rws, so + 1024); };
  const bool hv = b < 168 && w < 4; const int hu = 256 + (b >> 1), hkst = hu / 5, hcb = hu % 5;
  u32x2 pf, pf2, st; u32x4 pl0, pl1, cg4[4]; unsigned cg1[4]; f32x4 qs0, qs1;
  rdx(0, 0); rdw(0, 0); rdw(0, 1); rdw(0, 2); rdx(0, 1); rdx(0, 2); rdx(0, 3);
  static_for<0, 40>([&](auto N_) {
    constexpr int n = decltype(N_)::v;
    constexpr int s = n >> 2, j = n & 3;
    if constexpr (n == 3) WAITV_BAR(10);
    if constexpr (n == 7) { WAITV_BAR(9); qs0 = *(LAS f32x4*)(size_t)(L_W2S + ((ch * 2) * 32 + l31) * 16); qs1 = *(LAS f32x4*)(size_t)(L_W2S + ((ch * 2 + 1) * 32 + l31) * 16); }
    if constexpr (n == 11) WAITV_BAR(7);
    if constexpr (n == 15) { WAITV_BAR(5); st_b64_sc1_m(tag, rws, w == 0 ? WS_FP0 + (cg * 16 + rb) * 8 : OOB, M_ONE); }
    if constexpr (n == 19) WAITV_BAR(7);
    if constexpr (n == 23) WAITV_BAR(5);
    if constexpr (n == 27) { WAITV_BAR(6); pf = POLL16(WS_FP0 + cg * 128 + l * 8); pf2 = POLL16(WS_FP0 + rb * 8 + (l & 15) * 128); }
    if constexpr (n == 31) {
      WAITV(5); TIE(pf); TIE(pf2);
      if (w == 0) {
        if (__builtin_expect(!flags_ok(pf, tag, l < 16), 0)) wait_flags(rws, WS_FP0 + cg * 128, 8, 16, tag, l);
        if (__builtin_expect(!flags_ok(pf2, tag, l < 16), 0)) wait_flags(rws, WS_FP0 + rb * 8, 128, 16, tag, l);
      }
      asm volatile("s_barrier" ::: "memory");
      st_b64_sc1_m(tag, rws, w == 0 ? WS_FP1 + b * 8 : OOB, M_ONE);
      st = ld_b64_sc1(rws, WS_STATS + (128 * rb + xrow) * 8, 0);
      {
        const unsigned vo = w < 4 ? WS_CSP + (cg * 16 + 4 * w) * 1280 + l * 16 : OOB, vo1 = w < 4 ? WS_CSP + (cg * 16 + 4 * w) * 1280 + 1024 + l * 4 : OOB;
#pragma unroll
        for (int r = 0; r < 4; ++r) { cg4[r] = ld_b128_sc1_m(rws, vo, r * 1280, w < 4 ? M_ALL : M_ONE); cg1[r] = ld_b32_sc1_m(rws, vo1, r * 1280, w < 4 ? M_ALL : M_ONE); } }
      issue2(0); issue2(1);
    }
    if constexpr (n == 35) { WAITV_BAR(14); issue2(2); issue2(3); }
    SB();
#pragma unroll
    for (int i = 0; i < 3; ++i) {
      acc[i] = MFMA32(wf[n & 1][i], XF[n], acc[i]);
      SB();
      if (i == 0 && j == 3 && s < 9) rdx(s + 1, 0);
      if (n + 1 < 40) rdw(n + 1, i);
      if (j == 0 && s > 0) rdx(s, i + 1);
      if (n < 3 && i == 2) side(n);
      if (n < 8 && i == 2) { if ((unsigned)(n - kloc) < (unsigned)kcnt && tg == 0) { cs[0] = dot_ones(wf[n & 1][0], cs[0]); cs[1] = dot_ones(wf[n & 1][1], cs[1]); cs[2] = dot_ones(wf[n & 1][2], cs[2]); } }
      if (n == 8 && i == 0) {
        const unsigned dst = tg == 0 ? WS_CSP + (cg * 16 + rb) * 1280 + (ch * 32 + l31) * 16 : OOB; const u64 mk = tg == 0 ? 0xffffffffull : M_ONE;
        st_b128_sc1_m((u32x4){__float_as_uint(swap_add(cs[0])), __float_as_uint(swap_add(cs[1])), __float_as_uint(swap_add(cs[2])), __float_as_uint((qs0[0] + qs0[1]) + (qs0[2] + qs0[3]))}, rws, dst, mk);
        st_b32_sc1_m(__float_as_uint((qs1[0] + qs1[1]) + (qs1[2] + qs1[3])), rws, tg == 0 ? WS_CSP + (cg * 16 + rb) * 1280 + 1024 + (ch * 32 + l31) * 4 : OOB, mk);
      }
      if (j == 3 && s + 3 < 10) { if (i == 0) dmax(s + 3, 0); if (i == 2) dmax(s + 3, 1); }
      if (j < 3 && s >= 1 && s + 2 < 10) { if (i == 1) dmaw(s + 2, j); }
      if constexpr (n == 15) { if (i == 2) {
        const rsrc_t rr = mk_rsrc(wo, D * D * 2);
        dma16_m(hv ? L_W2S + w * 1024 : L_DUMMY, hv ? (unsigned)((l >> 3) * 1280 + (l & 7) * 16) : OOB, rr, hv ? (32 * (hkst - 48) + 8 * w) * 1280 + hcb * 256 + (b & 1) * 128 : 0, hv ? M_ALL : M_ONE);
      } }
      if constexpr (n == 23) { if (i == 2) {
        const int ct = 8 * hcb + 4 * (b & 1) + (w & 3);
        const s8 f = tr_frag(L_W2S + (8 * gi + q4) * 128 + (w & 3) * 32 + p4 * 8, 512);
        st_b128_sc1_m(__builtin_bit_cast(u32x4, f), rws, hv ? WS_W2P + (((ct / 5) * 68 + hkst) * 5 + ct % 5) * 1024 + l * 16 : OOB, hv ? M_ALL : M_ONE);
      } }
      SB();
    }
  });
  WAITV(4);
#pragma unroll
  for (int r = 0; r < 4; ++r) { TIE(cg4[r]); TIE(cg1[r]); }
  if (w < 4) { const unsigned ad = L_CS + ((h * 5) * 32 + l31) * 16 + w * 4;
#pragma unroll
    for (int e = 0; e < 4; ++e) *(LAS float*)(size_t)(ad + e * 512) = (__uint_as_float(cg4[0][e]) + __uint_as_float(cg4[1][e])) + (__uint_as_float(cg4[2][e]) + __uint_as_float(cg4[3][e]));
    *(LAS float*)(size_t)(ad + 4 * 512) = (__uint_as_float(cg1[0]) + __uint_as_float(cg1[1])) + (__uint_as_float(cg1[2]) + __uint_as_float(cg1[3])); }
  WAITVL_BAR(0);
  TIE(st);
  const float mean = __uint_as_float(st[0]), rstd = __uint_as_float(st[1]);
  s8 bm;
  { const float nm = -mean; const unsigned hi = pk2(nm, 0.f) & 0xffffu; const float lo = nm - __uint_as_float(hi << 16); unsigned mpk = hi | (pk2(lo, 0.f) << 16); if (h) mpk = 0; bm = __builtin_bit_cast(s8, (u32x4){mpk, mpk, 0u, 0u}); }
  auto cs_frag = [&](int tile) -> s8 {
    const f32x4 pp = *(LAS f32x4*)(size_t)(L_CS + ((ch * 5 + tile) * 32 + l31) * 16);
    const float c = (pp[0] + pp[1]) + (pp[2] + pp[3]);
    unsigned hi = pk2(c, c); const float lo = c - __uint_as_float(hi << 16); unsigned lo2 = pk2(lo, lo);
    if (h) { hi = 0; lo2 = 0; }
    return __builtin_bit_cast(s8, (u32x4){hi, lo2, 0u, 0u});
  };
  const unsigned tok = 128 * rb + xrow;
  const unsigned actbase = WS_ACT + (tok >> 4) * 68 * 1024 + (l & 15) * 16;
  {
    const float gs = -1.4426950408889634f * rstd, irs = __builtin_amdgcn_rcpf(rstd);
#pragma unroll
    for (int i = 0; i < 3; ++i) acc[i] = MFMA32(cs_frag(i), bm, acc[i]);
#pragma unroll
    for (int i = 0; i < 3; ++i) {
      issue2(4 + i);
      const f32x2 gs2 = {gs, gs}, irs2 = {irs, irs};
      u32x4 ho;
#pragma unroll
      for (int r = 0; r < 4; ++r) {
        const f32x2 g2 = {acc[i][2 * r], acc[i][2 * r + 1]}, u2 = {acc[i][8 + 2 * r], acc[i][9 + 2 * r]};
        const f32x2 dn = __builtin_elementwise_fma(exp2_2(g2 * gs2), irs2, irs2);
        const f32x2 rc = {__builtin_amdgcn_rcpf(dn[0]), __builtin_amdgcn_rcpf(dn[1])};
        ho[r] = pk2v(u2 * rc);
      }
      const unsigned G = 12 * cg + 6 * ch + 2 * i + h;
      st_b128_sc1(ho, rws, actbase + (G >> 2) * 1024 + (G & 3) * 256);
      SB();
    }
  }
  const int kq = w & 3, rh = w >> 2;
  const unsigned va = WS_ACT + (4 * r2 + 2 * rh) * 68 * 1024 + kq * 1024 + l * 16;
  u32x4 af[10][2];
  auto lda = [&](int t) { af[t % 10][0] = ld_b128_sc1(rws, va, t * 4096); af[t % 10][1] = ld_b128_sc1(rws, va + 68 * 1024, t * 4096); };
  f32x16 T[2];
#pragma unroll
  for (int i = 0; i < 2; ++i)
#pragma unroll
    for (int r = 0; r < 16; ++r) T[i][r] = 0.f;
  s8 qf[3][2];
  const unsigned ql = ch * 8192 + l * 16, ql2 = ql + L_W;
  auto rdq = [&](int m, int t) { const int q = m >> 2, j = m & 3; const unsigned sl = slot2(q); qf[m % 3][t] = lds_b128((sl >= L_W ? ql2 + (sl - L_W) : ql + sl) + j * 2048 + t * 1024); };
  rdq(0, 0); rdq(0, 1); rdq(1, 0); rdq(1, 1);
  static_for<0, 40>([&](auto M_) {
    constexpr int m = decltype(M_)::v;
    if constexpr (m == 14) { WAITV_BAR(0); st_b64_sc1_m(tag, rws, w == 0 ? WS_FH + (rb * 16 + cg) * 8 : OOB, M_ONE); issue2(7); issue2(8); issue2(9); }
    if constexpr (m == 26) {
      WAITV_BAR(0);
      pl0 = ld_b128_sc1_m(rws, WS_FP1 + l * 32, 0, w == 0 ? M_ALL : M_ONE); pl1 = ld_b128_sc1_m(rws, WS_FP1 + l * 32 + 16, 0, w == 0 ? M_ALL : M_ONE);
      pf = POLL16(WS_FH + (r2 >> 1) * 128 + l * 8);
    }
    if constexpr (m == 30) {
      WAITV(0); TIE(pl0); TIE(pl1); TIE(pf);
      if (w == 0) {
        if (__builtin_expect(!flags256_ok(pl0, pl1, tag), 0)) wait_flags256(rws, WS_FP1, tag, l);
        if (__builtin_expect(!flags_ok(pf, tag, l < 16), 0)) wait_flags(rws, WS_FH + (r2 >> 1) * 128, 8, 16, tag, l);
      }
      asm volatile("s_barrier" ::: "memory");
    }
    if constexpr (m >= 30 && (m & 1) == 0) lda((m - 30) >> 1);
    SB();
#pragma unroll
    for (int t = 0; t < 2; ++t) {
      T[t] = MFMA32(qf[m % 3][t], XF[m], T[t]);
      SB();
      if (m + 2 < 40) rdq(m + 2, t);
      SB();
    }
  });
  LBAR();
  const unsigned vb = WS_W2P + nb * 68 * 5 * 1024 + l * 16;
  const unsigned bf0 = w < 4 ? 3 * w : 12 + 2 * (w - 4);
  auto issueB1 = [&](int t, int k) {
    const unsigned base = L_BRING + (t % 6) * B_STAGE;
    if (k < 2) dma16_sc1(base + (bf0 + k) * 1024, vb, rws, t * B_STAGE + (bf0 + k) * 1024);
    else asm volatile("s_mov_b32 m0, %0\n\ts_mov_b64 exec, %4\n\tbuffer_load_dwordx4 %1, %2, %3 offen sc1 lds\n\ts_mov_b64 exec, -1" ::"s"(w < 4 ? base + (bf0 + 2) * 1024 : L_DUMMY), "v"(w < 4 ? vb : OOB), "s"(rws), "s"(t * B_STAGE + (bf0 + 2) * 1024), "s"(w < 4 ? M_ALL : M_ONE) : "memory");
  };
  auto issueB = [&](int t) { issueB1(t, 0); issueB1(t, 1); issueB1(t, 2); };
  f32x16 P;
  const float psc = 1.4426950408889634f * rstd;
  {
    issueB(0);
    T[0] = MFMA32(cs_frag(3), bm, T[0]);
    T[1] = MFMA32(cs_frag(4), bm, T[1]);
    const unsigned wpl = L_WPF + ch * 4096 + l * 16;
    const s8 A0 = lds_b128(wpl), A1 = lds_b128(wpl + 1024), A2 = lds_b128(wpl + 2048), A3 = lds_b128(wpl + 3072);
    s8 E0, E1;
    { const int k0 = l31 - 8 * h; const unsigned val = (k0 & 1) ? 0x3f800000u : 0x3f80u; u32x4 u0, u1;
#pragma unroll
      for (int d2 = 0; d2 < 4; ++d2) { u0[d2] = (k0 >> 1) == d2 ? val : 0u; u1[d2] = ((k0 - 16) >> 1) == d2 ? val : 0u; }
      E0 = __builtin_bit_cast(s8, u0); E1 = __builtin_bit_cast(s8, u1); }
    SB(); issueB(1); SB();
    const s8 t01 = pack_step(T[0], 1), t10u = pack_step(T[1], 0), t00 = pack_step(T[0], 0);
    f32x16 z16;
#pragma unroll
    for (int r = 0; r < 16; ++r) z16[r] = 0.f;
    f32x16 Pk = MFMA32(A2, t01, z16); Pk = MFMA32(A3, t10u, Pk);
#pragma unroll
    for (int r = 0; r < 8; ++r) { const f32x2 v = (f32x2){T[1][2 * r], T[1][2 * r + 1]} * (f32x2){rstd, rstd}; T[1][2 * r] = v[0]; T[1][2 * r + 1] = v[1]; }
    const s8 t10 = pack_step(T[1], 0);
    s8 t11 = pack_step(T[1], 1);
    { u32x4 u = __builtin_bit_cast(u32x4, t11); if (h) u[2] = (u[2] & 0xffff0000u) | 0x3f80u; t11 = __builtin_bit_cast(s8, u); }
    f32x16 V = MFMA32(t10, E0, z16); V = MFMA32(t11, E1, V);
    SB(); lda(5); SB();
    unsigned phk[10];
    { f32x2 ep[5], en[5]; phi_map1(Pk, psc, ep, en); SB(); issueB(2); SB(); phi_map2(ep, en, phk); }
    SB(); lda(6); SB();
    const s8 v0 = pack_step(V, 0), v1 = pack_step(V, 1);
    const s8 F0 = __builtin_bit_cast(s8, (u32x4){phk[0], phk[1], phk[2], phk[3]}), F1 = __builtin_bit_cast(s8, (u32x4){phk[4], phk[5], phk[6], phk[7]}), F2 = __builtin_bit_cast(s8, (u32x4){phk[8], phk[9], 0u, 0u});
    f32x16 Z0 = MFMA32(F0, E0, z16); Z0 = MFMA32(F1, E1, Z0);
    f32x16 Z1 = MFMA32(F2, E0, z16);
    f32x16 KV0 = MFMA32(pack_step(Z0, 0), v0, z16); KV0 = MFMA32(pack_step(Z0, 1), v1, KV0);
    f32x16 KV1 = MFMA32(pack_step(Z1, 0), v0, z16);
    const int rho = 16 * (l31 >> 4) + 8 * ((l31 & 7) >> 2) + 4 * ((l31 >> 3) & 1) + (l31 & 3);
    const int d = rho == 28 ? 20 : rho - 8;
    KV1 = MFMA32(pack_step(Z1, 1), v1, KV1);
    P = MFMA32(A0, t00, z16); P = MFMA32(A1, t01, P);
    if (d >= 0 && d <= 20) {
      const unsigned kb = L_KVR + w * 3360 + d * 160;
#pragma unroll
      for (int g = 0; g < 4; ++g) *(LAS f32x4*)(size_t)(kb + (8 * g + 4 * h) * 4) = (f32x4){KV0[4 * g], KV0[4 * g + 1], KV0[4 * g + 2], KV0[4 * g + 3]};
      if (h == 0) { *(LAS f32x4*)(size_t)(kb + 128) = (f32x4){KV1[0], KV1[1], KV1[2], KV1[3]}; *(LAS f32x4*)(size_t)(kb + 144) = (f32x4){KV1[4], KV1[5], KV1[6], KV1[7]}; }
    }
  }
  LBAR();
  {
    const int c = tid & 255, hc = tid >> 8;
    u32x4 o = {0u, 0u, 0u, 0u};
    if (c < 105) {
      float a[8];
#pragma unroll
      for (int e = 0; e < 8; ++e) a[e] = 0.f;
#pragma unroll
      for (int g = 0; g < 4; ++g) {
        const unsigned ad = L_KVR + (hc * 4 + g) * 3360 + c * 32;
        const f32x4 u0 = *(LAS f32x4*)(size_t)ad, u1 = *(LAS f32x4*)(size_t)(ad + 16);
#pragma unroll
        for (int e = 0; e < 4; ++e) { a[e] += u0[e]; a[4 + e] += u1[e]; }
      }
      o = (u32x4){pk2(a[0], a[1]), pk2(a[2], a[3]), pk2(a[4], a[5]), pk2(a[6], a[7])};
    }
    st_b128_sc1_m(o, rws, c < 105 ? WS_KVP + ((cg * 2 + hc) * 16 + rb) * KVP_STRIDE + c * 16 : OOB, tg == 0 ? M_ALL : (tg == 1 ? 0x1ffffffffffull : M_ONE));
  }
  const rsrc_t rout = mk_rsrc(out, S * D * 2);
  unsigned phq[10];
  lda(7); issueB(3); SB();
  { f32x2 ep[5], en[5]; phi_map1(P, psc, ep, en); SB(); phi_map2(ep, en, phq); }
#pragma unroll
  for (int r = 0; r < 10; ++r) TIE(phq[r]);
  WAITV_BAR(5);
  st_b64_sc1_m(tag, rws, w == 0 ? WS_FKV + (cg * 16 + rb) * 8 : OOB, M_ONE);
  lda(8); issueB(4);
  f32x4 a2[2][5];
#pragma unroll
  for (int a = 0; a < 2; ++a)
#pragma unroll
    for (int c = 0; c < 5; ++c) a2[a][c] = (f32x4){0.f, 0.f, 0.f, 0.f};
  u32x4 kp[8]; u32x2 xr[3]; unsigned go[3]; s8 bfr[2][5];
  const int kc = (64 * tg + l) >> 1; const bool kact = kc < 105;
  static_for<0, 17>([&](auto T_) {
    constexpr int t = decltype(T_)::v;
        constexpr int T_PKV = 2, T_KVL = 3, T_SUM = 5, T_O = 6, T_FO = 9, T_PO = 11, T_AO = 12;
    constexpr int allow_tab[17] = {19, 19, 16, 5, 22, 6, 17, 11, 11, 9, 10, 10, 3, 6, 4, 5, 3};
    asm volatile("s_waitcnt vmcnt(%0) lgkmcnt(0)" ::"i"(allow_tab[t]) : "memory");
    if constexpr (t == T_KVL) { TIE(pf); if (w == 0) { if (__builtin_expect(!flags_ok(pf, tag, l < 16 && l != rb), 0)) wait_flags(rws, WS_FKV + cg * 128, 8, 16, tag, l, rb); } }
    if constexpr (t == T_AO) { TIE(pf); if (w == 0) { { const int own = (r2 >> 1) == rb ? cg : -1; if (__builtin_expect(!flags_ok(pf, tag, l < 16 && l != own), 0)) wait_flags(rws, WS_FO + (r2 >> 1) * 128, 8, 16, tag, l, own); } } }
    asm volatile("s_barrier" ::: "memory");
    if constexpr (t == T_PKV) pf = POLL16(WS_FKV + cg * 128 + l * 8);
    if constexpr (t == T_KVL) {
#pragma unroll
      for (int r = 0; r < 8; ++r) kp[r] = ld_b128_sc1_m(rws, kact ? WS_KVP + ((cg * 2 + ch) * 16 + 8 * (l & 1)) * KVP_STRIDE + kc * 16 : OOB, r * KVP_STRIDE, tg < 3 ? M_ALL : 0x3ffffull);
    }
    if constexpr (t == T_SUM) {
      float a[8];
#pragma unroll
      for (int r = 0; r < 8; ++r) { TIE(kp[r]);
#pragma unroll
        for (int e = 0; e < 4; ++e) { a[2 * e] = r == 0 ? bf_lo(kp[r][e]) : sadd(a[2 * e], bf_lo(kp[r][e])); a[2 * e + 1] = r == 0 ? bf_hi(kp[r][e]) : sadd(a[2 * e + 1], bf_hi(kp[r][e])); } }
      asm volatile("s_nop 1" : "+v"(a[0]), "+v"(a[1]), "+v"(a[2]), "+v"(a[3]), "+v"(a[4]), "+v"(a[5]), "+v"(a[6]), "+v"(a[7]));
#pragma unroll
      for (int e = 0; e < 8; ++e) a[e] = dpp_add(a[e], 0);
      if (kact && (l & 1) == 0) { const int d = kc / 5, cc = kc % 5; *(LAS u32x4*)(size_t)(L_KVS + ch * 2016 + d * 96 + cc * 16) = (u32x4){pk2(a[0], a[1]), pk2(a[2], a[3]), pk2(a[4], a[5]), pk2(a[6], a[7])}; }
    }
    if constexpr (t == T_O) {
      const int reg = (l31 & 3) + 4 * (l31 >> 3), hh = (l31 >> 2) & 1;
      int d;
      if (reg >= 12) d = 20; else if (ch == 0) d = reg < 8 ? 8 * hh + reg : (hh == 0 ? 8 + reg : 20); else d = reg < 8 ? (hh == 0 ? 4 + reg : 12 + reg) : (hh == 0 ? reg - 8 : 20);
      const unsigned ka = L_KVS + ch * 2016 + d * 96;
      const s8 A0 = lds_b128(ka + h * 16), A1 = lds_b128(ka + 32 + h * 16);
      const u32x2 a2l = *(LAS u32x2*)(size_t)(ka + 64 + h * 8);
      const s8 A2 = __builtin_bit_cast(s8, (u32x4){a2l[0], a2l[1], 0u, 0u});
      f32x16 O;
#pragma unroll
      for (int r = 0; r < 16; ++r) O[r] = 0.f;
      O = MFMA32(A0, __builtin_bit_cast(s8, (u32x4){phq[0], phq[1], phq[2], phq[3]}), O);
      O = MFMA32(A1, __builtin_bit_cast(s8, (u32x4){phq[4], phq[5], phq[6], phq[7]}), O);
      O = MFMA32(A2, __builtin_bit_cast(s8, (u32x4){phq[8], phq[9], 0u, 0u}), O);
      float rc = __builtin_amdgcn_rcpf(O[12] + 1e-6f);
      asm volatile("s_nop 1" : "+v"(rc));
      const unsigned G0 = 192 + 5 * cg + (ch == 0 ? h : 3 + h);
      st_b128_sc1((u32x4){pk2(smul(O[0], rc), smul(O[1], rc)), pk2(smul(O[2], rc), smul(O[3], rc)), pk2(smul(O[4], rc), smul(O[5], rc)), pk2(smul(O[6], rc), smul(O[7], rc))}, rws, actbase + (G0 >> 2) * 1024 + (G0 & 3) * 256);
      const unsigned G2 = 192 + 5 * cg + 2;
      st_b64_sc1_m((u32x2){pk2(smul(O[8], rc), smul(O[9], rc)), pk2(smul(O[10], rc), smul(O[11], rc))}, rws, actbase + (G2 >> 2) * 1024 + (G2 & 3) * 256 + ch * 8, 0xffffffffull);
    }
    if constexpr (t == T_FO) st_b64_sc1_m(tag, rws, w == 0 ? WS_FO + (rb * 16 + cg) * 8 : OOB, M_ONE);
    if constexpr (t == T_PO) pf = POLL16(WS_FO + (r2 >> 1) * 128 + l * 8);
    if constexpr (t == T_AO) { lda(12); lda(13); lda(14); lda(15); lda(16); }
    if constexpr (t == 0) { lda(9); issueB(5); }
    if constexpr (t == 1) lda(10);
    if constexpr (t == 2) lda(11);
    if constexpr (t == 14) {
#pragma unroll
      for (int z = 0; z < 3; ++z) { const int ix = kq + 4 * z; const bool v = ix < 10; const int a = ix >= 5 ? 1 : 0, c = ix - 5 * a;
        go[z] = v ? (unsigned)((64 * r2 + 32 * rh + 16 * a + (l & 15)) * 1280 + (80 * nb + 16 * c + 4 * (l >> 4)) * 2) : OOB; xr[z] = ld_b64_m(rx, go[z], v ? M_ALL : M_ONE); }
    }
    if constexpr (t == 12 && T_AO == 12) WAITV(8);
    TIE(af[t % 10][0]); TIE(af[t % 10][1]);
    if constexpr (t == 0) {
#pragma unroll
      for (int c = 0; c < 5; ++c) bfr[0][c] = lds_b128(L_BRING + kq * 5120 + l * 16 + c * 1024);
    }
    SB();
#pragma unroll
    for (int c = 0; c < 5; ++c) {
      a2[0][c] = MFMA16(bfr[t & 1][c], af[t % 10][0], a2[0][c]);
      a2[1][c] = MFMA16(bfr[t & 1][c], af[t % 10][1], a2[1][c]);
      if constexpr (t + 1 < 17) bfr[(t + 1) & 1][c] = lds_b128(L_BRING + ((t + 1) % 6) * B_STAGE + kq * 5120 + l * 16 + c * 1024);
      if constexpr (t >= 1 && t + 5 < 17) { if (c == 0) issueB1(t + 5, 0); if (c == 2) issueB1(t + 5, 1); if (c == 4) issueB1(t + 5, 2); }
      SB();
    }
  });
  LBAR();
  static_for<0, 10>([&](auto I_) { constexpr int ix = decltype(I_)::v;
    if ((ix & 3) != kq) *(LAS f32x4*)(size_t)(L_RED + ((rh * 10 + ix) * 3 + ((kq - ix - 1) & 3)) * 1024 + l * 16) = a2[ix / 5][ix % 5]; });
  asm volatile("s_waitcnt vmcnt(0) lgkmcnt(0)\n\ts_barrier" ::: "memory");
  auto fin = [&](auto I_, int z) { constexpr int ix = decltype(I_)::v;
    const unsigned ad = L_RED + (rh * 10 + ix) * 3 * 1024 + l * 16;
    const f32x4 v = (a2[ix / 5][ix % 5] + *(LAS f32x4*)(size_t)ad) + (*(LAS f32x4*)(size_t)(ad + 1024) + *(LAS f32x4*)(size_t)(ad + 2048));
    st_b64((u32x2){pk2(v[0] + bf_lo(xr[z][0]), v[1] + bf_hi(xr[z][0])), pk2(v[2] + bf_lo(xr[z][1]), v[3] + bf_hi(xr[z][1]))}, rout, go[z]); };
#pragma unroll
  for (int z = 0; z < 3; ++z) TIE(xr[z]);
  if (kq == 0) { fin(IC<0>{}, 0); fin(IC<4>{}, 1); fin(IC<8>{}, 2); }
  else if (kq == 1) { fin(IC<1>{}, 0); fin(IC<5>{}, 1); fin(IC<9>{}, 2); }
  else if (kq == 2) { fin(IC<2>{}, 0); fin(IC<6>{}, 1); }
  else { fin(IC<3>{}, 0); fin(IC<7>{}, 1); }
  asm volatile(".Lfk_end:");
  if (b == 0 && tid == 0) st_b64_sc1(tag, rws, WS_EPOCH);
}

extern "C" void kernel_launch(void* const* d_in, const int* in_sizes, int n_in, void* d_out, int out_size, void* d_ws, size_t ws_size, hipStream_t stream) {
  (void)in_sizes; (void)n_in; (void)out_size; (void)ws_size;
  (void)hipFuncSetAttribute((const void*)fused_k, hipFuncAttributeMaxDynamicSharedMemorySize, LDS_BYTES);
  fused_k<<<256, 512, LDS_BYTES, stream>>>((const u16*)d_in[0], (const u16*)d_in[1], (const u16*)d_in[2], (const u16*)d_in[3], (const u16*)d_in[5], (const u16*)d_in[6], (const u16*)d_in[7], (u16*)d_out, (char*)d_ws);
}
```
